```python
import torch

def _softmax(x, axis=-1):
    return torch.softmax(x, dim=axis)
def _gelu(x):
    return torch.nn.functional.gelu(x, approximate="tanh")
def _silu(x):
    return torch.nn.functional.silu(x)
def _sigmoid(x):
    return torch.sigmoid(x)
def _layernorm(x, axis=-1, eps=1e-5):
    m = x.mean(dim=axis, keepdim=True)
    v = x.var(dim=axis, keepdim=True, unbiased=False)
    return (x - m) / torch.sqrt(v + eps)
def _scatter_add(tgt, idx, src, axis=0):
    return tgt.clone().index_add_(axis, idx, src)
def _segment_sum(src, seg, n):
    out = torch.zeros((n,) + src.shape[1:], dtype=src.dtype)
    return out.index_add_(0, seg, src)
def _pad(x, pw):
    flat = tuple(v for pair in reversed(pw) for v in pair)
    return torch.nn.functional.pad(x, flat)

kernel_name = 'standalone__mha_softmax__near_ridge'

def setup_inputs():

    _w = (lambda a: (lambda t: t.to(torch.bfloat16) if t.is_floating_point() else t)(torch.as_tensor(a)))
    B, H, Sq, Sk, D = 16, 32, 384, 1024, 128
    return {
        "q": _w((lambda *s: torch.randn(*s, dtype=torch.bfloat16) * 0.8)(B, H, Sq, D)),
        "k": _w((lambda *s: torch.randn(*s, dtype=torch.bfloat16) * 0.8)(B, H, Sk, D)),
        "v": _w((lambda *s: torch.randn(*s, dtype=torch.bfloat16) * 0.05)(B, H, Sk, D)),
    }

def reference(q, k, v):
    scale = q.shape[-1] ** -0.5
    scores = torch.einsum("bhqd,bhkd->bhqk", q, k) * scale
    p = _softmax(scores, axis=-1)
    return torch.einsum("bhqk,bhkd->bhqd", p, v)
```

```cpp
#include <hip/hip_runtime.h>
#include <hip/hip_bf16.h>

typedef float f32x16 __attribute__((ext_vector_type(16)));
typedef short bf16x8 __attribute__((ext_vector_type(8)));
typedef unsigned u32x4 __attribute__((ext_vector_type(4)));
typedef __bf16 bf16x2_t __attribute__((ext_vector_type(2)));
typedef float f32x2 __attribute__((ext_vector_type(2)));

constexpr int NH = 512, SQ = 384, SK = 1024, D = 128;

__device__ __forceinline__ unsigned cvtpk(float lo, float hi) {
  f32x2 v = {lo, hi}; bf16x2_t b = __builtin_convertvector(v, bf16x2_t); return __builtin_bit_cast(unsigned, b);
}

__device__ __forceinline__ void safe_block(const unsigned short* __restrict__ Q, const unsigned short* __restrict__ K,
                                        const unsigned short* __restrict__ V, unsigned short* __restrict__ O, int lane) {
  const int r = lane & 31, h = lane >> 5;
  const float C = 0.088388347648318440f * 1.4426950408889634f;
  bf16x8 qf[8];
#pragma unroll
  for (int d0 = 0; d0 < 8; ++d0) qf[d0] = *(const bf16x8*)(Q + r * D + d0 * 16 + h * 8);
  f32x16 o[4];
#pragma unroll
  for (int i = 0; i < 4; ++i) o[i] = f32x16{};
  float m = -INFINITY, l = 0.f;
  for (int k0 = 0; k0 < SK; k0 += 32) {
    f32x16 s = f32x16{};
#pragma unroll
    for (int d0 = 0; d0 < 8; ++d0) {
      bf16x8 kf = *(const bf16x8*)(K + (k0 + r) * D + d0 * 16 + h * 8);
      s = __builtin_amdgcn_mfma_f32_32x32x16_bf16(kf, qf[d0], s, 0, 0, 0);
    }
    float mx = s[0];
#pragma unroll
    for (int i = 1; i < 16; ++i) mx = fmaxf(mx, s[i]);
    mx = fmaxf(mx, __shfl_xor(mx, 32));
    float mn = fmaxf(m, mx);
    float alpha = __builtin_amdgcn_exp2f((m - mn) * C);
    if (mn == -INFINITY) alpha = 1.f;
    m = mn;
    float ps = 0.f;
#pragma unroll
    for (int i = 0; i < 16; ++i) { s[i] = __builtin_amdgcn_exp2f((s[i] - mn) * C); ps += s[i]; }
    ps += __shfl_xor(ps, 32);
    l = l * alpha + ps;
#pragma unroll
    for (int i = 0; i < 4; ++i)
#pragma unroll
      for (int e = 0; e < 16; ++e) o[i][e] *= alpha;
#pragma unroll
    for (int ks = 0; ks < 2; ++ks) {
      u32x4 pw = {cvtpk(s[8 * ks + 0], s[8 * ks + 1]), cvtpk(s[8 * ks + 2], s[8 * ks + 3]),
                  cvtpk(s[8 * ks + 4], s[8 * ks + 5]), cvtpk(s[8 * ks + 6], s[8 * ks + 7])};
      bf16x8 pf = __builtin_bit_cast(bf16x8, pw);
#pragma unroll
      for (int db = 0; db < 4; ++db) {
        bf16x8 vf;
#pragma unroll
        for (int j = 0; j < 8; ++j) {
          int key = k0 + 16 * ks + 8 * (j >> 2) + 4 * h + (j & 3);
          vf[j] = (short)V[key * D + db * 32 + r];
        }
        o[db] = __builtin_amdgcn_mfma_f32_32x32x16_bf16(vf, pf, o[db], 0, 0, 0);
      }
    }
  }
  float il = 1.f / l;
#pragma unroll
  for (int db = 0; db < 4; ++db)
#pragma unroll
    for (int g = 0; g < 4; ++g) {
      uint2 w = {cvtpk(o[db][4 * g + 0] * il, o[db][4 * g + 1] * il), cvtpk(o[db][4 * g + 2] * il, o[db][4 * g + 3] * il)};
      *(uint2*)(O + r * D + db * 32 + 8 * g + 4 * h) = w;
    }
}

__global__ __launch_bounds__(256) void attn_base(const unsigned short* __restrict__ Q, const unsigned short* __restrict__ K,
                                                 const unsigned short* __restrict__ V, unsigned short* __restrict__ O) {
  const int wid = threadIdx.x >> 6, lane = threadIdx.x & 63;
  const int blk = blockIdx.x * 4 + wid;
  const int head = blk / 12, qb = blk % 12;
  safe_block(Q + ((long)head * SQ + qb * 32) * D, K + (long)head * SK * D, V + (long)head * SK * D, O + ((long)head * SQ + qb * 32) * D, lane);
}

extern "C" void kernel_launch(void* const* d_in, const int* in_sizes, int n_in, void* d_out, int out_size, void* d_ws, size_t ws_size, hipStream_t stream) {
  attn_base<<<NH * 12 / 4, 256, 0, stream>>>((const unsigned short*)d_in[0], (const unsigned short*)d_in[1], (const unsigned short*)d_in[2], (unsigned short*)d_out);
}
```

```cpp
#include <hip/hip_runtime.h>
#include <hip/hip_bf16.h>

typedef float f32x16 __attribute__((ext_vector_type(16)));
typedef short bf16x8 __attribute__((ext_vector_type(8)));
typedef unsigned u32x4 __attribute__((ext_vector_type(4)));
typedef __bf16 bf16x2_t __attribute__((ext_vector_type(2)));
typedef float f32x2 __attribute__((ext_vector_type(2)));

constexpr int SQ = 384, SK = 1024, D = 128;

__device__ __forceinline__ unsigned cvtpk(float lo, float hi) {
  f32x2 v = {lo, hi}; bf16x2_t b = __builtin_convertvector(v, bf16x2_t); return __builtin_bit_cast(unsigned, b);
}

__device__ __forceinline__ void safe_block(const unsigned short* __restrict__ Q, const unsigned short* __restrict__ K,
                                           const unsigned short* __restrict__ V, unsigned short* __restrict__ O, int lane) {
  const int r = lane & 31, h = lane >> 5;
  const float C = 0.088388347648318440f * 1.4426950408889634f;
  bf16x8 qf[8];
#pragma unroll
  for (int d0 = 0; d0 < 8; ++d0) qf[d0] = *(const bf16x8*)(Q + r * D + d0 * 16 + h * 8);
  f32x16 o[4];
#pragma unroll
  for (int i = 0; i < 4; ++i) o[i] = f32x16{};
  float m = -INFINITY, l = 0.f;
#pragma unroll 1
  for (int k0 = 0; k0 < SK; k0 += 32) {
    f32x16 s = f32x16{};
#pragma unroll
    for (int d0 = 0; d0 < 8; ++d0) {
      bf16x8 kf = *(const bf16x8*)(K + (k0 + r) * D + d0 * 16 + h * 8);
      s = __builtin_amdgcn_mfma_f32_32x32x16_bf16(kf, qf[d0], s, 0, 0, 0);
    }
    float mx = s[0];
#pragma unroll
    for (int i = 1; i < 16; ++i) mx = fmaxf(mx, s[i]);
    mx = fmaxf(mx, __shfl_xor(mx, 32));
    const float mn = fmaxf(m, mx);
    float alpha = __builtin_amdgcn_exp2f((m - mn) * C);
    if (m == mn) alpha = 1.f;
    m = mn;
    float ps = 0.f;
#pragma unroll
    for (int i = 0; i < 16; ++i) { s[i] = __builtin_amdgcn_exp2f((s[i] - mn) * C); ps += s[i]; }
    ps += __shfl_xor(ps, 32);
    l = l * alpha + ps;
#pragma unroll
    for (int i = 0; i < 4; ++i)
#pragma unroll
      for (int e = 0; e < 16; ++e) o[i][e] *= alpha;
#pragma unroll
    for (int ks = 0; ks < 2; ++ks) {
      u32x4 pw = {cvtpk(s[8 * ks + 0], s[8 * ks + 1]), cvtpk(s[8 * ks + 2], s[8 * ks + 3]),
                  cvtpk(s[8 * ks + 4], s[8 * ks + 5]), cvtpk(s[8 * ks + 6], s[8 * ks + 7])};
      bf16x8 pf = __builtin_bit_cast(bf16x8, pw);
#pragma unroll
      for (int db = 0; db < 4; ++db) {
        bf16x8 vf;
#pragma unroll
        for (int j = 0; j < 8; ++j) {
          const int key = k0 + 16 * ks + 8 * (j >> 2) + 4 * h + (j & 3);
          vf[j] = (short)V[key * D + db * 32 + r];
        }
        o[db] = __builtin_amdgcn_mfma_f32_32x32x16_bf16(vf, pf, o[db], 0, 0, 0);
      }
    }
  }
  const float il = 1.f / l;
#pragma unroll
  for (int db = 0; db < 4; ++db)
#pragma unroll
    for (int g = 0; g < 4; ++g) {
      uint2 w = {cvtpk(o[db][4 * g + 0] * il, o[db][4 * g + 1] * il), cvtpk(o[db][4 * g + 2] * il, o[db][4 * g + 3] * il)};
      *(uint2*)(O + r * D + db * 32 + 8 * g + 4 * h) = w;
    }
}

__global__ __launch_bounds__(256, 1) void attn_fwd(const unsigned short* __restrict__ Q, const unsigned short* __restrict__ K,
                                                   const unsigned short* __restrict__ V, unsigned short* __restrict__ O) {
  __shared__ __attribute__((aligned(1024))) char lds[163840];
  const int wid = __builtin_amdgcn_readfirstlane(threadIdx.x >> 6);
  const unsigned ldsb = (unsigned)(size_t)lds;
  const unsigned b = blockIdx.x, head0 = b;
  unsigned flag;
  asm volatile(
    "; ---- prologue: descriptors, lane constants, first loads\n"
    "s_load_dwordx2 s[40:41], %1, 8\n"
    "s_load_dwordx2 s[36:37], %1, 0\n"
    "s_load_dwordx2 s[44:45], %1, 16\n"
    "s_load_dwordx2 s[52:53], %1, 24\n"
    "v_mbcnt_lo_u32_b32 v107, -1, 0\n"
    "v_mbcnt_hi_u32_b32 v107, -1, v107\n"
    "v_bfe_u32 v104, v107, 2, 2\n"
    "v_lshlrev_b32 v104, 8, v104\n"
    "v_lshrrev_b32 v105, 4, v107\n"
    "v_lshl_add_u32 v104, v105, 6, v104\n"
    "v_and_b32 v105, 3, v107\n"
    "v_lshl_add_u32 v5, v105, 4, v104\n"
    "v_xor_b32 v6, 48, v5\n"
    "v_xor_b32 v7, 32, v5\n"
    "v_xor_b32 v255, 16, v5\n"
    "s_mov_b32 s38, 50331648\n"
    "s_mov_b32 s39, 0x20000\n"
    "s_mov_b32 s42, 134217728\n"
    "s_mov_b32 s43, 0x20000\n"
    "s_mov_b32 s46, 134217728\n"
    "s_mov_b32 s47, 0x20000\n"
    "s_mov_b32 s54, 50331648\n"
    "s_mov_b32 s55, 0x20000\n"
    "s_mov_b32 s51, 0x20000\n"
    "s_mov_b32 s56, %4\n"
    "s_mov_b32 s85, %5\n"
    "s_lshl_b32 s84, s56, 12\n"
    "s_add_u32 s62, s84, %5\n"
    "s_lshl_b32 s59, %2, 18\n"
    "s_add_u32 s59, s59, s84\n"
    "s_mul_i32 s82, %2, 98304\n"
    "s_mul_i32 s77, s56, 24576\n"
    "s_add_u32 s82, s82, s77\n"
    "s_add_u32 s60, s82, 24576\n"
    "s_getpc_b64 s[70:71]\n"
    "s_and_b32 s70, s70, 0xffffff80\n"
    "s_lshr_b32 s77, %3, 3\n"
    "s_lshl_b32 s77, s77, 2\n"
    "s_add_u32 s77, s77, %4\n"
    "s_and_b32 s77, s77, 127\n"
    "s_lshl_b32 s77, s77, 7\n"
    "s_add_u32 s70, s70, s77\n"
    "s_addc_u32 s71, s71, 0\n"
    "s_waitcnt lgkmcnt(0)\n"
    "s_load_dword s95, s[70:71], 0\n"
    "s_load_dword s95, s[70:71], 64\n"
    "s_load_dword s95, s[70:71], 16384\n"
    "s_load_dword s95, s[70:71], 16448\n"
    "s_load_dword s95, s[70:71], 26752\n"
    "s_load_dword s95, s[70:71], 26816\n"
    "s_and_b32 s41, s41, 0xffff\n"
    "s_and_b32 s37, s37, 0xffff\n"
    "s_and_b32 s45, s45, 0xffff\n"
    "s_and_b32 s53, s53, 0xffff\n"
    "s_getreg_b32 s77, hwreg(HW_REG_XCC_ID)\n"
    "s_bitcmp1_b32 s77, 0\n"
    "s_cbranch_scc1 Lnodelay_%=\n"
    "s_sleep 30\n"
    "Lnodelay_%=:\n"
    "s_add_u32 m0, s62, 0\n"
    "s_add_u32 s63, s82, 0\n"
    "buffer_load_dwordx4 v5, s[36:39], s63 offen nt lds\n"
    "buffer_load_dwordx4 v6, s[36:39], s63 offen offset:1024 nt lds\n"
    "buffer_load_dwordx4 v7, s[36:39], s63 offen offset:2048 nt lds\n"
    "buffer_load_dwordx4 v255, s[36:39], s63 offen offset:3072 nt lds\n"
    "s_add_u32 m0, s62, 98304\n"
    "s_add_u32 s63, s59, 0\n"
    "buffer_load_dwordx4 v5, s[40:43], s63 offen nt lds\n"
    "buffer_load_dwordx4 v6, s[40:43], s63 offen offset:1024 nt lds\n"
    "buffer_load_dwordx4 v7, s[40:43], s63 offen offset:2048 nt lds\n"
    "buffer_load_dwordx4 v255, s[40:43], s63 offen offset:3072 nt lds\n"
    "s_add_u32 m0, s62, 16384\n"
    "s_add_u32 s63, s82, 4096\n"
    "buffer_load_dwordx4 v5, s[36:39], s63 offen nt lds\n"
    "buffer_load_dwordx4 v6, s[36:39], s63 offen offset:1024 nt lds\n"
    "buffer_load_dwordx4 v7, s[36:39], s63 offen offset:2048 nt lds\n"
    "buffer_load_dwordx4 v255, s[36:39], s63 offen offset:3072 nt lds\n"
    "s_mov_b32 s58, 7\n"
    "s_mov_b32 s61, 49152\n"
    "s_mov_b32 s57, 0\n"
    "s_mov_b32 s91, 0\n"
    "s_mov_b32 s66, 0\n"
    "s_mov_b32 s81, 0\n"
    "s_mov_b32 s90, 54\n"
    "v_bfe_u32 v105, v107, 2, 2\n"
    "v_lshlrev_b32 v104, 10, v105\n"
    "v_and_b32 v106, 3, v107\n"
    "v_lshl_add_u32 v104, v106, 6, v104\n"
    "v_sub_u32 v105, 4, v105\n"
    "v_lshrrev_b32 v106, 4, v107\n"
    "v_xor_b32 v105, v105, v106\n"
    "v_and_b32 v105, 3, v105\n"
    "v_lshl_add_u32 v3, v105, 4, v104\n"
    "v_add_u32 v3, %5, v3\n"
    "v_lshlrev_b32 v104, 10, v106\n"
    "v_bfe_u32 v105, v107, 2, 2\n"
    "v_lshl_add_u32 v104, v105, 6, v104\n"
    "v_and_b32 v105, 1, v107\n"
    "v_lshl_add_u32 v104, v105, 3, v104\n"
    "v_sub_u32 v105, 4, v106\n"
    "v_bfe_u32 v106, v107, 1, 1\n"
    "v_xor_b32 v105, v105, v106\n"
    "v_and_b32 v105, 3, v105\n"
    "v_lshl_add_u32 v4, v105, 4, v104\n"
    "v_add_u32 v4, %5, v4\n"
    "v_bfe_u32 v104, v107, 2, 2\n"
    "v_lshlrev_b32 v104, 3, v104\n"
    "v_and_b32 v105, 3, v107\n"
    "v_lshl_add_u32 v104, v105, 6, v104\n"
    "v_lshrrev_b32 v106, 4, v107\n"
    "v_lshl_add_u32 v254, v106, 8, v104\n"
    "v_add_u32 v254, s62, v254\n"
    "v_lshrrev_b32 v104, 3, v107\n"
    "v_lshlrev_b32 v104, 7, v104\n"
    "v_lshl_add_u32 v104, v105, 5, v104\n"
    "v_bfe_u32 v105, v107, 2, 1\n"
    "v_lshl_add_u32 v252, v105, 4, v104\n"
    "s_movk_i32 s78, 2048\n"
    "s_mov_b32 s79, 0\n"
    "v_mov_b32 v104, 0x3f803f80\n"
    "v_and_b32 v105, 2, v107\n"
    "v_cmp_eq_u32 vcc, 0, v105\n"
    "s_nop 1\n"
    "v_cndmask_b32 v104, 0, v104, vcc\n"
    "v_and_b32 v105, 1, v107\n"
    "v_mov_b32 v106, 0x44444444\n"
    "v_mov_b32 v253, 0xeeeeeeee\n"
    "v_cmp_eq_u32 vcc, 0, v105\n"
    "s_nop 1\n"
    "v_cndmask_b32 v253, v253, v106, vcc\n"
    "v_accvgpr_write_b32 a240, v104\n"
    "v_accvgpr_write_b32 a241, v104\n"
    "v_accvgpr_write_b32 a242, v104\n"
    "v_accvgpr_write_b32 a243, v104\n"
    "s_mov_b32 s72, 0x3e0293ee\n"
    "s_mov_b32 s73, 0x3e0293ee\n"
    "s_mov_b32 s74, 0xffff0000\n"
    "s_mov_b32 s80, 0x5f800000\n"
    "s_mov_b32 s83, 0xc2100000\n"
    "; ---- first head: Q prescale, reference scores, tiles 0 and 1\n"
    "v_add_u32 v1, s84, v3\n"
    "v_add_u32 v2, 65536, v1\n"
    "v_add_u32 v0, 98304, v3\n"
    "v_mbcnt_lo_u32_b32 v244, -1, 0\n"
    "v_mbcnt_hi_u32_b32 v244, -1, v244\n"
    "v_and_b32 v244, 48, v244\n"
    "v_add_u32 v244, s85, v244\n"
    "v_add_u32 v244, 98304, v244\n"
    "v_accvgpr_write_b32 a244, 0\n"
    "v_accvgpr_write_b32 a245, 0\n"
    "v_accvgpr_write_b32 a248, 0\n"
    "v_accvgpr_write_b32 a249, 0\n"
    "v_accvgpr_write_b32 a252, 0\n"
    "v_accvgpr_write_b32 a253, 0\n"
    "v_mov_b32 v245, 0\n"
    "s_waitcnt vmcnt(8)\n"
    "s_waitcnt lgkmcnt(0)\n"
    "s_add_u32 m0, s62, 32768\n"
    "s_add_u32 s63, s82, 8192\n"
    "buffer_load_dwordx4 v5, s[36:39], s63 offen nt lds\n"
    "ds_read_b128 v[8:11], v1 offset:0\n"
    "ds_read_b128 v[12:15], v1 offset:256\n"
    "ds_read_b128 v[16:19], v1 offset:512\n"
    "ds_read_b128 v[20:23], v1 offset:768\n"
    "buffer_load_dwordx4 v6, s[36:39], s63 offen offset:1024 nt lds\n"
    "buffer_load_dwordx4 v7, s[36:39], s63 offen offset:2048 nt lds\n"
    "s_waitcnt lgkmcnt(3)\n"
    "v_lshlrev_b32 v236, 16, v8\n"
    "v_and_b32 v237, s74, v8\n"
    "v_pk_mul_f32 v[236:237], v[236:237], s[72:73]\n"
    "v_lshlrev_b32 v238, 16, v9\n"
    "v_and_b32 v239, s74, v9\n"
    "v_pk_mul_f32 v[238:239], v[238:239], s[72:73]\n"
    "v_lshlrev_b32 v240, 16, v10\n"
    "v_and_b32 v241, s74, v10\n"
    "v_pk_mul_f32 v[240:241], v[240:241], s[72:73]\n"
    "v_lshlrev_b32 v242, 16, v11\n"
    "v_and_b32 v243, s74, v11\n"
    "v_pk_mul_f32 v[242:243], v[242:243], s[72:73]\n"
    "v_cvt_pk_bf16_f32 v8, v236, v237\n"
    "v_cvt_pk_bf16_f32 v9, v238, v239\n"
    "v_cvt_pk_bf16_f32 v10, v240, v241\n"
    "buffer_load_dwordx4 v255, s[36:39], s63 offen offset:3072 nt lds\n"
    "v_cvt_pk_bf16_f32 v11, v242, v243\n"
    "s_waitcnt lgkmcnt(0)\n"
    "v_lshlrev_b32 v236, 16, v12\n"
    "v_and_b32 v237, s74, v12\n"
    "v_pk_mul_f32 v[236:237], v[236:237], s[72:73]\n"
    "v_lshlrev_b32 v238, 16, v13\n"
    "v_and_b32 v239, s74, v13\n"
    "v_pk_mul_f32 v[238:239], v[238:239], s[72:73]\n"
    "v_lshlrev_b32 v240, 16, v14\n"
    "v_and_b32 v241, s74, v14\n"
    "v_pk_mul_f32 v[240:241], v[240:241], s[72:73]\n"
    "v_lshlrev_b32 v242, 16, v15\n"
    "v_and_b32 v243, s74, v15\n"
    "v_pk_mul_f32 v[242:243], v[242:243], s[72:73]\n"
    "v_cvt_pk_bf16_f32 v12, v236, v237\n"
    "v_cvt_pk_bf16_f32 v13, v238, v239\n"
    "v_cvt_pk_bf16_f32 v14, v240, v241\n"
    "v_cvt_pk_bf16_f32 v15, v242, v243\n"
    "v_lshlrev_b32 v236, 16, v16\n"
    "v_and_b32 v237, s74, v16\n"
    "v_pk_mul_f32 v[236:237], v[236:237], s[72:73]\n"
    "v_lshlrev_b32 v238, 16, v17\n"
    "v_and_b32 v239, s74, v17\n"
    "v_pk_mul_f32 v[238:239], v[238:239], s[72:73]\n"
    "v_lshlrev_b32 v240, 16, v18\n"
    "v_and_b32 v241, s74, v18\n"
    "v_pk_mul_f32 v[240:241], v[240:241], s[72:73]\n"
    "v_lshlrev_b32 v242, 16, v19\n"
    "v_and_b32 v243, s74, v19\n"
    "v_pk_mul_f32 v[242:243], v[242:243], s[72:73]\n"
    "v_cvt_pk_bf16_f32 v16, v236, v237\n"
    "v_cvt_pk_bf16_f32 v17, v238, v239\n"
    "v_cvt_pk_bf16_f32 v18, v240, v241\n"
    "v_cvt_pk_bf16_f32 v19, v242, v243\n"
    "v_lshlrev_b32 v236, 16, v20\n"
    "v_and_b32 v237, s74, v20\n"
    "v_pk_mul_f32 v[236:237], v[236:237], s[72:73]\n"
    "v_lshlrev_b32 v238, 16, v21\n"
    "v_and_b32 v239, s74, v21\n"
    "v_pk_mul_f32 v[238:239], v[238:239], s[72:73]\n"
    "v_lshlrev_b32 v240, 16, v22\n"
    "v_and_b32 v241, s74, v22\n"
    "v_pk_mul_f32 v[240:241], v[240:241], s[72:73]\n"
    "v_lshlrev_b32 v242, 16, v23\n"
    "v_and_b32 v243, s74, v23\n"
    "v_pk_mul_f32 v[242:243], v[242:243], s[72:73]\n"
    "v_cvt_pk_bf16_f32 v20, v236, v237\n"
    "v_cvt_pk_bf16_f32 v21, v238, v239\n"
    "v_cvt_pk_bf16_f32 v22, v240, v241\n"
    "v_cvt_pk_bf16_f32 v23, v242, v243\n"
    "s_waitcnt vmcnt(8)\n"
    "s_barrier\n"
    "s_add_u32 m0, s62, 131072\n"
    "s_add_u32 s63, s59, 16384\n"
    "buffer_load_dwordx4 v5, s[40:43], s63 offen nt lds\n"
    "ds_read_b128 a[144:147], v244 offset:0\n"
    "ds_read_b128 a[148:151], v244 offset:256\n"
    "ds_read_b128 a[152:155], v244 offset:512\n"
    "ds_read_b128 a[156:159], v244 offset:768\n"
    "ds_read_b128 a[72:75], v0 offset:0\n"
    "ds_read_b128 a[76:79], v0 offset:256\n"
    "ds_read_b128 a[80:83], v0 offset:512\n"
    "ds_read_b128 a[84:87], v0 offset:768\n"
    "buffer_load_dwordx4 v6, s[40:43], s63 offen offset:1024 nt lds\n"
    "ds_read_b128 a[88:91], v0 offset:4096\n"
    "ds_read_b128 a[92:95], v0 offset:4352\n"
    "ds_read_b128 a[96:99], v0 offset:4608\n"
    "ds_read_b128 a[100:103], v0 offset:4864\n"
    "ds_read_b128 a[104:107], v0 offset:8192\n"
    "ds_read_b128 a[108:111], v0 offset:8448\n"
    "ds_read_b128 a[112:115], v0 offset:8704\n"
    "ds_read_b128 a[116:119], v0 offset:8960\n"
    "buffer_load_dwordx4 v7, s[40:43], s63 offen offset:2048 nt lds\n"
    "s_waitcnt lgkmcnt(15)\n"
    "v_mfma_f32_16x16x32_bf16 v[212:215], a[144:147], v[8:11], 0\n"
    "ds_read_b128 a[120:123], v0 offset:12288\n"
    "ds_read_b128 a[124:127], v0 offset:12544\n"
    "s_waitcnt lgkmcnt(14)\n"
    "v_mfma_f32_16x16x32_bf16 v[212:215], a[148:151], v[12:15], v[212:215]\n"
    "ds_read_b128 a[128:131], v0 offset:12800\n"
    "ds_read_b128 a[132:135], v0 offset:13056\n"
    "v_mfma_f32_16x16x32_bf16 v[212:215], a[152:155], v[16:19], v[212:215]\n"
    "v_mov_b32 v244, 0\n"
    "v_mfma_f32_16x16x32_bf16 v[212:215], a[156:159], v[20:23], v[212:215]\n"
    "buffer_load_dwordx4 v255, s[40:43], s63 offen offset:3072 nt lds\n"
    "s_waitcnt vmcnt(8)\n"
    "s_add_u32 m0, s62, 49152\n"
    "s_add_u32 s63, s82, 12288\n"
    "buffer_load_dwordx4 v5, s[36:39], s63 offen nt lds\n"
    "ds_read_b128 v[24:27], v1 offset:16384\n"
    "ds_read_b128 v[28:31], v1 offset:16640\n"
    "ds_read_b128 v[32:35], v1 offset:16896\n"
    "v_sub_f32 v212, s83, v212\n"
    "v_sub_f32 v213, s83, v213\n"
    "v_sub_f32 v214, s83, v214\n"
    "v_sub_f32 v215, s83, v215\n"
    "ds_read_b128 v[36:39], v1 offset:17152\n"
    "buffer_load_dwordx4 v6, s[36:39], s63 offen offset:1024 nt lds\n"
    "s_waitcnt lgkmcnt(4)\n"
    "v_mfma_f32_16x16x32_bf16 v[200:203], a[72:75], v[8:11], v[212:215]\n"
    "v_mfma_f32_16x16x32_bf16 v[200:203], a[76:79], v[12:15], v[200:203]\n"
    "v_mfma_f32_16x16x32_bf16 v[200:203], a[80:83], v[16:19], v[200:203]\n"
    "v_mfma_f32_16x16x32_bf16 v[200:203], a[84:87], v[20:23], v[200:203]\n"
    "buffer_load_dwordx4 v7, s[36:39], s63 offen offset:2048 nt lds\n"
    "v_mfma_f32_16x16x32_bf16 v[248:251], a[88:91], v[8:11], v[212:215]\n"
    "s_waitcnt lgkmcnt(2)\n"
    "v_lshlrev_b32 v236, 16, v24\n"
    "v_mfma_f32_16x16x32_bf16 v[248:251], a[92:95], v[12:15], v[248:251]\n"
    "v_and_b32 v237, s74, v24\n"
    "v_lshlrev_b32 v238, 16, v25\n"
    "v_mfma_f32_16x16x32_bf16 v[248:251], a[96:99], v[16:19], v[248:251]\n"
    "v_exp_f32 v200, v200\n"
    "v_mfma_f32_16x16x32_bf16 v[248:251], a[100:103], v[20:23], v[248:251]\n"
    "v_exp_f32 v201, v201\n"
    "v_exp_f32 v202, v202\n"
    "v_exp_f32 v203, v203\n"
    "v_pk_mul_f32 v[236:237], v[236:237], s[72:73]\n"
    "v_cvt_pk_bf16_f32 v104, v200, v201\n"
    "v_cvt_pk_bf16_f32 v105, v202, v203\n"
    "v_mfma_f32_16x16x32_bf16 v[200:203], a[104:107], v[8:11], v[212:215]\n"
    "buffer_load_dwordx4 v255, s[36:39], s63 offen offset:3072 nt lds\n"
    "v_exp_f32 v248, v248\n"
    "v_mfma_f32_16x16x32_bf16 v[200:203], a[108:111], v[12:15], v[200:203]\n"
    "v_exp_f32 v249, v249\n"
    "v_mfma_f32_16x16x32_bf16 v[200:203], a[112:115], v[16:19], v[200:203]\n"
    "v_exp_f32 v250, v250\n"
    "v_mfma_f32_16x16x32_bf16 v[200:203], a[116:119], v[20:23], v[200:203]\n"
    "v_exp_f32 v251, v251\n"
    "v_cvt_pk_bf16_f32 v106, v248, v249\n"
    "v_cvt_pk_bf16_f32 v107, v250, v251\n"
    "v_mfma_f32_16x16x32_bf16 v[248:251], a[120:123], v[8:11], v[212:215]\n"
    "v_and_b32 v239, s74, v25\n"
    "v_lshlrev_b32 v240, 16, v26\n"
    "v_mfma_f32_16x16x32_bf16 v[248:251], a[124:127], v[12:15], v[248:251]\n"
    "v_and_b32 v241, s74, v26\n"
    "v_exp_f32 v200, v200\n"
    "v_mfma_f32_16x16x32_bf16 v[248:251], a[128:131], v[16:19], v[248:251]\n"
    "v_exp_f32 v201, v201\n"
    "v_mfma_f32_16x16x32_bf16 v[248:251], a[132:135], v[20:23], v[248:251]\n"
    "v_exp_f32 v202, v202\n"
    "v_exp_f32 v203, v203\n"
    "v_cvt_pk_bf16_f32 v108, v200, v201\n"
    "v_pk_mul_f32 v[238:239], v[238:239], s[72:73]\n"
    "v_pk_mul_f32 v[240:241], v[240:241], s[72:73]\n"
    "v_cvt_pk_bf16_f32 v109, v202, v203\n"
    "v_lshlrev_b32 v242, 16, v27\n"
    "v_and_b32 v243, s74, v27\n"
    "v_pk_mul_f32 v[242:243], v[242:243], s[72:73]\n"
    "v_exp_f32 v248, v248\n"
    "v_exp_f32 v249, v249\n"
    "v_exp_f32 v250, v250\n"
    "v_exp_f32 v251, v251\n"
    "v_cvt_pk_bf16_f32 v110, v248, v249\n"
    "v_cvt_pk_bf16_f32 v111, v250, v251\n"
    "v_cvt_pk_bf16_f32 v24, v236, v237\n"
    "v_cvt_pk_bf16_f32 v25, v238, v239\n"
    "v_smfmac_f32_16x16x64_bf16 a[244:247], a[240:243], v[104:111], v253\n"
    "v_cvt_pk_bf16_f32 v26, v240, v241\n"
    "v_cvt_pk_bf16_f32 v27, v242, v243\n"
    "v_lshlrev_b32 v236, 16, v28\n"
    "v_and_b32 v237, s74, v28\n"
    "v_pk_mul_f32 v[236:237], v[236:237], s[72:73]\n"
    "v_lshlrev_b32 v238, 16, v29\n"
    "v_mfma_f32_16x16x32_bf16 v[216:219], a[144:147], v[24:27], 0\n"
    "v_and_b32 v239, s74, v29\n"
    "v_lshlrev_b32 v240, 16, v30\n"
    "v_and_b32 v241, s74, v30\n"
    "v_lshlrev_b32 v242, 16, v31\n"
    "v_and_b32 v243, s74, v31\n"
    "v_pk_mul_f32 v[238:239], v[238:239], s[72:73]\n"
    "v_pk_mul_f32 v[240:241], v[240:241], s[72:73]\n"
    "v_pk_mul_f32 v[242:243], v[242:243], s[72:73]\n"
    "v_cvt_pk_bf16_f32 v28, v236, v237\n"
    "v_cvt_pk_bf16_f32 v29, v238, v239\n"
    "v_cvt_pk_bf16_f32 v30, v240, v241\n"
    "v_cvt_pk_bf16_f32 v31, v242, v243\n"
    "s_waitcnt lgkmcnt(0)\n"
    "v_lshlrev_b32 v236, 16, v32\n"
    "v_mfma_f32_16x16x32_bf16 v[216:219], a[148:151], v[28:31], v[216:219]\n"
    "v_and_b32 v237, s74, v32\n"
    "v_lshlrev_b32 v238, 16, v33\n"
    "v_and_b32 v239, s74, v33\n"
    "v_lshlrev_b32 v240, 16, v34\n"
    "v_and_b32 v241, s74, v34\n"
    "v_pk_mul_f32 v[236:237], v[236:237], s[72:73]\n"
    "v_pk_mul_f32 v[238:239], v[238:239], s[72:73]\n"
    "v_pk_mul_f32 v[240:241], v[240:241], s[72:73]\n"
    "v_lshlrev_b32 v242, 16, v35\n"
    "v_and_b32 v243, s74, v35\n"
    "v_pk_mul_f32 v[242:243], v[242:243], s[72:73]\n"
    "v_cvt_pk_bf16_f32 v32, v236, v237\n"
    "v_cvt_pk_bf16_f32 v33, v238, v239\n"
    "v_cvt_pk_bf16_f32 v34, v240, v241\n"
    "v_cvt_pk_bf16_f32 v35, v242, v243\n"
    "v_lshlrev_b32 v236, 16, v36\n"
    "v_and_b32 v237, s74, v36\n"
    "v_pk_mul_f32 v[236:237], v[236:237], s[72:73]\n"
    "v_lshlrev_b32 v238, 16, v37\n"
    "v_mfma_f32_16x16x32_bf16 v[216:219], a[152:155], v[32:35], v[216:219]\n"
    "v_and_b32 v239, s74, v37\n"
    "v_lshlrev_b32 v240, 16, v38\n"
    "v_and_b32 v241, s74, v38\n"
    "v_lshlrev_b32 v242, 16, v39\n"
    "v_and_b32 v243, s74, v39\n"
    "v_pk_mul_f32 v[238:239], v[238:239], s[72:73]\n"
    "v_pk_mul_f32 v[240:241], v[240:241], s[72:73]\n"
    "v_pk_mul_f32 v[242:243], v[242:243], s[72:73]\n"
    "v_cvt_pk_bf16_f32 v36, v236, v237\n"
    "v_cvt_pk_bf16_f32 v37, v238, v239\n"
    "v_cvt_pk_bf16_f32 v38, v240, v241\n"
    "v_cvt_pk_bf16_f32 v39, v242, v243\n"
    "s_waitcnt vmcnt(8)\n"
    "s_add_u32 m0, s62, 65536\n"
    "v_mfma_f32_16x16x32_bf16 v[216:219], a[156:159], v[36:39], v[216:219]\n"
    "s_add_u32 s63, s82, 16384\n"
    "buffer_load_dwordx4 v5, s[36:39], s63 offen nt lds\n"
    "ds_read_b128 v[40:43], v1 offset:32768\n"
    "ds_read_b128 v[44:47], v1 offset:33024\n"
    "ds_read_b128 v[48:51], v1 offset:33280\n"
    "ds_read_b128 v[52:55], v1 offset:33536\n"
    "buffer_load_dwordx4 v6, s[36:39], s63 offen offset:1024 nt lds\n"
    "buffer_load_dwordx4 v7, s[36:39], s63 offen offset:2048 nt lds\n"
    "v_sub_f32 v216, s83, v216\n"
    "v_sub_f32 v217, s83, v217\n"
    "v_sub_f32 v218, s83, v218\n"
    "v_sub_f32 v219, s83, v219\n"
    "s_waitcnt lgkmcnt(1)\n"
    "v_lshlrev_b32 v236, 16, v40\n"
    "v_mfma_f32_16x16x32_bf16 v[200:203], a[72:75], v[24:27], v[216:219]\n"
    "v_and_b32 v237, s74, v40\n"
    "v_lshlrev_b32 v238, 16, v41\n"
    "v_mfma_f32_16x16x32_bf16 v[200:203], a[76:79], v[28:31], v[200:203]\n"
    "v_and_b32 v239, s74, v41\n"
    "v_lshlrev_b32 v240, 16, v42\n"
    "v_mfma_f32_16x16x32_bf16 v[200:203], a[80:83], v[32:35], v[200:203]\n"
    "v_and_b32 v241, s74, v42\n"
    "v_lshlrev_b32 v242, 16, v43\n"
    "buffer_load_dwordx4 v255, s[36:39], s63 offen offset:3072 nt lds\n"
    "v_and_b32 v243, s74, v43\n"
    "v_pk_mul_f32 v[236:237], v[236:237], s[72:73]\n"
    "v_pk_mul_f32 v[238:239], v[238:239], s[72:73]\n"
    "v_pk_mul_f32 v[240:241], v[240:241], s[72:73]\n"
    "v_pk_mul_f32 v[242:243], v[242:243], s[72:73]\n"
    "v_cvt_pk_bf16_f32 v40, v236, v237\n"
    "v_mfma_f32_16x16x32_bf16 v[200:203], a[84:87], v[36:39], v[200:203]\n"
    "v_cvt_pk_bf16_f32 v41, v238, v239\n"
    "v_cvt_pk_bf16_f32 v42, v240, v241\n"
    "v_mfma_f32_16x16x32_bf16 v[248:251], a[88:91], v[24:27], v[216:219]\n"
    "v_cvt_pk_bf16_f32 v43, v242, v243\n"
    "v_lshlrev_b32 v236, 16, v44\n"
    "v_mfma_f32_16x16x32_bf16 v[248:251], a[92:95], v[28:31], v[248:251]\n"
    "v_and_b32 v237, s74, v44\n"
    "v_lshlrev_b32 v238, 16, v45\n"
    "v_mfma_f32_16x16x32_bf16 v[248:251], a[96:99], v[32:35], v[248:251]\n"
    "v_exp_f32 v200, v200\n"
    "v_mfma_f32_16x16x32_bf16 v[248:251], a[100:103], v[36:39], v[248:251]\n"
    "v_exp_f32 v201, v201\n"
    "v_mfma_f32_16x16x32_bf16 v[220:223], a[144:147], v[40:43], 0\n"
    "v_exp_f32 v202, v202\n"
    "v_exp_f32 v203, v203\n"
    "v_cvt_pk_bf16_f32 v112, v200, v201\n"
    "v_pk_mul_f32 v[236:237], v[236:237], s[72:73]\n"
    "v_cvt_pk_bf16_f32 v113, v202, v203\n"
    "v_mfma_f32_16x16x32_bf16 v[200:203], a[104:107], v[24:27], v[216:219]\n"
    "v_exp_f32 v248, v248\n"
    "v_mfma_f32_16x16x32_bf16 v[200:203], a[108:111], v[28:31], v[200:203]\n"
    "v_exp_f32 v249, v249\n"
    "v_mfma_f32_16x16x32_bf16 v[200:203], a[112:115], v[32:35], v[200:203]\n"
    "v_exp_f32 v250, v250\n"
    "v_mfma_f32_16x16x32_bf16 v[200:203], a[116:119], v[36:39], v[200:203]\n"
    "v_exp_f32 v251, v251\n"
    "v_cvt_pk_bf16_f32 v114, v248, v249\n"
    "v_cvt_pk_bf16_f32 v115, v250, v251\n"
    "v_mfma_f32_16x16x32_bf16 v[248:251], a[120:123], v[24:27], v[216:219]\n"
    "v_and_b32 v239, s74, v45\n"
    "v_lshlrev_b32 v240, 16, v46\n"
    "v_mfma_f32_16x16x32_bf16 v[248:251], a[124:127], v[28:31], v[248:251]\n"
    "v_and_b32 v241, s74, v46\n"
    "v_exp_f32 v200, v200\n"
    "v_mfma_f32_16x16x32_bf16 v[248:251], a[128:131], v[32:35], v[248:251]\n"
    "v_exp_f32 v201, v201\n"
    "v_mfma_f32_16x16x32_bf16 v[248:251], a[132:135], v[36:39], v[248:251]\n"
    "v_exp_f32 v202, v202\n"
    "v_exp_f32 v203, v203\n"
    "v_cvt_pk_bf16_f32 v116, v200, v201\n"
    "v_pk_mul_f32 v[238:239], v[238:239], s[72:73]\n"
    "v_pk_mul_f32 v[240:241], v[240:241], s[72:73]\n"
    "v_cvt_pk_bf16_f32 v117, v202, v203\n"
    "v_lshlrev_b32 v242, 16, v47\n"
    "v_and_b32 v243, s74, v47\n"
    "v_pk_mul_f32 v[242:243], v[242:243], s[72:73]\n"
    "v_exp_f32 v248, v248\n"
    "v_exp_f32 v249, v249\n"
    "v_exp_f32 v250, v250\n"
    "v_exp_f32 v251, v251\n"
    "v_cvt_pk_bf16_f32 v118, v248, v249\n"
    "v_cvt_pk_bf16_f32 v119, v250, v251\n"
    "v_cvt_pk_bf16_f32 v44, v236, v237\n"
    "v_cvt_pk_bf16_f32 v45, v238, v239\n"
    "v_smfmac_f32_16x16x64_bf16 a[248:251], a[240:243], v[112:119], v253\n"
    "v_cvt_pk_bf16_f32 v46, v240, v241\n"
    "v_cvt_pk_bf16_f32 v47, v242, v243\n"
    "v_lshlrev_b32 v236, 16, v48\n"
    "v_and_b32 v237, s74, v48\n"
    "v_pk_mul_f32 v[236:237], v[236:237], s[72:73]\n"
    "v_lshlrev_b32 v238, 16, v49\n"
    "v_mfma_f32_16x16x32_bf16 v[220:223], a[148:151], v[44:47], v[220:223]\n"
    "v_and_b32 v239, s74, v49\n"
    "v_lshlrev_b32 v240, 16, v50\n"
    "v_and_b32 v241, s74, v50\n"
    "v_lshlrev_b32 v242, 16, v51\n"
    "v_and_b32 v243, s74, v51\n"
    "v_pk_mul_f32 v[238:239], v[238:239], s[72:73]\n"
    "v_pk_mul_f32 v[240:241], v[240:241], s[72:73]\n"
    "v_pk_mul_f32 v[242:243], v[242:243], s[72:73]\n"
    "v_cvt_pk_bf16_f32 v48, v236, v237\n"
    "v_cvt_pk_bf16_f32 v49, v238, v239\n"
    "v_cvt_pk_bf16_f32 v50, v240, v241\n"
    "v_cvt_pk_bf16_f32 v51, v242, v243\n"
    "s_waitcnt lgkmcnt(0)\n"
    "v_lshlrev_b32 v236, 16, v52\n"
    "v_mfma_f32_16x16x32_bf16 v[220:223], a[152:155], v[48:51], v[220:223]\n"
    "v_and_b32 v237, s74, v52\n"
    "v_lshlrev_b32 v238, 16, v53\n"
    "v_and_b32 v239, s74, v53\n"
    "v_lshlrev_b32 v240, 16, v54\n"
    "v_and_b32 v241, s74, v54\n"
    "v_pk_mul_f32 v[236:237], v[236:237], s[72:73]\n"
    "v_pk_mul_f32 v[238:239], v[238:239], s[72:73]\n"
    "v_pk_mul_f32 v[240:241], v[240:241], s[72:73]\n"
    "v_lshlrev_b32 v242, 16, v55\n"
    "v_and_b32 v243, s74, v55\n"
    "v_pk_mul_f32 v[242:243], v[242:243], s[72:73]\n"
    "v_cvt_pk_bf16_f32 v52, v236, v237\n"
    "v_cvt_pk_bf16_f32 v53, v238, v239\n"
    "v_cvt_pk_bf16_f32 v54, v240, v241\n"
    "v_cvt_pk_bf16_f32 v55, v242, v243\n"
    "s_waitcnt vmcnt(8)\n"
    "s_barrier\n"
    "v_mfma_f32_16x16x32_bf16 v[220:223], a[156:159], v[52:55], v[220:223]\n"
    "s_add_u32 m0, s62, 81920\n"
    "s_add_u32 s63, s82, 20480\n"
    "buffer_load_dwordx4 v5, s[36:39], s63 offen nt lds\n"
    "ds_read_b128 a[192:195], v0 offset:32768\n"
    "ds_read_b128 a[196:199], v0 offset:33024\n"
    "ds_read_b128 a[200:203], v0 offset:33280\n"
    "ds_read_b128 a[204:207], v0 offset:33536\n"
    "ds_read_b128 a[208:211], v0 offset:36864\n"
    "v_sub_f32 v220, s83, v220\n"
    "v_sub_f32 v221, s83, v221\n"
    "v_sub_f32 v222, s83, v222\n"
    "v_sub_f32 v223, s83, v223\n"
    "ds_read_b128 a[212:215], v0 offset:37120\n"
    "buffer_load_dwordx4 v6, s[36:39], s63 offen offset:1024 nt lds\n"
    "v_mfma_f32_16x16x32_bf16 v[200:203], a[72:75], v[40:43], v[220:223]\n"
    "ds_read_b128 a[216:219], v0 offset:37376\n"
    "ds_read_b128 a[220:223], v0 offset:37632\n"
    "v_mfma_f32_16x16x32_bf16 v[200:203], a[76:79], v[44:47], v[200:203]\n"
    "ds_read_b128 a[168:171], v0 offset:40960\n"
    "ds_read_b128 a[172:175], v0 offset:41216\n"
    "v_mfma_f32_16x16x32_bf16 v[200:203], a[80:83], v[48:51], v[200:203]\n"
    "ds_read_b128 a[176:179], v0 offset:41472\n"
    "ds_read_b128 a[180:183], v0 offset:41728\n"
    "v_mfma_f32_16x16x32_bf16 v[200:203], a[84:87], v[52:55], v[200:203]\n"
    "buffer_load_dwordx4 v7, s[36:39], s63 offen offset:2048 nt lds\n"
    "ds_read_b128 a[184:187], v0 offset:45056\n"
    "v_mfma_f32_16x16x32_bf16 v[248:251], a[88:91], v[40:43], v[220:223]\n"
    "ds_read_b128 a[188:191], v0 offset:45312\n"
    "ds_read_b128 a[160:163], v0 offset:45568\n"
    "v_mfma_f32_16x16x32_bf16 v[248:251], a[92:95], v[44:47], v[248:251]\n"
    "ds_read_b128 a[164:167], v0 offset:45824\n"
    "v_mov_b32 v0, v3\n"
    "v_mfma_f32_16x16x32_bf16 v[248:251], a[96:99], v[48:51], v[248:251]\n"
    "v_exp_f32 v200, v200\n"
    "v_mfma_f32_16x16x32_bf16 v[248:251], a[100:103], v[52:55], v[248:251]\n"
    "buffer_load_dwordx4 v255, s[36:39], s63 offen offset:3072 nt lds\n"
    "v_exp_f32 v201, v201\n"
    "s_waitcnt lgkmcnt(8)\n"
    "v_mfma_f32_16x16x32_bf16 v[204:207], a[192:195], v[8:11], v[212:215]\n"
    "v_exp_f32 v202, v202\n"
    "v_mfma_f32_16x16x32_bf16 v[204:207], a[196:199], v[12:15], v[204:207]\n"
    "v_exp_f32 v203, v203\n"
    "v_mfma_f32_16x16x32_bf16 v[204:207], a[200:203], v[16:19], v[204:207]\n"
    "v_cvt_pk_bf16_f32 v120, v200, v201\n"
    "v_cvt_pk_bf16_f32 v121, v202, v203\n"
    "v_mfma_f32_16x16x32_bf16 v[200:203], a[104:107], v[40:43], v[220:223]\n"
    "v_exp_f32 v248, v248\n"
    "v_mfma_f32_16x16x32_bf16 v[200:203], a[108:111], v[44:47], v[200:203]\n"
    "v_exp_f32 v249, v249\n"
    "v_mfma_f32_16x16x32_bf16 v[200:203], a[112:115], v[48:51], v[200:203]\n"
    "v_exp_f32 v250, v250\n"
    "v_mfma_f32_16x16x32_bf16 v[200:203], a[116:119], v[52:55], v[200:203]\n"
    "v_exp_f32 v251, v251\n"
    "v_mfma_f32_16x16x32_bf16 v[204:207], a[204:207], v[20:23], v[204:207]\n"
    "v_cvt_pk_bf16_f32 v122, v248, v249\n"
    "v_cvt_pk_bf16_f32 v123, v250, v251\n"
    "v_mfma_f32_16x16x32_bf16 v[248:251], a[120:123], v[40:43], v[220:223]\n"
    "v_mfma_f32_16x16x32_bf16 v[248:251], a[124:127], v[44:47], v[248:251]\n"
    "v_mfma_f32_16x16x32_bf16 v[248:251], a[128:131], v[48:51], v[248:251]\n"
    "v_mfma_f32_16x16x32_bf16 v[248:251], a[132:135], v[52:55], v[248:251]\n"
    "v_exp_f32 v200, v200\n"
    "v_mfma_f32_16x16x32_bf16 v[208:211], a[208:211], v[8:11], v[212:215]\n"
    "v_exp_f32 v201, v201\n"
    "v_mfma_f32_16x16x32_bf16 v[208:211], a[212:215], v[12:15], v[208:211]\n"
    "v_exp_f32 v202, v202\n"
    "v_mfma_f32_16x16x32_bf16 v[208:211], a[216:219], v[16:19], v[208:211]\n"
    "v_exp_f32 v203, v203\n"
    "v_mfma_f32_16x16x32_bf16 v[208:211], a[220:223], v[20:23], v[208:211]\n"
    "v_cvt_pk_bf16_f32 v124, v200, v201\n"
    "v_cvt_pk_bf16_f32 v125, v202, v203\n"
    "v_exp_f32 v248, v248\n"
    "v_exp_f32 v249, v249\n"
    "v_exp_f32 v250, v250\n"
    "v_exp_f32 v251, v251\n"
    "v_cvt_pk_bf16_f32 v126, v248, v249\n"
    "v_cvt_pk_bf16_f32 v127, v250, v251\n"
    "v_exp_f32 v204, v204\n"
    "v_exp_f32 v205, v205\n"
    "v_smfmac_f32_16x16x64_bf16 a[252:255], a[240:243], v[120:127], v253\n"
    "v_exp_f32 v206, v206\n"
    "v_exp_f32 v207, v207\n"
    "v_cvt_pk_bf16_f32 v152, v204, v205\n"
    "v_cvt_pk_bf16_f32 v153, v206, v207\n"
    "s_waitcnt lgkmcnt(0)\n"
    "v_mfma_f32_16x16x32_bf16 v[204:207], a[168:171], v[8:11], v[212:215]\n"
    "v_exp_f32 v208, v208\n"
    "v_mfma_f32_16x16x32_bf16 v[204:207], a[172:175], v[12:15], v[204:207]\n"
    "v_exp_f32 v209, v209\n"
    "v_mfma_f32_16x16x32_bf16 v[204:207], a[176:179], v[16:19], v[204:207]\n"
    "v_exp_f32 v210, v210\n"
    "v_mfma_f32_16x16x32_bf16 v[204:207], a[180:183], v[20:23], v[204:207]\n"
    "v_exp_f32 v211, v211\n"
    "v_cvt_pk_bf16_f32 v154, v208, v209\n"
    "v_cvt_pk_bf16_f32 v155, v210, v211\n"
    "v_mfma_f32_16x16x32_bf16 v[208:211], a[184:187], v[8:11], v[212:215]\n"
    "v_mfma_f32_16x16x32_bf16 v[208:211], a[188:191], v[12:15], v[208:211]\n"
    "v_mfma_f32_16x16x32_bf16 v[208:211], a[160:163], v[16:19], v[208:211]\n"
    "v_mfma_f32_16x16x32_bf16 v[208:211], a[164:167], v[20:23], v[208:211]\n"
    "s_waitcnt vmcnt(8)\n"
    "v_exp_f32 v204, v204\n"
    "v_exp_f32 v205, v205\n"
    "v_exp_f32 v206, v206\n"
    "v_exp_f32 v207, v207\n"
    "v_cvt_pk_bf16_f32 v156, v204, v205\n"
    "v_cvt_pk_bf16_f32 v157, v206, v207\n"
    "v_mfma_f32_16x16x32_bf16 v[204:207], a[192:195], v[24:27], v[216:219]\n"
    "s_add_u32 m0, s62, 114688\n"
    "v_exp_f32 v208, v208\n"
    "v_mfma_f32_16x16x32_bf16 v[204:207], a[196:199], v[28:31], v[204:207]\n"
    "s_add_u32 s63, s59, 0\n"
    "v_exp_f32 v209, v209\n"
    "v_mfma_f32_16x16x32_bf16 v[204:207], a[200:203], v[32:35], v[204:207]\n"
    "buffer_load_dwordx4 v5, s[44:47], s63 offen nt lds\n"
    "v_exp_f32 v210, v210\n"
    "v_mfma_f32_16x16x32_bf16 v[204:207], a[204:207], v[36:39], v[204:207]\n"
    "ds_read_b128 v[56:59], v1 offset:49152\n"
    "v_exp_f32 v211, v211\n"
    "v_cvt_pk_bf16_f32 v158, v208, v209\n"
    "v_cvt_pk_bf16_f32 v159, v210, v211\n"
    "v_mfma_f32_16x16x32_bf16 v[208:211], a[208:211], v[24:27], v[216:219]\n"
    "ds_read_b128 v[60:63], v1 offset:49408\n"
    "ds_read_b128 v[64:67], v1 offset:49664\n"
    "v_mfma_f32_16x16x32_bf16 v[208:211], a[212:215], v[28:31], v[208:211]\n"
    "ds_read_b128 v[68:71], v1 offset:49920\n"
    "v_exp_f32 v204, v204\n"
    "v_mfma_f32_16x16x32_bf16 v[208:211], a[216:219], v[32:35], v[208:211]\n"
    "buffer_load_dwordx4 v6, s[44:47], s63 offen offset:1024 nt lds\n"
    "v_exp_f32 v205, v205\n"
    "v_mfma_f32_16x16x32_bf16 v[208:211], a[220:223], v[36:39], v[208:211]\n"
    "v_exp_f32 v206, v206\n"
    "v_exp_f32 v207, v207\n"
    "v_cvt_pk_bf16_f32 v160, v204, v205\n"
    "v_cvt_pk_bf16_f32 v161, v206, v207\n"
    "v_mfma_f32_16x16x32_bf16 v[204:207], a[168:171], v[24:27], v[216:219]\n"
    "v_add_u32 v1, 114688, v4\n"
    "v_mfma_f32_16x16x32_bf16 v[204:207], a[172:175], v[28:31], v[204:207]\n"
    "s_waitcnt lgkmcnt(3)\n"
    "v_lshlrev_b32 v236, 16, v56\n"
    "v_mfma_f32_16x16x32_bf16 v[204:207], a[176:179], v[32:35], v[204:207]\n"
    "buffer_load_dwordx4 v7, s[44:47], s63 offen offset:2048 nt lds\n"
    "v_exp_f32 v208, v208\n"
    "v_mfma_f32_16x16x32_bf16 v[204:207], a[180:183], v[36:39], v[204:207]\n"
    "v_exp_f32 v209, v209\n"
    "v_exp_f32 v210, v210\n"
    "v_exp_f32 v211, v211\n"
    "v_cvt_pk_bf16_f32 v162, v208, v209\n"
    "v_cvt_pk_bf16_f32 v163, v210, v211\n"
    "v_mfma_f32_16x16x32_bf16 v[208:211], a[184:187], v[24:27], v[216:219]\n"
    "v_and_b32 v237, s74, v56\n"
    "v_lshlrev_b32 v238, 16, v57\n"
    "v_mfma_f32_16x16x32_bf16 v[208:211], a[188:191], v[28:31], v[208:211]\n"
    "buffer_load_dwordx4 v255, s[44:47], s63 offen offset:3072 nt lds\n"
    "v_exp_f32 v204, v204\n"
    "v_mfma_f32_16x16x32_bf16 v[208:211], a[160:163], v[32:35], v[208:211]\n"
    "v_exp_f32 v205, v205\n"
    "v_mfma_f32_16x16x32_bf16 v[208:211], a[164:167], v[36:39], v[208:211]\n"
    "v_exp_f32 v206, v206\n"
    "v_exp_f32 v207, v207\n"
    "v_cvt_pk_bf16_f32 v164, v204, v205\n"
    "v_pk_mul_f32 v[236:237], v[236:237], s[72:73]\n"
    "v_cvt_pk_bf16_f32 v165, v206, v207\n"
    "v_mfma_f32_16x16x32_bf16 v[204:207], a[192:195], v[40:43], v[220:223]\n"
    "v_and_b32 v239, s74, v57\n"
    "v_lshlrev_b32 v240, 16, v58\n"
    "v_mfma_f32_16x16x32_bf16 v[204:207], a[196:199], v[44:47], v[204:207]\n"
    "v_exp_f32 v208, v208\n"
    "v_mfma_f32_16x16x32_bf16 v[204:207], a[200:203], v[48:51], v[204:207]\n"
    "v_exp_f32 v209, v209\n"
    "v_mfma_f32_16x16x32_bf16 v[204:207], a[204:207], v[52:55], v[204:207]\n"
    "v_exp_f32 v210, v210\n"
    "v_exp_f32 v211, v211\n"
    "v_cvt_pk_bf16_f32 v166, v208, v209\n"
    "v_pk_mul_f32 v[238:239], v[238:239], s[72:73]\n"
    "v_cvt_pk_bf16_f32 v167, v210, v211\n"
    "v_mfma_f32_16x16x32_bf16 v[208:211], a[208:211], v[40:43], v[220:223]\n"
    "v_and_b32 v241, s74, v58\n"
    "v_lshlrev_b32 v242, 16, v59\n"
    "v_mfma_f32_16x16x32_bf16 v[208:211], a[212:215], v[44:47], v[208:211]\n"
    "v_exp_f32 v204, v204\n"
    "v_mfma_f32_16x16x32_bf16 v[208:211], a[216:219], v[48:51], v[208:211]\n"
    "v_exp_f32 v205, v205\n"
    "v_mfma_f32_16x16x32_bf16 v[208:211], a[220:223], v[52:55], v[208:211]\n"
    "v_exp_f32 v206, v206\n"
    "v_exp_f32 v207, v207\n"
    "v_cvt_pk_bf16_f32 v168, v204, v205\n"
    "v_pk_mul_f32 v[240:241], v[240:241], s[72:73]\n"
    "v_cvt_pk_bf16_f32 v169, v206, v207\n"
    "v_mfma_f32_16x16x32_bf16 v[204:207], a[168:171], v[40:43], v[220:223]\n"
    "v_and_b32 v243, s74, v59\n"
    "v_cvt_pk_bf16_f32 v56, v236, v237\n"
    "v_mfma_f32_16x16x32_bf16 v[204:207], a[172:175], v[44:47], v[204:207]\n"
    "v_exp_f32 v208, v208\n"
    "v_mfma_f32_16x16x32_bf16 v[204:207], a[176:179], v[48:51], v[204:207]\n"
    "v_exp_f32 v209, v209\n"
    "v_mfma_f32_16x16x32_bf16 v[204:207], a[180:183], v[52:55], v[204:207]\n"
    "v_exp_f32 v210, v210\n"
    "v_exp_f32 v211, v211\n"
    "v_cvt_pk_bf16_f32 v170, v208, v209\n"
    "v_pk_mul_f32 v[242:243], v[242:243], s[72:73]\n"
    "v_cvt_pk_bf16_f32 v171, v210, v211\n"
    "v_mfma_f32_16x16x32_bf16 v[208:211], a[184:187], v[40:43], v[220:223]\n"
    "v_cvt_pk_bf16_f32 v57, v238, v239\n"
    "v_cvt_pk_bf16_f32 v58, v240, v241\n"
    "v_mfma_f32_16x16x32_bf16 v[208:211], a[188:191], v[44:47], v[208:211]\n"
    "v_exp_f32 v204, v204\n"
    "v_mfma_f32_16x16x32_bf16 v[208:211], a[160:163], v[48:51], v[208:211]\n"
    "v_exp_f32 v205, v205\n"
    "v_mfma_f32_16x16x32_bf16 v[208:211], a[164:167], v[52:55], v[208:211]\n"
    "v_exp_f32 v206, v206\n"
    "v_exp_f32 v207, v207\n"
    "v_cvt_pk_bf16_f32 v172, v204, v205\n"
    "v_cvt_pk_bf16_f32 v173, v206, v207\n"
    "v_cvt_pk_bf16_f32 v59, v242, v243\n"
    "s_waitcnt lgkmcnt(0)\n"
    "v_lshlrev_b32 v236, 16, v60\n"
    "v_mfma_f32_16x16x32_bf16 v[224:227], a[144:147], v[56:59], 0\n"
    "v_exp_f32 v208, v208\n"
    "v_exp_f32 v209, v209\n"
    "v_exp_f32 v210, v210\n"
    "v_exp_f32 v211, v211\n"
    "v_cvt_pk_bf16_f32 v174, v208, v209\n"
    "v_cvt_pk_bf16_f32 v175, v210, v211\n"
    "v_and_b32 v237, s74, v60\n"
    "v_pk_mul_f32 v[236:237], v[236:237], s[72:73]\n"
    "v_lshlrev_b32 v238, 16, v61\n"
    "v_and_b32 v239, s74, v61\n"
    "v_pk_mul_f32 v[238:239], v[238:239], s[72:73]\n"
    "v_lshlrev_b32 v240, 16, v62\n"
    "v_and_b32 v241, s74, v62\n"
    "v_pk_mul_f32 v[240:241], v[240:241], s[72:73]\n"
    "v_lshlrev_b32 v242, 16, v63\n"
    "v_and_b32 v243, s74, v63\n"
    "v_pk_mul_f32 v[242:243], v[242:243], s[72:73]\n"
    "v_cvt_pk_bf16_f32 v60, v236, v237\n"
    "v_cvt_pk_bf16_f32 v61, v238, v239\n"
    "v_cvt_pk_bf16_f32 v62, v240, v241\n"
    "v_cvt_pk_bf16_f32 v63, v242, v243\n"
    "v_lshlrev_b32 v236, 16, v64\n"
    "v_and_b32 v237, s74, v64\n"
    "v_pk_mul_f32 v[236:237], v[236:237], s[72:73]\n"
    "v_lshlrev_b32 v238, 16, v65\n"
    "v_mfma_f32_16x16x32_bf16 v[224:227], a[148:151], v[60:63], v[224:227]\n"
    "s_waitcnt vmcnt(8)\n"
    "v_and_b32 v239, s74, v65\n"
    "v_lshlrev_b32 v240, 16, v66\n"
    "v_and_b32 v241, s74, v66\n"
    "v_lshlrev_b32 v242, 16, v67\n"
    "v_and_b32 v243, s74, v67\n"
    "v_pk_mul_f32 v[238:239], v[238:239], s[72:73]\n"
    "v_pk_mul_f32 v[240:241], v[240:241], s[72:73]\n"
    "v_pk_mul_f32 v[242:243], v[242:243], s[72:73]\n"
    "v_cvt_pk_bf16_f32 v64, v236, v237\n"
    "v_cvt_pk_bf16_f32 v65, v238, v239\n"
    "v_cvt_pk_bf16_f32 v66, v240, v241\n"
    "v_cvt_pk_bf16_f32 v67, v242, v243\n"
    "v_lshlrev_b32 v236, 16, v68\n"
    "v_and_b32 v237, s74, v68\n"
    "v_pk_mul_f32 v[236:237], v[236:237], s[72:73]\n"
    "v_lshlrev_b32 v238, 16, v69\n"
    "v_mfma_f32_16x16x32_bf16 v[224:227], a[152:155], v[64:67], v[224:227]\n"
    "s_add_u32 m0, s62, 147456\n"
    "v_and_b32 v239, s74, v69\n"
    "v_lshlrev_b32 v240, 16, v70\n"
    "v_and_b32 v241, s74, v70\n"
    "v_lshlrev_b32 v242, 16, v71\n"
    "v_and_b32 v243, s74, v71\n"
    "v_pk_mul_f32 v[238:239], v[238:239], s[72:73]\n"
    "v_pk_mul_f32 v[240:241], v[240:241], s[72:73]\n"
    "v_pk_mul_f32 v[242:243], v[242:243], s[72:73]\n"
    "v_cvt_pk_bf16_f32 v68, v236, v237\n"
    "v_cvt_pk_bf16_f32 v69, v238, v239\n"
    "v_cvt_pk_bf16_f32 v70, v240, v241\n"
    "v_cvt_pk_bf16_f32 v71, v242, v243\n"
    "s_add_u32 s63, s59, 16384\n"
    "buffer_load_dwordx4 v5, s[44:47], s63 offen nt lds\n"
    "v_mfma_f32_16x16x32_bf16 v[224:227], a[156:159], v[68:71], v[224:227]\n"
    "ds_read_b128 v[72:75], v2 offset:0\n"
    "ds_read_b128 v[76:79], v2 offset:256\n"
    "ds_read_b128 v[80:83], v2 offset:512\n"
    "ds_read_b128 v[84:87], v2 offset:768\n"
    "buffer_load_dwordx4 v6, s[44:47], s63 offen offset:1024 nt lds\n"
    "buffer_load_dwordx4 v7, s[44:47], s63 offen offset:2048 nt lds\n"
    "s_waitcnt lgkmcnt(3)\n"
    "v_lshlrev_b32 v236, 16, v72\n"
    "v_sub_f32 v224, s83, v224\n"
    "v_sub_f32 v225, s83, v225\n"
    "v_sub_f32 v226, s83, v226\n"
    "v_sub_f32 v227, s83, v227\n"
    "v_and_b32 v237, s74, v72\n"
    "v_pk_mul_f32 v[236:237], v[236:237], s[72:73]\n"
    "v_lshlrev_b32 v238, 16, v73\n"
    "v_mfma_f32_16x16x32_bf16 v[200:203], a[72:75], v[56:59], v[224:227]\n"
    "v_and_b32 v239, s74, v73\n"
    "v_lshlrev_b32 v240, 16, v74\n"
    "v_mfma_f32_16x16x32_bf16 v[200:203], a[76:79], v[60:63], v[200:203]\n"
    "v_and_b32 v241, s74, v74\n"
    "v_lshlrev_b32 v242, 16, v75\n"
    "v_mfma_f32_16x16x32_bf16 v[200:203], a[80:83], v[64:67], v[200:203]\n"
    "buffer_load_dwordx4 v255, s[44:47], s63 offen offset:3072 nt lds\n"
    "v_and_b32 v243, s74, v75\n"
    "v_cvt_pk_bf16_f32 v72, v236, v237\n"
    "s_waitcnt lgkmcnt(0)\n"
    "v_lshlrev_b32 v236, 16, v76\n"
    "v_pk_mul_f32 v[238:239], v[238:239], s[72:73]\n"
    "v_pk_mul_f32 v[240:241], v[240:241], s[72:73]\n"
    "v_pk_mul_f32 v[242:243], v[242:243], s[72:73]\n"
    "v_cvt_pk_bf16_f32 v73, v238, v239\n"
    "v_mfma_f32_16x16x32_bf16 v[200:203], a[84:87], v[68:71], v[200:203]\n"
    "v_cvt_pk_bf16_f32 v74, v240, v241\n"
    "v_cvt_pk_bf16_f32 v75, v242, v243\n"
    "v_mfma_f32_16x16x32_bf16 v[248:251], a[88:91], v[56:59], v[224:227]\n"
    "v_and_b32 v237, s74, v76\n"
    "v_lshlrev_b32 v238, 16, v77\n"
    "v_mfma_f32_16x16x32_bf16 v[248:251], a[92:95], v[60:63], v[248:251]\n"
    "v_and_b32 v239, s74, v77\n"
    "v_lshlrev_b32 v240, 16, v78\n"
    "v_mfma_f32_16x16x32_bf16 v[248:251], a[96:99], v[64:67], v[248:251]\n"
    "v_exp_f32 v200, v200\n"
    "v_mfma_f32_16x16x32_bf16 v[248:251], a[100:103], v[68:71], v[248:251]\n"
    "v_exp_f32 v201, v201\n"
    "v_mfma_f32_16x16x32_bf16 v[204:207], a[192:195], v[56:59], v[224:227]\n"
    "v_exp_f32 v202, v202\n"
    "v_mfma_f32_16x16x32_bf16 v[204:207], a[196:199], v[60:63], v[204:207]\n"
    "v_exp_f32 v203, v203\n"
    "v_mfma_f32_16x16x32_bf16 v[204:207], a[200:203], v[64:67], v[204:207]\n"
    "v_cvt_pk_bf16_f32 v128, v200, v201\n"
    "v_cvt_pk_bf16_f32 v129, v202, v203\n"
    "v_mfma_f32_16x16x32_bf16 v[200:203], a[104:107], v[56:59], v[224:227]\n"
    "v_exp_f32 v248, v248\n"
    "v_mfma_f32_16x16x32_bf16 v[200:203], a[108:111], v[60:63], v[200:203]\n"
    "v_exp_f32 v249, v249\n"
    "v_mfma_f32_16x16x32_bf16 v[200:203], a[112:115], v[64:67], v[200:203]\n"
    "v_exp_f32 v250, v250\n"
    "v_mfma_f32_16x16x32_bf16 v[200:203], a[116:119], v[68:71], v[200:203]\n"
    "v_exp_f32 v251, v251\n"
    "v_mfma_f32_16x16x32_bf16 v[204:207], a[204:207], v[68:71], v[204:207]\n"
    "v_cvt_pk_bf16_f32 v130, v248, v249\n"
    "v_cvt_pk_bf16_f32 v131, v250, v251\n"
    "v_mfma_f32_16x16x32_bf16 v[248:251], a[120:123], v[56:59], v[224:227]\n"
    "v_and_b32 v241, s74, v78\n"
    "v_lshlrev_b32 v242, 16, v79\n"
    "v_and_b32 v243, s74, v79\n"
    "v_exp_f32 v200, v200\n"
    "v_pk_mul_f32 v[236:237], v[236:237], s[72:73]\n"
    "v_pk_mul_f32 v[238:239], v[238:239], s[72:73]\n"
    "v_pk_mul_f32 v[240:241], v[240:241], s[72:73]\n"
    "v_pk_mul_f32 v[242:243], v[242:243], s[72:73]\n"
    "v_exp_f32 v201, v201\n"
    "v_mfma_f32_16x16x32_bf16 v[248:251], a[124:127], v[60:63], v[248:251]\n"
    "v_exp_f32 v202, v202\n"
    "v_mfma_f32_16x16x32_bf16 v[248:251], a[128:131], v[64:67], v[248:251]\n"
    "v_exp_f32 v203, v203\n"
    "v_mfma_f32_16x16x32_bf16 v[248:251], a[132:135], v[68:71], v[248:251]\n"
    "v_cvt_pk_bf16_f32 v132, v200, v201\n"
    "v_cvt_pk_bf16_f32 v133, v202, v203\n"
    "v_mfma_f32_16x16x32_bf16 v[208:211], a[208:211], v[56:59], v[224:227]\n"
    "v_exp_f32 v204, v204\n"
    "v_mfma_f32_16x16x32_bf16 v[208:211], a[212:215], v[60:63], v[208:211]\n"
    "v_exp_f32 v205, v205\n"
    "v_mfma_f32_16x16x32_bf16 v[208:211], a[216:219], v[64:67], v[208:211]\n"
    "v_exp_f32 v206, v206\n"
    "v_mfma_f32_16x16x32_bf16 v[208:211], a[220:223], v[68:71], v[208:211]\n"
    "v_exp_f32 v248, v248\n"
    "v_mfma_f32_16x16x32_bf16 v[228:231], a[144:147], v[72:75], 0\n"
    "v_exp_f32 v249, v249\n"
    "v_exp_f32 v250, v250\n"
    "v_exp_f32 v251, v251\n"
    "v_cvt_pk_bf16_f32 v134, v248, v249\n"
    "v_cvt_pk_bf16_f32 v135, v250, v251\n"
    "v_exp_f32 v207, v207\n"
    "v_cvt_pk_bf16_f32 v176, v204, v205\n"
    "v_cvt_pk_bf16_f32 v177, v206, v207\n"
    "v_mfma_f32_16x16x32_bf16 v[204:207], a[168:171], v[56:59], v[224:227]\n"
    "v_exp_f32 v208, v208\n"
    "v_mfma_f32_16x16x32_bf16 v[204:207], a[172:175], v[60:63], v[204:207]\n"
    "s_waitcnt vmcnt(8)\n"
    "v_exp_f32 v209, v209\n"
    "v_mfma_f32_16x16x32_bf16 v[204:207], a[176:179], v[64:67], v[204:207]\n"
    "s_add_u32 m0, s62, 0\n"
    "v_exp_f32 v210, v210\n"
    "v_mfma_f32_16x16x32_bf16 v[204:207], a[180:183], v[68:71], v[204:207]\n"
    "s_add_u32 s63, s59, 32768\n"
    "v_exp_f32 v211, v211\n"
    "v_cvt_pk_bf16_f32 v178, v208, v209\n"
    "v_cvt_pk_bf16_f32 v179, v210, v211\n"
    "v_mfma_f32_16x16x32_bf16 v[208:211], a[184:187], v[56:59], v[224:227]\n"
    "buffer_load_dwordx4 v5, s[40:43], s63 offen nt lds\n"
    "v_cvt_pk_bf16_f32 v76, v236, v237\n"
    "v_mfma_f32_16x16x32_bf16 v[208:211], a[188:191], v[60:63], v[208:211]\n"
    "ds_read_b128 v[88:91], v2 offset:16384\n"
    "v_exp_f32 v204, v204\n"
    "v_mfma_f32_16x16x32_bf16 v[208:211], a[160:163], v[64:67], v[208:211]\n"
    "ds_read_b128 v[92:95], v2 offset:16640\n"
    "v_exp_f32 v205, v205\n"
    "v_mfma_f32_16x16x32_bf16 v[208:211], a[164:167], v[68:71], v[208:211]\n"
    "ds_read_b128 v[96:99], v2 offset:16896\n"
    "v_exp_f32 v206, v206\n"
    "v_exp_f32 v207, v207\n"
    "v_cvt_pk_bf16_f32 v180, v204, v205\n"
    "v_cvt_pk_bf16_f32 v181, v206, v207\n"
    "v_cvt_pk_bf16_f32 v77, v238, v239\n"
    "v_cvt_pk_bf16_f32 v78, v240, v241\n"
    "v_cvt_pk_bf16_f32 v79, v242, v243\n"
    "v_exp_f32 v208, v208\n"
    "v_exp_f32 v209, v209\n"
    "v_mfma_f32_16x16x32_bf16 v[228:231], a[148:151], v[76:79], v[228:231]\n"
    "buffer_load_dwordx4 v6, s[40:43], s63 offen offset:1024 nt lds\n"
    "v_exp_f32 v210, v210\n"
    "v_exp_f32 v211, v211\n"
    "v_cvt_pk_bf16_f32 v182, v208, v209\n"
    "v_cvt_pk_bf16_f32 v183, v210, v211\n"
    "v_lshlrev_b32 v236, 16, v80\n"
    "v_and_b32 v237, s74, v80\n"
    "v_pk_mul_f32 v[236:237], v[236:237], s[72:73]\n"
    "v_lshlrev_b32 v238, 16, v81\n"
    "v_and_b32 v239, s74, v81\n"
    "v_pk_mul_f32 v[238:239], v[238:239], s[72:73]\n"
    "v_lshlrev_b32 v240, 16, v82\n"
    "v_and_b32 v241, s74, v82\n"
    "v_pk_mul_f32 v[240:241], v[240:241], s[72:73]\n"
    "v_lshlrev_b32 v242, 16, v83\n"
    "v_and_b32 v243, s74, v83\n"
    "v_pk_mul_f32 v[242:243], v[242:243], s[72:73]\n"
    "v_cvt_pk_bf16_f32 v80, v236, v237\n"
    "v_cvt_pk_bf16_f32 v81, v238, v239\n"
    "v_cvt_pk_bf16_f32 v82, v240, v241\n"
    "v_cvt_pk_bf16_f32 v83, v242, v243\n"
    "v_lshlrev_b32 v236, 16, v84\n"
    "v_and_b32 v237, s74, v84\n"
    "v_pk_mul_f32 v[236:237], v[236:237], s[72:73]\n"
    "v_lshlrev_b32 v238, 16, v85\n"
    "v_mfma_f32_16x16x32_bf16 v[228:231], a[152:155], v[80:83], v[228:231]\n"
    "buffer_load_dwordx4 v7, s[40:43], s63 offen offset:2048 nt lds\n"
    "v_and_b32 v239, s74, v85\n"
    "v_lshlrev_b32 v240, 16, v86\n"
    "v_and_b32 v241, s74, v86\n"
    "v_lshlrev_b32 v242, 16, v87\n"
    "v_pk_mul_f32 v[238:239], v[238:239], s[72:73]\n"
    "v_pk_mul_f32 v[240:241], v[240:241], s[72:73]\n"
    "v_and_b32 v243, s74, v87\n"
    "v_pk_mul_f32 v[242:243], v[242:243], s[72:73]\n"
    "v_cvt_pk_bf16_f32 v84, v236, v237\n"
    "v_cvt_pk_bf16_f32 v85, v238, v239\n"
    "v_cvt_pk_bf16_f32 v86, v240, v241\n"
    "v_cvt_pk_bf16_f32 v87, v242, v243\n"
    "ds_read_b128 v[100:103], v2 offset:17152\n"
    "s_waitcnt lgkmcnt(1)\n"
    "v_lshlrev_b32 v236, 16, v88\n"
    "v_mfma_f32_16x16x32_bf16 v[228:231], a[156:159], v[84:87], v[228:231]\n"
    "buffer_load_dwordx4 v255, s[40:43], s63 offen offset:3072 nt lds\n"
    "v_and_b32 v237, s74, v88\n"
    "v_lshlrev_b32 v238, 16, v89\n"
    "v_and_b32 v239, s74, v89\n"
    "v_lshlrev_b32 v240, 16, v90\n"
    "v_pk_mul_f32 v[236:237], v[236:237], s[72:73]\n"
    "v_pk_mul_f32 v[238:239], v[238:239], s[72:73]\n"
    "v_and_b32 v241, s74, v90\n"
    "v_pk_mul_f32 v[240:241], v[240:241], s[72:73]\n"
    "v_sub_f32 v228, s83, v228\n"
    "v_sub_f32 v229, s83, v229\n"
    "v_sub_f32 v230, s83, v230\n"
    "v_sub_f32 v231, s83, v231\n"
    "v_lshlrev_b32 v242, 16, v91\n"
    "v_and_b32 v243, s74, v91\n"
    "v_pk_mul_f32 v[242:243], v[242:243], s[72:73]\n"
    "v_cvt_pk_bf16_f32 v88, v236, v237\n"
    "v_mfma_f32_16x16x32_bf16 v[200:203], a[72:75], v[72:75], v[228:231]\n"
    "v_cvt_pk_bf16_f32 v89, v238, v239\n"
    "v_cvt_pk_bf16_f32 v90, v240, v241\n"
    "v_mfma_f32_16x16x32_bf16 v[200:203], a[76:79], v[76:79], v[200:203]\n"
    "v_cvt_pk_bf16_f32 v91, v242, v243\n"
    "v_lshlrev_b32 v236, 16, v92\n"
    "v_mfma_f32_16x16x32_bf16 v[200:203], a[80:83], v[80:83], v[200:203]\n"
    "v_and_b32 v237, s74, v92\n"
    "v_lshlrev_b32 v238, 16, v93\n"
    "v_mfma_f32_16x16x32_bf16 v[200:203], a[84:87], v[84:87], v[200:203]\n"
    "v_and_b32 v239, s74, v93\n"
    "v_lshlrev_b32 v240, 16, v94\n"
    "v_mfma_f32_16x16x32_bf16 v[248:251], a[88:91], v[72:75], v[228:231]\n"
    "v_and_b32 v241, s74, v94\n"
    "v_lshlrev_b32 v242, 16, v95\n"
    "v_and_b32 v243, s74, v95\n"
    "v_xor_b32 v2, 32, v1\n"
    "v_pk_mul_f32 v[236:237], v[236:237], s[72:73]\n"
    "v_pk_mul_f32 v[238:239], v[238:239], s[72:73]\n"
    "v_pk_mul_f32 v[240:241], v[240:241], s[72:73]\n"
    "v_pk_mul_f32 v[242:243], v[242:243], s[72:73]\n"
    "v_exp_f32 v200, v200\n"
    "v_mfma_f32_16x16x32_bf16 v[248:251], a[92:95], v[76:79], v[248:251]\n"
    "v_exp_f32 v201, v201\n"
    "v_mfma_f32_16x16x32_bf16 v[248:251], a[96:99], v[80:83], v[248:251]\n"
    "v_exp_f32 v202, v202\n"
    "v_mfma_f32_16x16x32_bf16 v[248:251], a[100:103], v[84:87], v[248:251]\n"
    "v_exp_f32 v203, v203\n"
    "v_mfma_f32_16x16x32_bf16 v[204:207], a[192:195], v[72:75], v[228:231]\n"
    "v_cvt_pk_bf16_f32 v136, v200, v201\n"
    "v_cvt_pk_bf16_f32 v137, v202, v203\n"
    "v_mfma_f32_16x16x32_bf16 v[200:203], a[104:107], v[72:75], v[228:231]\n"
    "v_cvt_pk_bf16_f32 v92, v236, v237\n"
    "v_cvt_pk_bf16_f32 v93, v238, v239\n"
    "v_mfma_f32_16x16x32_bf16 v[200:203], a[108:111], v[76:79], v[200:203]\n"
    "v_exp_f32 v248, v248\n"
    "v_mfma_f32_16x16x32_bf16 v[200:203], a[112:115], v[80:83], v[200:203]\n"
    "v_exp_f32 v249, v249\n"
    "v_mfma_f32_16x16x32_bf16 v[200:203], a[116:119], v[84:87], v[200:203]\n"
    "v_exp_f32 v250, v250\n"
    "v_mfma_f32_16x16x32_bf16 v[204:207], a[196:199], v[76:79], v[204:207]\n"
    "v_exp_f32 v251, v251\n"
    "v_mfma_f32_16x16x32_bf16 v[204:207], a[200:203], v[80:83], v[204:207]\n"
    "v_cvt_pk_bf16_f32 v138, v248, v249\n"
    "v_cvt_pk_bf16_f32 v139, v250, v251\n"
    "v_mfma_f32_16x16x32_bf16 v[248:251], a[120:123], v[72:75], v[228:231]\n"
    "v_cvt_pk_bf16_f32 v94, v240, v241\n"
    "v_exp_f32 v200, v200\n"
    "v_mfma_f32_16x16x32_bf16 v[248:251], a[124:127], v[76:79], v[248:251]\n"
    "v_exp_f32 v201, v201\n"
    "v_mfma_f32_16x16x32_bf16 v[248:251], a[128:131], v[80:83], v[248:251]\n"
    "v_exp_f32 v202, v202\n"
    "v_mfma_f32_16x16x32_bf16 v[248:251], a[132:135], v[84:87], v[248:251]\n"
    "v_exp_f32 v203, v203\n"
    "v_mfma_f32_16x16x32_bf16 v[204:207], a[204:207], v[84:87], v[204:207]\n"
    "v_cvt_pk_bf16_f32 v140, v200, v201\n"
    "v_cvt_pk_bf16_f32 v141, v202, v203\n"
    "v_mfma_f32_16x16x32_bf16 v[208:211], a[208:211], v[72:75], v[228:231]\n"
    "v_cvt_pk_bf16_f32 v95, v242, v243\n"
    "v_lshlrev_b32 v236, 16, v96\n"
    "v_mfma_f32_16x16x32_bf16 v[208:211], a[212:215], v[76:79], v[208:211]\n"
    "v_exp_f32 v248, v248\n"
    "v_mfma_f32_16x16x32_bf16 v[208:211], a[216:219], v[80:83], v[208:211]\n"
    "v_exp_f32 v249, v249\n"
    "v_mfma_f32_16x16x32_bf16 v[208:211], a[220:223], v[84:87], v[208:211]\n"
    "s_waitcnt vmcnt(8)\n"
    "v_exp_f32 v250, v250\n"
    "v_mfma_f32_16x16x32_bf16 v[232:235], a[144:147], v[88:91], 0\n"
    "s_barrier\n"
    "v_exp_f32 v251, v251\n"
    "v_mfma_f32_16x16x32_bf16 v[232:235], a[148:151], v[92:95], v[232:235]\n"
    "s_add_u32 m0, s62, 16384\n"
    "v_cvt_pk_bf16_f32 v142, v248, v249\n"
    "v_cvt_pk_bf16_f32 v143, v250, v251\n"
    "v_exp_f32 v204, v204\n"
    "v_exp_f32 v205, v205\n"
    "v_exp_f32 v206, v206\n"
    "v_exp_f32 v207, v207\n"
    "v_cvt_pk_bf16_f32 v184, v204, v205\n"
    "v_cvt_pk_bf16_f32 v185, v206, v207\n"
    "v_mfma_f32_16x16x32_bf16 v[204:207], a[168:171], v[72:75], v[228:231]\n"
    "s_add_u32 s63, s59, 32768\n"
    "v_exp_f32 v208, v208\n"
    "v_mfma_f32_16x16x32_bf16 v[204:207], a[172:175], v[76:79], v[204:207]\n"
    "buffer_load_dwordx4 v5, s[44:47], s63 offen nt lds\n"
    "v_exp_f32 v209, v209\n"
    "v_mfma_f32_16x16x32_bf16 v[204:207], a[176:179], v[80:83], v[204:207]\n"
    "ds_read_b64_tr_b16 a[224:225], v1 offset:0\n"
    "v_exp_f32 v210, v210\n"
    "v_mfma_f32_16x16x32_bf16 v[204:207], a[180:183], v[84:87], v[204:207]\n"
    "ds_read_b64_tr_b16 a[226:227], v1 offset:4096\n"
    "v_exp_f32 v211, v211\n"
    "v_cvt_pk_bf16_f32 v186, v208, v209\n"
    "v_cvt_pk_bf16_f32 v187, v210, v211\n"
    "v_and_b32 v237, s74, v96\n"
    "v_pk_mul_f32 v[236:237], v[236:237], s[72:73]\n"
    "v_lshlrev_b32 v238, 16, v97\n"
    "v_and_b32 v239, s74, v97\n"
    "v_pk_mul_f32 v[238:239], v[238:239], s[72:73]\n"
    "v_exp_f32 v204, v204\n"
    "v_exp_f32 v205, v205\n"
    "v_exp_f32 v206, v206\n"
    "v_exp_f32 v207, v207\n"
    "v_cvt_pk_bf16_f32 v188, v204, v205\n"
    "v_cvt_pk_bf16_f32 v189, v206, v207\n"
    "v_lshlrev_b32 v240, 16, v98\n"
    "v_and_b32 v241, s74, v98\n"
    "v_pk_mul_f32 v[240:241], v[240:241], s[72:73]\n"
    "v_lshlrev_b32 v242, 16, v99\n"
    "v_and_b32 v243, s74, v99\n"
    "v_pk_mul_f32 v[242:243], v[242:243], s[72:73]\n"
    "v_cvt_pk_bf16_f32 v96, v236, v237\n"
    "v_cvt_pk_bf16_f32 v97, v238, v239\n"
    "v_cvt_pk_bf16_f32 v98, v240, v241\n"
    "v_cvt_pk_bf16_f32 v99, v242, v243\n"
    "s_waitcnt lgkmcnt(1)\n"
    "v_lshlrev_b32 v236, 16, v100\n"
    "v_mfma_f32_16x16x32_bf16 v[232:235], a[152:155], v[96:99], v[232:235]\n"
    "buffer_load_dwordx4 v6, s[44:47], s63 offen offset:1024 nt lds\n"
    "v_and_b32 v237, s74, v100\n"
    "s_waitcnt lgkmcnt(0)\n"
    "v_mfma_f32_16x16x32_bf16 a[0:3], v[104:107], a[224:227], 0\n"
    "ds_read_b64_tr_b16 a[228:229], v1 offset:8192\n"
    "v_lshlrev_b32 v238, 16, v101\n"
    "v_mfma_f32_16x16x32_bf16 a[4:7], v[112:115], a[224:227], 0\n"
    "ds_read_b64_tr_b16 a[230:231], v1 offset:12288\n"
    "v_and_b32 v239, s74, v101\n"
    "v_mfma_f32_16x16x32_bf16 a[8:11], v[120:123], a[224:227], 0\n"
    "ds_read_b64_tr_b16 a[232:233], v2 offset:0\n"
    "v_lshlrev_b32 v240, 16, v102\n"
    "v_mfma_f32_16x16x32_bf16 a[12:15], v[128:131], a[224:227], 0\n"
    "ds_read_b64_tr_b16 a[234:235], v2 offset:4096\n"
    "v_and_b32 v241, s74, v102\n"
    "v_lshlrev_b32 v242, 16, v103\n"
    "v_and_b32 v243, s74, v103\n"
    "buffer_load_dwordx4 v7, s[44:47], s63 offen offset:2048 nt lds\n"
    "v_pk_mul_f32 v[236:237], v[236:237], s[72:73]\n"
    "v_pk_mul_f32 v[238:239], v[238:239], s[72:73]\n"
    "v_pk_mul_f32 v[240:241], v[240:241], s[72:73]\n"
    "v_pk_mul_f32 v[242:243], v[242:243], s[72:73]\n"
    "v_cvt_pk_bf16_f32 v100, v236, v237\n"
    "v_mfma_f32_16x16x32_bf16 a[16:19], v[136:139], a[224:227], 0\n"
    "ds_read_b64_tr_b16 a[236:237], v2 offset:8192\n"
    "v_cvt_pk_bf16_f32 v101, v238, v239\n"
    "v_cvt_pk_bf16_f32 v102, v240, v241\n"
    "v_cvt_pk_bf16_f32 v103, v242, v243\n"
    "v_mov_b32 v236, 0\n"
    "v_mov_b32 v237, 0\n"
    "v_mfma_f32_16x16x32_bf16 v[232:235], a[156:159], v[100:103], v[232:235]\n"
    "ds_read_b64_tr_b16 a[238:239], v2 offset:12288\n"
    "v_mov_b32 v240, 0\n"
    "v_smfmac_f32_16x16x64_bf16 v[236:239], a[240:243], v[128:135], v253\n"
    "buffer_load_dwordx4 v255, s[44:47], s63 offen offset:3072 nt lds\n"
    "v_mov_b32 v241, 0\n"
    "s_waitcnt lgkmcnt(4)\n"
    "v_mfma_f32_16x16x32_bf16 a[0:3], v[108:111], a[228:231], a[0:3]\n"
    "s_add_u32 m0, s62, 32768\n"
    "v_sub_f32 v232, s83, v232\n"
    "v_sub_f32 v233, s83, v233\n"
    "v_mfma_f32_16x16x32_bf16 a[4:7], v[116:119], a[228:231], a[4:7]\n"
    "s_add_u32 s63, s59, 49152\n"
    "v_sub_f32 v234, s83, v234\n"
    "v_sub_f32 v235, s83, v235\n"
    "v_mfma_f32_16x16x32_bf16 a[8:11], v[124:127], a[228:231], a[8:11]\n"
    "v_mfma_f32_16x16x32_bf16 a[12:15], v[132:135], a[228:231], a[12:15]\n"
    "buffer_load_dwordx4 v5, s[40:43], s63 offen nt lds\n"
    "v_mfma_f32_16x16x32_bf16 v[200:203], a[72:75], v[88:91], v[232:235]\n"
    "v_mfma_f32_16x16x32_bf16 v[200:203], a[76:79], v[92:95], v[200:203]\n"
    "v_mfma_f32_16x16x32_bf16 v[200:203], a[80:83], v[96:99], v[200:203]\n"
    "v_mfma_f32_16x16x32_bf16 v[200:203], a[84:87], v[100:103], v[200:203]\n"
    "v_mfma_f32_16x16x32_bf16 v[248:251], a[88:91], v[88:91], v[232:235]\n"
    "buffer_load_dwordx4 v6, s[40:43], s63 offen offset:1024 nt lds\n"
    "v_mfma_f32_16x16x32_bf16 v[248:251], a[92:95], v[92:95], v[248:251]\n"
    "v_mfma_f32_16x16x32_bf16 v[248:251], a[96:99], v[96:99], v[248:251]\n"
    "v_mfma_f32_16x16x32_bf16 v[248:251], a[100:103], v[100:103], v[248:251]\n"
    "v_mfma_f32_16x16x32_bf16 v[208:211], a[192:195], v[88:91], v[232:235]\n"
    "buffer_load_dwordx4 v7, s[40:43], s63 offen offset:2048 nt lds\n"
    "v_mfma_f32_16x16x32_bf16 v[208:211], a[196:199], v[92:95], v[208:211]\n"
    "v_exp_f32 v200, v200\n"
    "v_mfma_f32_16x16x32_bf16 v[208:211], a[200:203], v[96:99], v[208:211]\n"
    "v_exp_f32 v201, v201\n"
    "v_mfma_f32_16x16x32_bf16 v[208:211], a[204:207], v[100:103], v[208:211]\n"
    "v_exp_f32 v202, v202\n"
    "v_mfma_f32_16x16x32_bf16 v[204:207], a[208:211], v[88:91], v[232:235]\n"
    "v_exp_f32 v203, v203\n"
    "v_mfma_f32_16x16x32_bf16 v[204:207], a[212:215], v[92:95], v[204:207]\n"
    "buffer_load_dwordx4 v255, s[40:43], s63 offen offset:3072 nt lds\n"
    "v_cvt_pk_bf16_f32 v144, v200, v201\n"
    "v_mfma_f32_16x16x32_bf16 v[204:207], a[216:219], v[96:99], v[204:207]\n"
    "v_cvt_pk_bf16_f32 v145, v202, v203\n"
    "v_exp_f32 v248, v248\n"
    "v_mfma_f32_16x16x32_bf16 v[200:203], a[104:107], v[88:91], v[232:235]\n"
    "v_exp_f32 v249, v249\n"
    "v_mfma_f32_16x16x32_bf16 v[200:203], a[108:111], v[92:95], v[200:203]\n"
    "v_exp_f32 v250, v250\n"
    "v_mfma_f32_16x16x32_bf16 v[200:203], a[112:115], v[96:99], v[200:203]\n"
    "v_exp_f32 v251, v251\n"
    "v_mfma_f32_16x16x32_bf16 v[200:203], a[116:119], v[100:103], v[200:203]\n"
    "v_cvt_pk_bf16_f32 v146, v248, v249\n"
    "v_cvt_pk_bf16_f32 v147, v250, v251\n"
    "v_mfma_f32_16x16x32_bf16 v[248:251], a[120:123], v[88:91], v[232:235]\n"
    "v_exp_f32 v208, v208\n"
    "v_mfma_f32_16x16x32_bf16 v[248:251], a[124:127], v[92:95], v[248:251]\n"
    "v_exp_f32 v209, v209\n"
    "v_mfma_f32_16x16x32_bf16 v[248:251], a[128:131], v[96:99], v[248:251]\n"
    "v_exp_f32 v210, v210\n"
    "v_mfma_f32_16x16x32_bf16 v[248:251], a[132:135], v[100:103], v[248:251]\n"
    "v_exp_f32 v200, v200\n"
    "v_mfma_f32_16x16x32_bf16 v[204:207], a[220:223], v[100:103], v[204:207]\n"
    "v_exp_f32 v201, v201\n"
    "v_mfma_f32_16x16x32_bf16 a[16:19], v[140:143], a[228:231], a[16:19]\n"
    "v_exp_f32 v202, v202\n"
    "v_mfma_f32_16x16x32_bf16 a[20:23], v[144:147], a[224:227], 0\n"
    "ds_read_b64_tr_b16 a[224:225], v1 offset:256\n"
    "v_exp_f32 v203, v203\n"
    "s_waitcnt lgkmcnt(1)\n"
    "v_mfma_f32_16x16x32_bf16 a[24:27], v[104:107], a[232:235], 0\n"
    "ds_read_b64_tr_b16 a[226:227], v1 offset:4352\n"
    "v_cvt_pk_bf16_f32 v148, v200, v201\n"
    "v_mfma_f32_16x16x32_bf16 a[24:27], v[108:111], a[236:239], a[24:27]\n"
    "v_cvt_pk_bf16_f32 v149, v202, v203\n"
    "v_exp_f32 v248, v248\n"
    "v_mfma_f32_16x16x32_bf16 a[28:31], v[112:115], a[232:235], 0\n"
    "v_exp_f32 v249, v249\n"
    "v_mfma_f32_16x16x32_bf16 a[28:31], v[116:119], a[236:239], a[28:31]\n"
    "v_exp_f32 v250, v250\n"
    "v_mfma_f32_16x16x32_bf16 a[32:35], v[120:123], a[232:235], 0\n"
    "v_exp_f32 v251, v251\n"
    "v_mfma_f32_16x16x32_bf16 a[32:35], v[124:127], a[236:239], a[32:35]\n"
    "v_cvt_pk_bf16_f32 v150, v248, v249\n"
    "v_cvt_pk_bf16_f32 v151, v250, v251\n"
    "v_mfma_f32_16x16x32_bf16 a[36:39], v[128:131], a[232:235], 0\n"
    "v_exp_f32 v211, v211\n"
    "v_mfma_f32_16x16x32_bf16 a[20:23], v[148:151], a[228:231], a[20:23]\n"
    "ds_read_b64_tr_b16 a[228:229], v1 offset:8448\n"
    "v_cvt_pk_bf16_f32 v192, v208, v209\n"
    "v_mfma_f32_16x16x32_bf16 a[36:39], v[132:135], a[236:239], a[36:39]\n"
    "ds_read_b64_tr_b16 a[230:231], v1 offset:12544\n"
    "v_cvt_pk_bf16_f32 v193, v210, v211\n"
    "v_mfma_f32_16x16x32_bf16 v[208:211], a[168:171], v[88:91], v[232:235]\n"
    "v_exp_f32 v204, v204\n"
    "v_mfma_f32_16x16x32_bf16 v[208:211], a[172:175], v[92:95], v[208:211]\n"
    "v_exp_f32 v205, v205\n"
    "v_mfma_f32_16x16x32_bf16 v[208:211], a[176:179], v[96:99], v[208:211]\n"
    "v_exp_f32 v206, v206\n"
    "v_mfma_f32_16x16x32_bf16 v[208:211], a[180:183], v[100:103], v[208:211]\n"
    "v_exp_f32 v207, v207\n"
    "v_mfma_f32_16x16x32_bf16 a[40:43], v[136:139], a[232:235], 0\n"
    "v_cvt_pk_bf16_f32 v194, v204, v205\n"
    "v_cvt_pk_bf16_f32 v195, v206, v207\n"
    "v_mfma_f32_16x16x32_bf16 v[204:207], a[184:187], v[72:75], v[228:231]\n"
    "v_mfma_f32_16x16x32_bf16 v[204:207], a[188:191], v[76:79], v[204:207]\n"
    "v_mfma_f32_16x16x32_bf16 v[204:207], a[160:163], v[80:83], v[204:207]\n"
    "v_mfma_f32_16x16x32_bf16 v[204:207], a[164:167], v[84:87], v[204:207]\n"
    "v_exp_f32 v208, v208\n"
    "v_mfma_f32_16x16x32_bf16 a[40:43], v[140:143], a[236:239], a[40:43]\n"
    "v_exp_f32 v209, v209\n"
    "v_mfma_f32_16x16x32_bf16 a[44:47], v[144:147], a[232:235], 0\n"
    "ds_read_b64_tr_b16 a[232:233], v2 offset:256\n"
    "v_exp_f32 v210, v210\n"
    "v_mfma_f32_16x16x32_bf16 a[44:47], v[148:151], a[236:239], a[44:47]\n"
    "ds_read_b64_tr_b16 a[234:235], v2 offset:4352\n"
    "v_exp_f32 v211, v211\n"
    "s_waitcnt lgkmcnt(2)\n"
    "v_mfma_f32_16x16x32_bf16 a[48:51], v[104:107], a[224:227], 0\n"
    "ds_read_b64_tr_b16 a[236:237], v2 offset:8448\n"
    "v_cvt_pk_bf16_f32 v196, v208, v209\n"
    "v_mfma_f32_16x16x32_bf16 a[48:51], v[108:111], a[228:231], a[48:51]\n"
    "ds_read_b64_tr_b16 a[238:239], v2 offset:12544\n"
    "v_cvt_pk_bf16_f32 v197, v210, v211\n"
    "v_mfma_f32_16x16x32_bf16 v[208:211], a[184:187], v[88:91], v[232:235]\n"
    "v_exp_f32 v204, v204\n"
    "v_mfma_f32_16x16x32_bf16 v[208:211], a[188:191], v[92:95], v[208:211]\n"
    "v_exp_f32 v205, v205\n"
    "v_mfma_f32_16x16x32_bf16 v[208:211], a[160:163], v[96:99], v[208:211]\n"
    "v_exp_f32 v206, v206\n"
    "v_mfma_f32_16x16x32_bf16 v[208:211], a[164:167], v[100:103], v[208:211]\n"
    "v_cvt_pk_bf16_f32 v190, v204, v205\n"
    "v_mfma_f32_16x16x32_bf16 a[52:55], v[112:115], a[224:227], 0\n"
    "v_mfma_f32_16x16x32_bf16 a[52:55], v[116:119], a[228:231], a[52:55]\n"
    "v_mfma_f32_16x16x32_bf16 a[56:59], v[120:123], a[224:227], 0\n"
    "v_mfma_f32_16x16x32_bf16 a[56:59], v[124:127], a[228:231], a[56:59]\n"
    "v_mfma_f32_16x16x32_bf16 a[60:63], v[128:131], a[224:227], 0\n"
    "v_mfma_f32_16x16x32_bf16 a[60:63], v[132:135], a[228:231], a[60:63]\n"
    "v_mfma_f32_16x16x32_bf16 a[64:67], v[136:139], a[224:227], 0\n"
    "v_mfma_f32_16x16x32_bf16 a[64:67], v[140:143], a[228:231], a[64:67]\n"
    "v_mfma_f32_16x16x32_bf16 a[68:71], v[144:147], a[224:227], 0\n"
    "ds_read_b64_tr_b16 a[224:225], v1 offset:512\n"
    "ds_read_b64_tr_b16 a[226:227], v1 offset:4608\n"
    "v_mfma_f32_16x16x32_bf16 a[68:71], v[148:151], a[228:231], a[68:71]\n"
    "ds_read_b64_tr_b16 a[228:229], v1 offset:8704\n"
    "ds_read_b64_tr_b16 a[230:231], v1 offset:12800\n"
    "s_waitcnt lgkmcnt(4)\n"
    "v_mfma_f32_16x16x32_bf16 a[72:75], v[104:107], a[232:235], 0\n"
    "v_mfma_f32_16x16x32_bf16 a[72:75], v[108:111], a[236:239], a[72:75]\n"
    "v_mfma_f32_16x16x32_bf16 a[76:79], v[112:115], a[232:235], 0\n"
    "v_mfma_f32_16x16x32_bf16 a[76:79], v[116:119], a[236:239], a[76:79]\n"
    "v_mfma_f32_16x16x32_bf16 a[80:83], v[120:123], a[232:235], 0\n"
    "v_mfma_f32_16x16x32_bf16 a[80:83], v[124:127], a[236:239], a[80:83]\n"
    "v_mfma_f32_16x16x32_bf16 a[84:87], v[128:131], a[232:235], 0\n"
    "v_mfma_f32_16x16x32_bf16 a[84:87], v[132:135], a[236:239], a[84:87]\n"
    "v_mfma_f32_16x16x32_bf16 a[88:91], v[136:139], a[232:235], 0\n"
    "v_mfma_f32_16x16x32_bf16 a[88:91], v[140:143], a[236:239], a[88:91]\n"
    "v_mfma_f32_16x16x32_bf16 a[92:95], v[144:147], a[232:235], 0\n"
    "ds_read_b64_tr_b16 a[232:233], v2 offset:512\n"
    "ds_read_b64_tr_b16 a[234:235], v2 offset:4608\n"
    "v_mfma_f32_16x16x32_bf16 a[92:95], v[148:151], a[236:239], a[92:95]\n"
    "ds_read_b64_tr_b16 a[236:237], v2 offset:8704\n"
    "ds_read_b64_tr_b16 a[238:239], v2 offset:12800\n"
    "s_waitcnt lgkmcnt(4)\n"
    "v_mfma_f32_16x16x32_bf16 a[96:99], v[104:107], a[224:227], 0\n"
    "v_mfma_f32_16x16x32_bf16 a[96:99], v[108:111], a[228:231], a[96:99]\n"
    "v_mfma_f32_16x16x32_bf16 a[100:103], v[112:115], a[224:227], 0\n"
    "v_mfma_f32_16x16x32_bf16 a[100:103], v[116:119], a[228:231], a[100:103]\n"
    "v_mfma_f32_16x16x32_bf16 a[104:107], v[120:123], a[224:227], 0\n"
    "v_mfma_f32_16x16x32_bf16 a[104:107], v[124:127], a[228:231], a[104:107]\n"
    "v_mfma_f32_16x16x32_bf16 a[108:111], v[128:131], a[224:227], 0\n"
    "v_mfma_f32_16x16x32_bf16 a[108:111], v[132:135], a[228:231], a[108:111]\n"
    "v_mfma_f32_16x16x32_bf16 a[112:115], v[136:139], a[224:227], 0\n"
    "v_mfma_f32_16x16x32_bf16 a[112:115], v[140:143], a[228:231], a[112:115]\n"
    "v_mfma_f32_16x16x32_bf16 a[116:119], v[144:147], a[224:227], 0\n"
    "ds_read_b64_tr_b16 a[224:225], v1 offset:768\n"
    "ds_read_b64_tr_b16 a[226:227], v1 offset:4864\n"
    "v_mfma_f32_16x16x32_bf16 a[116:119], v[148:151], a[228:231], a[116:119]\n"
    "ds_read_b64_tr_b16 a[228:229], v1 offset:8960\n"
    "ds_read_b64_tr_b16 a[230:231], v1 offset:13056\n"
    "v_smfmac_f32_16x16x64_bf16 v[240:243], a[240:243], v[136:143], v253\n"
    "v_add_u32 v1, 147456, v4\n"
    "s_waitcnt lgkmcnt(4)\n"
    "v_mfma_f32_16x16x32_bf16 a[120:123], v[104:107], a[232:235], 0\n"
    "v_mfma_f32_16x16x32_bf16 a[120:123], v[108:111], a[236:239], a[120:123]\n"
    "v_mfma_f32_16x16x32_bf16 a[124:127], v[112:115], a[232:235], 0\n"
    "v_mfma_f32_16x16x32_bf16 a[124:127], v[116:119], a[236:239], a[124:127]\n"
    "v_mfma_f32_16x16x32_bf16 a[128:131], v[120:123], a[232:235], 0\n"
    "v_mfma_f32_16x16x32_bf16 a[128:131], v[124:127], a[236:239], a[128:131]\n"
    "v_mfma_f32_16x16x32_bf16 a[132:135], v[128:131], a[232:235], 0\n"
    "v_mfma_f32_16x16x32_bf16 a[132:135], v[132:135], a[236:239], a[132:135]\n"
    "v_mfma_f32_16x16x32_bf16 a[136:139], v[136:139], a[232:235], 0\n"
    "v_mfma_f32_16x16x32_bf16 a[136:139], v[140:143], a[236:239], a[136:139]\n"
    "v_mfma_f32_16x16x32_bf16 a[140:143], v[144:147], a[232:235], 0\n"
    "ds_read_b64_tr_b16 a[232:233], v2 offset:768\n"
    "ds_read_b64_tr_b16 a[234:235], v2 offset:4864\n"
    "v_mfma_f32_16x16x32_bf16 a[140:143], v[148:151], a[236:239], a[140:143]\n"
    "ds_read_b64_tr_b16 a[236:237], v2 offset:8960\n"
    "ds_read_b64_tr_b16 a[238:239], v2 offset:13056\n"
    "v_smfmac_f32_16x16x64_bf16 v[244:247], a[240:243], v[144:151], v253\n"
    "v_xor_b32 v2, 32, v1\n"
    "s_waitcnt lgkmcnt(4)\n"
    "v_mfma_f32_16x16x32_bf16 a[144:147], v[104:107], a[224:227], 0\n"
    "v_mfma_f32_16x16x32_bf16 a[144:147], v[108:111], a[228:231], a[144:147]\n"
    "v_mfma_f32_16x16x32_bf16 a[148:151], v[112:115], a[224:227], 0\n"
    "v_mfma_f32_16x16x32_bf16 a[148:151], v[116:119], a[228:231], a[148:151]\n"
    "v_mfma_f32_16x16x32_bf16 a[152:155], v[120:123], a[224:227], 0\n"
    "v_mfma_f32_16x16x32_bf16 a[152:155], v[124:127], a[228:231], a[152:155]\n"
    "v_mfma_f32_16x16x32_bf16 a[156:159], v[128:131], a[224:227], 0\n"
    "v_mfma_f32_16x16x32_bf16 a[156:159], v[132:135], a[228:231], a[156:159]\n"
    "s_waitcnt lgkmcnt(0)\n"
    "s_waitcnt vmcnt(8)\n"
    "s_barrier\n"
    "v_mfma_f32_16x16x32_bf16 a[160:163], v[136:139], a[224:227], 0\n"
    "ds_read_b128 a[192:195], v0 offset:0\n"
    "ds_read_b128 a[196:199], v0 offset:256\n"
    "v_mfma_f32_16x16x32_bf16 a[160:163], v[140:143], a[228:231], a[160:163]\n"
    "ds_read_b128 a[200:203], v0 offset:512\n"
    "ds_read_b128 a[204:207], v0 offset:768\n"
    "v_mfma_f32_16x16x32_bf16 a[164:167], v[144:147], a[224:227], 0\n"
    "ds_read_b64_tr_b16 a[224:225], v1 offset:0\n"
    "ds_read_b64_tr_b16 a[226:227], v1 offset:4096\n"
    "v_mfma_f32_16x16x32_bf16 a[164:167], v[148:151], a[228:231], a[164:167]\n"
    "ds_read_b64_tr_b16 a[228:229], v1 offset:8192\n"
    "ds_read_b64_tr_b16 a[230:231], v1 offset:12288\n"
    "v_mfma_f32_16x16x32_bf16 a[168:171], v[104:107], a[232:235], 0\n"
    "s_mov_b32 s66, 16384\n"
    "s_mov_b32 s69, 7\n"
    "v_mfma_f32_16x16x32_bf16 a[168:171], v[108:111], a[236:239], a[168:171]\n"
    "v_mfma_f32_16x16x32_bf16 a[172:175], v[112:115], a[232:235], 0\n"
    "v_mfma_f32_16x16x32_bf16 a[172:175], v[116:119], a[236:239], a[172:175]\n"
    "v_mfma_f32_16x16x32_bf16 a[176:179], v[120:123], a[232:235], 0\n"
    "v_mfma_f32_16x16x32_bf16 a[176:179], v[124:127], a[236:239], a[176:179]\n"
    "v_mfma_f32_16x16x32_bf16 a[180:183], v[128:131], a[232:235], 0\n"
    "v_mfma_f32_16x16x32_bf16 a[180:183], v[132:135], a[236:239], a[180:183]\n"
    "v_mfma_f32_16x16x32_bf16 a[184:187], v[136:139], a[232:235], 0\n"
    "v_mfma_f32_16x16x32_bf16 a[184:187], v[140:143], a[236:239], a[184:187]\n"
    "v_mfma_f32_16x16x32_bf16 a[188:191], v[144:147], a[232:235], 0\n"
    "v_mfma_f32_16x16x32_bf16 a[188:191], v[148:151], a[236:239], a[188:191]\n"
    "s_waitcnt lgkmcnt(0)\n"
    "s_nop 7\n"
    "; ---- first head, tiles 2..15: QK(t) with PV(t-1)\n"
    ".p2align 6\n"
    "Lloop_%=:\n"
    "v_mfma_f32_16x16x32_bf16 a[0:3], v[152:155], a[224:227], a[0:3]\n"
    "ds_read_b128 a[208:211], v0 offset:4096\n"
    "s_bitcmp1_b32 s58, 0\n"
    "v_mfma_f32_16x16x32_bf16 a[0:3], v[156:159], a[228:231], a[0:3]\n"
    "s_cselect_b64 s[48:49], s[44:45], s[40:41]\n"
    "v_exp_f32 v207, v207\n"
    "v_mfma_f32_16x16x32_bf16 a[4:7], v[164:167], a[228:231], a[4:7]\n"
    "ds_read_b64_tr_b16 a[232:233], v2 offset:0\n"
    "v_cvt_pk_bf16_f32 v191, v206, v207\n"
    "v_mfma_f32_16x16x32_bf16 a[4:7], v[160:163], a[224:227], a[4:7]\n"
    "s_lshl_b32 s75, s58, 13\n"
    "v_exp_f32 v208, v208\n"
    "v_mfma_f32_16x16x32_bf16 v[200:203], a[192:195], v[8:11], v[212:215]\n"
    "s_and_b32 s75, s75, 0x3c000\n"
    "s_add_u32 s63, s59, s75\n"
    "s_lshl_b32 s76, s58, 12\n"
    "v_mfma_f32_16x16x32_bf16 v[200:203], a[196:199], v[12:15], v[200:203]\n"
    "s_add_u32 s76, s60, s76\n"
    "v_exp_f32 v209, v209\n"
    "v_mfma_f32_16x16x32_bf16 v[200:203], a[200:203], v[16:19], v[200:203]\n"
    "ds_read_b64_tr_b16 a[234:235], v2 offset:4096\n"
    "v_cvt_pk_bf16_f32 v198, v208, v209\n"
    "v_mfma_f32_16x16x32_bf16 v[200:203], a[204:207], v[20:23], v[200:203]\n"
    "s_cmp_lt_i32 s58, 0\n"
    "v_exp_f32 v210, v210\n"
    "v_mfma_f32_16x16x32_bf16 v[204:207], a[204:207], v[36:39], v[216:219]\n"
    "ds_read_b128 a[212:215], v0 offset:4352\n"
    "s_cselect_b64 s[48:49], s[36:37], s[48:49]\n"
    "v_mfma_f32_16x16x32_bf16 v[204:207], a[200:203], v[32:35], v[204:207]\n"
    "s_cselect_b32 s50, s38, s42\n"
    "v_exp_f32 v211, v211\n"
    "v_mfma_f32_16x16x32_bf16 v[204:207], a[196:199], v[28:31], v[204:207]\n"
    "ds_read_b64_tr_b16 a[236:237], v2 offset:8192\n"
    "v_cvt_pk_bf16_f32 v199, v210, v211\n"
    "v_mfma_f32_16x16x32_bf16 v[204:207], a[192:195], v[24:27], v[204:207]\n"
    "s_cselect_b32 s63, s76, s63\n"
    "v_exp_f32 v200, v200\n"
    "v_mfma_f32_16x16x32_bf16 a[8:11], v[168:171], a[224:227], a[8:11]\n"
    "s_add_u32 s65, s61, s62\n"
    "s_add_u32 s61, s61, 16384\n"
    "s_cmp_eq_u32 s61, 163840\n"
    "v_mfma_f32_16x16x32_bf16 a[8:11], v[172:175], a[228:231], a[8:11]\n"
    "s_cselect_b32 s61, s79, s61\n"
    "v_exp_f32 v201, v201\n"
    "v_mfma_f32_16x16x32_bf16 a[12:15], v[180:183], a[228:231], a[12:15]\n"
    "ds_read_b64_tr_b16 a[238:239], v2 offset:12288\n"
    "v_cvt_pk_bf16_f32 v104, v200, v201\n"
    "v_mfma_f32_16x16x32_bf16 a[12:15], v[176:179], a[224:227], a[12:15]\n"
    "s_add_i32 s58, s58, 1\n"
    "v_exp_f32 v202, v202\n"
    "v_mfma_f32_16x16x32_bf16 a[16:19], v[184:187], a[224:227], a[16:19]\n"
    "ds_read_b128 a[216:219], v0 offset:4608\n"
    "s_cmp_eq_u32 s58, 32\n"
    "v_mfma_f32_16x16x32_bf16 a[16:19], v[188:191], a[228:231], a[16:19]\n"
    "s_cselect_b32 s58, -6, s58\n"
    "v_exp_f32 v203, v203\n"
    "v_mfma_f32_16x16x32_bf16 a[20:23], v[196:199], a[228:231], a[20:23]\n"
    "s_cselect_b32 s75, 67108864, 0\n"
    "v_cvt_pk_bf16_f32 v105, v202, v203\n"
    "s_cselect_b32 s76, 25165824, 0\n"
    "v_mfma_f32_16x16x32_bf16 a[20:23], v[192:195], a[224:227], a[20:23]\n"
    "s_add_u32 s59, s59, s75\n"
    "v_exp_f32 v204, v204\n"
    "v_mfma_f32_16x16x32_bf16 v[208:211], a[192:195], v[40:43], v[220:223]\n"
    "s_add_u32 s60, s60, s76\n"
    "s_mov_b32 m0, s65\n"
    "v_mfma_f32_16x16x32_bf16 v[208:211], a[196:199], v[44:47], v[208:211]\n"
    "v_exp_f32 v205, v205\n"
    "v_mfma_f32_16x16x32_bf16 v[208:211], a[200:203], v[48:51], v[208:211]\n"
    "v_cvt_pk_bf16_f32 v112, v204, v205\n"
    "v_mfma_f32_16x16x32_bf16 v[208:211], a[204:207], v[52:55], v[208:211]\n"
    "v_exp_f32 v206, v206\n"
    "v_mfma_f32_16x16x32_bf16 v[200:203], a[204:207], v[68:71], v[224:227]\n"
    "ds_read_b128 a[220:223], v0 offset:4864\n"
    "v_mfma_f32_16x16x32_bf16 v[200:203], a[200:203], v[64:67], v[200:203]\n"
    "v_exp_f32 v207, v207\n"
    "v_mfma_f32_16x16x32_bf16 v[200:203], a[196:199], v[60:63], v[200:203]\n"
    "ds_read_b64_tr_b16 a[224:225], v1 offset:256\n"
    "v_cvt_pk_bf16_f32 v113, v206, v207\n"
    "v_mfma_f32_16x16x32_bf16 v[200:203], a[192:195], v[56:59], v[200:203]\n"
    "s_waitcnt lgkmcnt(3)\n"
    "v_exp_f32 v208, v208\n"
    "v_mfma_f32_16x16x32_bf16 a[24:27], v[152:155], a[232:235], a[24:27]\n"
    "buffer_load_dwordx4 v5, s[48:51], s63 offen nt lds\n"
    "v_mfma_f32_16x16x32_bf16 a[24:27], v[156:159], a[236:239], a[24:27]\n"
    "v_exp_f32 v209, v209\n"
    "v_mfma_f32_16x16x32_bf16 a[28:31], v[164:167], a[236:239], a[28:31]\n"
    "ds_read_b64_tr_b16 a[226:227], v1 offset:4352\n"
    "v_cvt_pk_bf16_f32 v120, v208, v209\n"
    "v_mfma_f32_16x16x32_bf16 a[28:31], v[160:163], a[232:235], a[28:31]\n"
    "v_exp_f32 v210, v210\n"
    "v_smfmac_f32_16x16x64_bf16 a[244:247], a[240:243], v[152:159], v253\n"
    "v_mfma_f32_16x16x32_bf16 a[32:35], v[168:171], a[232:235], a[32:35]\n"
    "v_mfma_f32_16x16x32_bf16 a[32:35], v[172:175], a[236:239], a[32:35]\n"
    "v_exp_f32 v211, v211\n"
    "v_mfma_f32_16x16x32_bf16 a[36:39], v[180:183], a[236:239], a[36:39]\n"
    "ds_read_b64_tr_b16 a[228:229], v1 offset:8448\n"
    "v_cvt_pk_bf16_f32 v121, v210, v211\n"
    "v_mfma_f32_16x16x32_bf16 a[36:39], v[176:179], a[232:235], a[36:39]\n"
    "v_exp_f32 v200, v200\n"
    "v_mfma_f32_16x16x32_bf16 v[204:207], a[192:195], v[72:75], v[228:231]\n"
    "buffer_load_dwordx4 v6, s[48:51], s63 offen offset:1024 nt lds\n"
    "v_mfma_f32_16x16x32_bf16 v[204:207], a[196:199], v[76:79], v[204:207]\n"
    "v_exp_f32 v201, v201\n"
    "v_mfma_f32_16x16x32_bf16 v[204:207], a[200:203], v[80:83], v[204:207]\n"
    "ds_read_b64_tr_b16 a[230:231], v1 offset:12544\n"
    "v_cvt_pk_bf16_f32 v128, v200, v201\n"
    "v_mfma_f32_16x16x32_bf16 v[204:207], a[204:207], v[84:87], v[204:207]\n"
    "s_nop 0\n"
    "v_exp_f32 v202, v202\n"
    "v_mfma_f32_16x16x32_bf16 v[208:211], a[204:207], v[100:103], v[232:235]\n"
    "v_mfma_f32_16x16x32_bf16 v[208:211], a[200:203], v[96:99], v[208:211]\n"
    "v_exp_f32 v203, v203\n"
    "v_mfma_f32_16x16x32_bf16 v[208:211], a[196:199], v[92:95], v[208:211]\n"
    "v_cvt_pk_bf16_f32 v129, v202, v203\n"
    "v_mfma_f32_16x16x32_bf16 v[208:211], a[192:195], v[88:91], v[208:211]\n"
    "v_exp_f32 v204, v204\n"
    "v_mfma_f32_16x16x32_bf16 a[40:43], v[184:187], a[232:235], a[40:43]\n"
    "buffer_load_dwordx4 v7, s[48:51], s63 offen offset:2048 nt lds\n"
    "v_mfma_f32_16x16x32_bf16 a[40:43], v[188:191], a[236:239], a[40:43]\n"
    "v_exp_f32 v205, v205\n"
    "v_mfma_f32_16x16x32_bf16 a[44:47], v[196:199], a[236:239], a[44:47]\n"
    "s_waitcnt lgkmcnt(0)\n"
    "v_cvt_pk_bf16_f32 v136, v204, v205\n"
    "v_mfma_f32_16x16x32_bf16 a[44:47], v[192:195], a[232:235], a[44:47]\n"
    "s_waitcnt lgkmcnt(0)\n"
    "v_exp_f32 v206, v206\n"
    "v_mfma_f32_16x16x32_bf16 a[48:51], v[152:155], a[224:227], a[48:51]\n"
    "ds_read_b128 a[192:195], v0 offset:8192\n"
    "v_mfma_f32_16x16x32_bf16 a[48:51], v[156:159], a[228:231], a[48:51]\n"
    "v_exp_f32 v207, v207\n"
    "v_mfma_f32_16x16x32_bf16 a[52:55], v[164:167], a[228:231], a[52:55]\n"
    "ds_read_b64_tr_b16 a[232:233], v2 offset:256\n"
    "v_cvt_pk_bf16_f32 v137, v206, v207\n"
    "v_mfma_f32_16x16x32_bf16 a[52:55], v[160:163], a[224:227], a[52:55]\n"
    "v_exp_f32 v208, v208\n"
    "v_mfma_f32_16x16x32_bf16 v[200:203], a[208:211], v[8:11], v[212:215]\n"
    "buffer_load_dwordx4 v255, s[48:51], s63 offen offset:3072 nt lds\n"
    "v_mfma_f32_16x16x32_bf16 v[200:203], a[212:215], v[12:15], v[200:203]\n"
    "s_mov_b32 s68, s66\n"
    "v_exp_f32 v209, v209\n"
    "v_mfma_f32_16x16x32_bf16 v[200:203], a[216:219], v[16:19], v[200:203]\n"
    "ds_read_b64_tr_b16 a[234:235], v2 offset:4352\n"
    "v_cvt_pk_bf16_f32 v144, v208, v209\n"
    "v_mfma_f32_16x16x32_bf16 v[200:203], a[220:223], v[20:23], v[200:203]\n"
    "s_add_u32 s66, s66, 16384\n"
    "v_exp_f32 v210, v210\n"
    "v_mfma_f32_16x16x32_bf16 v[204:207], a[220:223], v[36:39], v[216:219]\n"
    "ds_read_b128 a[196:199], v0 offset:8448\n"
    "s_cmp_eq_u32 s66, 163840\n"
    "v_mfma_f32_16x16x32_bf16 v[204:207], a[216:219], v[32:35], v[204:207]\n"
    "s_cselect_b32 s66, s79, s66\n"
    "v_exp_f32 v211, v211\n"
    "v_mfma_f32_16x16x32_bf16 v[204:207], a[212:215], v[28:31], v[204:207]\n"
    "ds_read_b64_tr_b16 a[236:237], v2 offset:8448\n"
    "v_cvt_pk_bf16_f32 v145, v210, v211\n"
    "v_mfma_f32_16x16x32_bf16 v[204:207], a[208:211], v[24:27], v[204:207]\n"
    "s_mov_b32 s67, s66\n"
    "v_exp_f32 v200, v200\n"
    "v_mfma_f32_16x16x32_bf16 a[56:59], v[168:171], a[224:227], a[56:59]\n"
    "s_add_u32 s66, s66, 16384\n"
    "s_cmp_eq_u32 s66, 163840\n"
    "s_cselect_b32 s66, s79, s66\n"
    "v_mfma_f32_16x16x32_bf16 a[56:59], v[172:175], a[228:231], a[56:59]\n"
    "s_bitcmp1_b32 s58, 0\n"
    "v_exp_f32 v201, v201\n"
    "v_mfma_f32_16x16x32_bf16 a[60:63], v[180:183], a[228:231], a[60:63]\n"
    "ds_read_b64_tr_b16 a[238:239], v2 offset:12544\n"
    "v_cvt_pk_bf16_f32 v106, v200, v201\n"
    "v_mfma_f32_16x16x32_bf16 a[60:63], v[176:179], a[224:227], a[60:63]\n"
    "s_cselect_b64 s[48:49], s[44:45], s[40:41]\n"
    "v_exp_f32 v202, v202\n"
    "v_smfmac_f32_16x16x64_bf16 a[248:251], a[240:243], v[160:167], v253\n"
    "s_lshl_b32 s75, s58, 13\n"
    "s_and_b32 s75, s75, 0x3c000\n"
    "s_add_u32 s63, s59, s75\n"
    "v_mfma_f32_16x16x32_bf16 a[64:67], v[184:187], a[224:227], a[64:67]\n"
    "ds_read_b128 a[200:203], v0 offset:8704\n"
    "s_lshl_b32 s76, s58, 12\n"
    "v_mfma_f32_16x16x32_bf16 a[64:67], v[188:191], a[228:231], a[64:67]\n"
    "s_add_u32 s76, s60, s76\n"
    "v_exp_f32 v203, v203\n"
    "v_mfma_f32_16x16x32_bf16 a[68:71], v[196:199], a[228:231], a[68:71]\n"
    "s_cmp_lt_i32 s58, 0\n"
    "v_cvt_pk_bf16_f32 v107, v202, v203\n"
    "s_cselect_b64 s[48:49], s[36:37], s[48:49]\n"
    "v_mfma_f32_16x16x32_bf16 a[68:71], v[192:195], a[224:227], a[68:71]\n"
    "s_cselect_b32 s50, s38, s42\n"
    "v_exp_f32 v204, v204\n"
    "v_mfma_f32_16x16x32_bf16 v[208:211], a[208:211], v[40:43], v[220:223]\n"
    "s_cselect_b32 s63, s76, s63\n"
    "s_add_u32 s65, s61, s62\n"
    "s_add_u32 s61, s61, 16384\n"
    "v_mfma_f32_16x16x32_bf16 v[208:211], a[212:215], v[44:47], v[208:211]\n"
    "s_cmp_eq_u32 s61, 163840\n"
    "v_exp_f32 v205, v205\n"
    "v_mfma_f32_16x16x32_bf16 v[208:211], a[216:219], v[48:51], v[208:211]\n"
    "s_cselect_b32 s61, s79, s61\n"
    "v_cvt_pk_bf16_f32 v114, v204, v205\n"
    "s_add_i32 s58, s58, 1\n"
    "v_mfma_f32_16x16x32_bf16 v[208:211], a[220:223], v[52:55], v[208:211]\n"
    "s_cmp_eq_u32 s58, 32\n"
    "v_exp_f32 v206, v206\n"
    "v_mfma_f32_16x16x32_bf16 v[200:203], a[220:223], v[68:71], v[224:227]\n"
    "ds_read_b128 a[204:207], v0 offset:8960\n"
    "s_cselect_b32 s58, -6, s58\n"
    "v_mfma_f32_16x16x32_bf16 v[200:203], a[216:219], v[64:67], v[200:203]\n"
    "s_cselect_b32 s75, 67108864, 0\n"
    "v_exp_f32 v207, v207\n"
    "v_mfma_f32_16x16x32_bf16 v[200:203], a[212:215], v[60:63], v[200:203]\n"
    "ds_read_b64_tr_b16 a[224:225], v1 offset:512\n"
    "v_cvt_pk_bf16_f32 v115, v206, v207\n"
    "v_mfma_f32_16x16x32_bf16 v[200:203], a[208:211], v[56:59], v[200:203]\n"
    "s_waitcnt lgkmcnt(3)\n"
    "v_exp_f32 v208, v208\n"
    "v_mfma_f32_16x16x32_bf16 a[72:75], v[152:155], a[232:235], a[72:75]\n"
    "s_cselect_b32 s76, 25165824, 0\n"
    "s_add_u32 s59, s59, s75\n"
    "s_add_u32 s60, s60, s76\n"
    "v_mfma_f32_16x16x32_bf16 a[72:75], v[156:159], a[236:239], a[72:75]\n"
    "s_mov_b32 m0, s65\n"
    "v_exp_f32 v209, v209\n"
    "v_mfma_f32_16x16x32_bf16 a[76:79], v[164:167], a[236:239], a[76:79]\n"
    "ds_read_b64_tr_b16 a[226:227], v1 offset:4608\n"
    "v_cvt_pk_bf16_f32 v122, v208, v209\n"
    "v_mfma_f32_16x16x32_bf16 a[76:79], v[160:163], a[232:235], a[76:79]\n"
    "v_exp_f32 v210, v210\n"
    "v_mfma_f32_16x16x32_bf16 a[80:83], v[168:171], a[232:235], a[80:83]\n"
    "buffer_load_dwordx4 v5, s[48:51], s63 offen nt lds\n"
    "v_mfma_f32_16x16x32_bf16 a[80:83], v[172:175], a[236:239], a[80:83]\n"
    "v_exp_f32 v211, v211\n"
    "v_mfma_f32_16x16x32_bf16 a[84:87], v[180:183], a[236:239], a[84:87]\n"
    "ds_read_b64_tr_b16 a[228:229], v1 offset:8704\n"
    "v_cvt_pk_bf16_f32 v123, v210, v211\n"
    "v_mfma_f32_16x16x32_bf16 a[84:87], v[176:179], a[232:235], a[84:87]\n"
    "v_exp_f32 v200, v200\n"
    "v_mfma_f32_16x16x32_bf16 v[204:207], a[208:211], v[72:75], v[228:231]\n"
    "v_mfma_f32_16x16x32_bf16 v[204:207], a[212:215], v[76:79], v[204:207]\n"
    "v_exp_f32 v201, v201\n"
    "v_mfma_f32_16x16x32_bf16 v[204:207], a[216:219], v[80:83], v[204:207]\n"
    "ds_read_b64_tr_b16 a[230:231], v1 offset:12800\n"
    "v_cvt_pk_bf16_f32 v130, v200, v201\n"
    "v_mfma_f32_16x16x32_bf16 v[204:207], a[220:223], v[84:87], v[204:207]\n"
    "v_exp_f32 v202, v202\n"
    "v_mfma_f32_16x16x32_bf16 v[208:211], a[220:223], v[100:103], v[232:235]\n"
    "buffer_load_dwordx4 v6, s[48:51], s63 offen offset:1024 nt lds\n"
    "v_mfma_f32_16x16x32_bf16 v[208:211], a[216:219], v[96:99], v[208:211]\n"
    "v_exp_f32 v203, v203\n"
    "v_mfma_f32_16x16x32_bf16 v[208:211], a[212:215], v[92:95], v[208:211]\n"
    "v_cvt_pk_bf16_f32 v131, v202, v203\n"
    "v_mfma_f32_16x16x32_bf16 v[208:211], a[208:211], v[88:91], v[208:211]\n"
    "v_exp_f32 v204, v204\n"
    "v_mfma_f32_16x16x32_bf16 a[88:91], v[184:187], a[232:235], a[88:91]\n"
    "v_mfma_f32_16x16x32_bf16 a[88:91], v[188:191], a[236:239], a[88:91]\n"
    "v_exp_f32 v205, v205\n"
    "v_mfma_f32_16x16x32_bf16 a[92:95], v[196:199], a[236:239], a[92:95]\n"
    "v_cvt_pk_bf16_f32 v138, v204, v205\n"
    "v_mfma_f32_16x16x32_bf16 a[92:95], v[192:195], a[232:235], a[92:95]\n"
    "s_waitcnt lgkmcnt(0)\n"
    "v_exp_f32 v206, v206\n"
    "v_smfmac_f32_16x16x64_bf16 a[252:255], a[240:243], v[168:175], v253\n"
    "s_waitcnt lgkmcnt(0)\n"
    "v_mfma_f32_16x16x32_bf16 a[96:99], v[152:155], a[224:227], a[96:99]\n"
    "ds_read_b128 a[208:211], v0 offset:12288\n"
    "v_mfma_f32_16x16x32_bf16 a[96:99], v[156:159], a[228:231], a[96:99]\n"
    "v_exp_f32 v207, v207\n"
    "v_mfma_f32_16x16x32_bf16 a[100:103], v[164:167], a[228:231], a[100:103]\n"
    "ds_read_b64_tr_b16 a[232:233], v2 offset:512\n"
    "v_cvt_pk_bf16_f32 v139, v206, v207\n"
    "v_mfma_f32_16x16x32_bf16 a[100:103], v[160:163], a[224:227], a[100:103]\n"
    "v_exp_f32 v208, v208\n"
    "v_mfma_f32_16x16x32_bf16 v[200:203], a[192:195], v[8:11], v[212:215]\n"
    "buffer_load_dwordx4 v7, s[48:51], s63 offen offset:2048 nt lds\n"
    "v_mfma_f32_16x16x32_bf16 v[200:203], a[196:199], v[12:15], v[200:203]\n"
    "v_exp_f32 v209, v209\n"
    "v_mfma_f32_16x16x32_bf16 v[200:203], a[200:203], v[16:19], v[200:203]\n"
    "ds_read_b64_tr_b16 a[234:235], v2 offset:4608\n"
    "v_cvt_pk_bf16_f32 v146, v208, v209\n"
    "v_mfma_f32_16x16x32_bf16 v[200:203], a[204:207], v[20:23], v[200:203]\n"
    "v_exp_f32 v210, v210\n"
    "v_mfma_f32_16x16x32_bf16 v[204:207], a[204:207], v[36:39], v[216:219]\n"
    "ds_read_b128 a[212:215], v0 offset:12544\n"
    "v_mfma_f32_16x16x32_bf16 v[204:207], a[200:203], v[32:35], v[204:207]\n"
    "v_exp_f32 v211, v211\n"
    "v_mfma_f32_16x16x32_bf16 v[204:207], a[196:199], v[28:31], v[204:207]\n"
    "ds_read_b64_tr_b16 a[236:237], v2 offset:8704\n"
    "v_cvt_pk_bf16_f32 v147, v210, v211\n"
    "v_mfma_f32_16x16x32_bf16 v[204:207], a[192:195], v[24:27], v[204:207]\n"
    "v_exp_f32 v200, v200\n"
    "v_mfma_f32_16x16x32_bf16 a[104:107], v[168:171], a[224:227], a[104:107]\n"
    "buffer_load_dwordx4 v255, s[48:51], s63 offen offset:3072 nt lds\n"
    "v_mfma_f32_16x16x32_bf16 a[104:107], v[172:175], a[228:231], a[104:107]\n"
    "s_mov_b32 s86, s58\n"
    "v_exp_f32 v201, v201\n"
    "v_mfma_f32_16x16x32_bf16 a[108:111], v[180:183], a[228:231], a[108:111]\n"
    "ds_read_b64_tr_b16 a[238:239], v2 offset:12800\n"
    "v_cvt_pk_bf16_f32 v108, v200, v201\n"
    "v_mfma_f32_16x16x32_bf16 a[108:111], v[176:179], a[224:227], a[108:111]\n"
    "s_mov_b32 s87, s61\n"
    "v_exp_f32 v202, v202\n"
    "v_mfma_f32_16x16x32_bf16 a[112:115], v[184:187], a[224:227], a[112:115]\n"
    "ds_read_b128 a[216:219], v0 offset:12800\n"
    "s_mov_b32 s88, s59\n"
    "v_mfma_f32_16x16x32_bf16 a[112:115], v[188:191], a[228:231], a[112:115]\n"
    "s_mov_b32 s89, s60\n"
    "v_exp_f32 v203, v203\n"
    "v_mfma_f32_16x16x32_bf16 a[116:119], v[196:199], a[228:231], a[116:119]\n"
    "s_bitcmp1_b32 s58, 0\n"
    "v_cvt_pk_bf16_f32 v109, v202, v203\n"
    "s_cselect_b64 s[48:49], s[44:45], s[40:41]\n"
    "v_mfma_f32_16x16x32_bf16 a[116:119], v[192:195], a[224:227], a[116:119]\n"
    "s_lshl_b32 s75, s58, 13\n"
    "v_exp_f32 v204, v204\n"
    "v_mfma_f32_16x16x32_bf16 v[208:211], a[192:195], v[40:43], v[220:223]\n"
    "s_and_b32 s75, s75, 0x3c000\n"
    "s_add_u32 s63, s59, s75\n"
    "s_lshl_b32 s76, s58, 12\n"
    "v_mfma_f32_16x16x32_bf16 v[208:211], a[196:199], v[44:47], v[208:211]\n"
    "s_add_u32 s76, s60, s76\n"
    "v_exp_f32 v205, v205\n"
    "v_mfma_f32_16x16x32_bf16 v[208:211], a[200:203], v[48:51], v[208:211]\n"
    "s_cmp_lt_i32 s58, 0\n"
    "v_cvt_pk_bf16_f32 v116, v204, v205\n"
    "s_cselect_b64 s[48:49], s[36:37], s[48:49]\n"
    "v_mfma_f32_16x16x32_bf16 v[208:211], a[204:207], v[52:55], v[208:211]\n"
    "s_cselect_b32 s50, s38, s42\n"
    "v_exp_f32 v206, v206\n"
    "v_mfma_f32_16x16x32_bf16 v[200:203], a[204:207], v[68:71], v[224:227]\n"
    "ds_read_b128 a[220:223], v0 offset:13056\n"
    "s_cselect_b32 s63, s76, s63\n"
    "v_mfma_f32_16x16x32_bf16 v[200:203], a[200:203], v[64:67], v[200:203]\n"
    "s_add_u32 s65, s61, s62\n"
    "v_exp_f32 v207, v207\n"
    "v_mfma_f32_16x16x32_bf16 v[200:203], a[196:199], v[60:63], v[200:203]\n"
    "ds_read_b64_tr_b16 a[224:225], v1 offset:768\n"
    "v_cvt_pk_bf16_f32 v117, v206, v207\n"
    "v_mfma_f32_16x16x32_bf16 v[200:203], a[192:195], v[56:59], v[200:203]\n"
    "s_waitcnt lgkmcnt(3)\n"
    "v_exp_f32 v208, v208\n"
    "v_mfma_f32_16x16x32_bf16 a[120:123], v[152:155], a[232:235], a[120:123]\n"
    "s_add_u32 s61, s61, 16384\n"
    "s_cmp_eq_u32 s61, 163840\n"
    "s_cselect_b32 s61, s79, s61\n"
    "v_mfma_f32_16x16x32_bf16 a[120:123], v[156:159], a[236:239], a[120:123]\n"
    "s_add_i32 s58, s58, 1\n"
    "v_exp_f32 v209, v209\n"
    "v_mfma_f32_16x16x32_bf16 a[124:127], v[164:167], a[236:239], a[124:127]\n"
    "ds_read_b64_tr_b16 a[226:227], v1 offset:4864\n"
    "v_cvt_pk_bf16_f32 v124, v208, v209\n"
    "v_mfma_f32_16x16x32_bf16 a[124:127], v[160:163], a[232:235], a[124:127]\n"
    "s_cmp_eq_u32 s58, 32\n"
    "v_exp_f32 v210, v210\n"
    "v_smfmac_f32_16x16x64_bf16 v[236:239], a[240:243], v[176:183], v253\n"
    "s_cselect_b32 s58, -6, s58\n"
    "s_cselect_b32 s75, 67108864, 0\n"
    "s_cselect_b32 s76, 25165824, 0\n"
    "v_mfma_f32_16x16x32_bf16 a[128:131], v[168:171], a[232:235], a[128:131]\n"
    "s_add_u32 s59, s59, s75\n"
    "s_add_u32 s60, s60, s76\n"
    "s_bitcmp1_b32 s90, s69\n"
    "v_mfma_f32_16x16x32_bf16 a[128:131], v[172:175], a[236:239], a[128:131]\n"
    "s_cselect_b32 s58, s58, s86\n"
    "v_exp_f32 v211, v211\n"
    "v_mfma_f32_16x16x32_bf16 a[132:135], v[180:183], a[236:239], a[132:135]\n"
    "ds_read_b64_tr_b16 a[228:229], v1 offset:8960\n"
    "v_cvt_pk_bf16_f32 v125, v210, v211\n"
    "v_mfma_f32_16x16x32_bf16 a[132:135], v[176:179], a[232:235], a[132:135]\n"
    "s_cselect_b32 s61, s61, s87\n"
    "v_exp_f32 v200, v200\n"
    "v_mfma_f32_16x16x32_bf16 v[204:207], a[192:195], v[72:75], v[228:231]\n"
    "s_cselect_b32 s50, s50, 0\n"
    "s_cselect_b32 s65, s65, 0x30000\n"
    "s_cselect_b32 s59, s59, s88\n"
    "v_mfma_f32_16x16x32_bf16 v[204:207], a[196:199], v[76:79], v[204:207]\n"
    "s_cselect_b32 s60, s60, s89\n"
    "v_exp_f32 v201, v201\n"
    "v_mfma_f32_16x16x32_bf16 v[204:207], a[200:203], v[80:83], v[204:207]\n"
    "ds_read_b64_tr_b16 a[230:231], v1 offset:13056\n"
    "v_cvt_pk_bf16_f32 v132, v200, v201\n"
    "v_mfma_f32_16x16x32_bf16 v[204:207], a[204:207], v[84:87], v[204:207]\n"
    "s_mov_b32 m0, s65\n"
    "v_exp_f32 v202, v202\n"
    "v_mfma_f32_16x16x32_bf16 v[208:211], a[204:207], v[100:103], v[232:235]\n"
    "buffer_load_dwordx4 v5, s[48:51], s63 offen nt lds\n"
    "v_mfma_f32_16x16x32_bf16 v[208:211], a[200:203], v[96:99], v[208:211]\n"
    "v_exp_f32 v203, v203\n"
    "v_mfma_f32_16x16x32_bf16 v[208:211], a[196:199], v[92:95], v[208:211]\n"
    "v_cvt_pk_bf16_f32 v133, v202, v203\n"
    "v_mfma_f32_16x16x32_bf16 v[208:211], a[192:195], v[88:91], v[208:211]\n"
    "v_exp_f32 v204, v204\n"
    "v_mfma_f32_16x16x32_bf16 a[136:139], v[184:187], a[232:235], a[136:139]\n"
    "v_mfma_f32_16x16x32_bf16 a[136:139], v[188:191], a[236:239], a[136:139]\n"
    "v_exp_f32 v205, v205\n"
    "v_mfma_f32_16x16x32_bf16 a[140:143], v[196:199], a[236:239], a[140:143]\n"
    "s_waitcnt lgkmcnt(0)\n"
    "v_cvt_pk_bf16_f32 v140, v204, v205\n"
    "v_mfma_f32_16x16x32_bf16 a[140:143], v[192:195], a[232:235], a[140:143]\n"
    "s_waitcnt lgkmcnt(0)\n"
    "v_exp_f32 v206, v206\n"
    "v_mfma_f32_16x16x32_bf16 a[144:147], v[152:155], a[224:227], a[144:147]\n"
    "buffer_load_dwordx4 v6, s[48:51], s63 offen offset:1024 nt lds\n"
    "v_mfma_f32_16x16x32_bf16 a[144:147], v[156:159], a[228:231], a[144:147]\n"
    "v_exp_f32 v207, v207\n"
    "v_mfma_f32_16x16x32_bf16 a[148:151], v[164:167], a[228:231], a[148:151]\n"
    "ds_read_b64_tr_b16 a[232:233], v2 offset:768\n"
    "v_cvt_pk_bf16_f32 v141, v206, v207\n"
    "v_mfma_f32_16x16x32_bf16 a[148:151], v[160:163], a[224:227], a[148:151]\n"
    "v_exp_f32 v208, v208\n"
    "v_mfma_f32_16x16x32_bf16 v[200:203], a[208:211], v[8:11], v[212:215]\n"
    "v_mfma_f32_16x16x32_bf16 v[200:203], a[212:215], v[12:15], v[200:203]\n"
    "v_exp_f32 v209, v209\n"
    "v_mfma_f32_16x16x32_bf16 v[200:203], a[216:219], v[16:19], v[200:203]\n"
    "ds_read_b64_tr_b16 a[234:235], v2 offset:4864\n"
    "v_cvt_pk_bf16_f32 v148, v208, v209\n"
    "v_mfma_f32_16x16x32_bf16 v[200:203], a[220:223], v[20:23], v[200:203]\n"
    "v_exp_f32 v210, v210\n"
    "v_mfma_f32_16x16x32_bf16 v[204:207], a[220:223], v[36:39], v[216:219]\n"
    "buffer_load_dwordx4 v7, s[48:51], s63 offen offset:2048 nt lds\n"
    "v_mfma_f32_16x16x32_bf16 v[204:207], a[216:219], v[32:35], v[204:207]\n"
    "v_exp_f32 v211, v211\n"
    "v_mfma_f32_16x16x32_bf16 v[204:207], a[212:215], v[28:31], v[204:207]\n"
    "ds_read_b64_tr_b16 a[236:237], v2 offset:8960\n"
    "v_cvt_pk_bf16_f32 v149, v210, v211\n"
    "v_mfma_f32_16x16x32_bf16 v[204:207], a[208:211], v[24:27], v[204:207]\n"
    "v_exp_f32 v200, v200\n"
    "v_mfma_f32_16x16x32_bf16 a[152:155], v[168:171], a[224:227], a[152:155]\n"
    "v_mfma_f32_16x16x32_bf16 a[152:155], v[172:175], a[228:231], a[152:155]\n"
    "v_exp_f32 v201, v201\n"
    "v_mfma_f32_16x16x32_bf16 a[156:159], v[180:183], a[228:231], a[156:159]\n"
    "ds_read_b64_tr_b16 a[238:239], v2 offset:13056\n"
    "v_cvt_pk_bf16_f32 v110, v200, v201\n"
    "v_mfma_f32_16x16x32_bf16 a[156:159], v[176:179], a[224:227], a[156:159]\n"
    "v_exp_f32 v202, v202\n"
    "v_smfmac_f32_16x16x64_bf16 v[240:243], a[240:243], v[184:191], v253\n"
    "buffer_load_dwordx4 v255, s[48:51], s63 offen offset:3072 nt lds\n"
    "v_mfma_f32_16x16x32_bf16 a[160:163], v[184:187], a[224:227], a[160:163]\n"
    "v_add_u32 v0, s67, v3\n"
    "v_add_u32 v1, s68, v4\n"
    "v_mfma_f32_16x16x32_bf16 a[160:163], v[188:191], a[228:231], a[160:163]\n"
    "v_exp_f32 v203, v203\n"
    "v_mfma_f32_16x16x32_bf16 a[164:167], v[196:199], a[228:231], a[164:167]\n"
    "v_cvt_pk_bf16_f32 v111, v202, v203\n"
    "v_mfma_f32_16x16x32_bf16 a[164:167], v[192:195], a[224:227], a[164:167]\n"
    "s_waitcnt lgkmcnt(0)\n"
    "v_exp_f32 v204, v204\n"
    "v_mfma_f32_16x16x32_bf16 v[208:211], a[208:211], v[40:43], v[220:223]\n"
    "v_xor_b32 v2, 32, v1\n"
    "v_mfma_f32_16x16x32_bf16 v[208:211], a[212:215], v[44:47], v[208:211]\n"
    "s_waitcnt vmcnt(12)\n"
    "v_exp_f32 v205, v205\n"
    "v_mfma_f32_16x16x32_bf16 v[208:211], a[216:219], v[48:51], v[208:211]\n"
    "v_cvt_pk_bf16_f32 v118, v204, v205\n"
    "v_mfma_f32_16x16x32_bf16 v[208:211], a[220:223], v[52:55], v[208:211]\n"
    "s_barrier\n"
    "v_exp_f32 v206, v206\n"
    "v_mfma_f32_16x16x32_bf16 v[200:203], a[220:223], v[68:71], v[224:227]\n"
    "ds_read_b128 a[192:195], v0 offset:0\n"
    "v_mfma_f32_16x16x32_bf16 v[200:203], a[216:219], v[64:67], v[200:203]\n"
    "v_exp_f32 v207, v207\n"
    "v_mfma_f32_16x16x32_bf16 v[200:203], a[212:215], v[60:63], v[200:203]\n"
    "ds_read_b64_tr_b16 a[224:225], v1 offset:0\n"
    "v_cvt_pk_bf16_f32 v119, v206, v207\n"
    "v_mfma_f32_16x16x32_bf16 v[200:203], a[208:211], v[56:59], v[200:203]\n"
    "v_exp_f32 v208, v208\n"
    "v_mfma_f32_16x16x32_bf16 a[168:171], v[152:155], a[232:235], a[168:171]\n"
    "ds_read_b128 a[196:199], v0 offset:256\n"
    "v_mfma_f32_16x16x32_bf16 a[168:171], v[156:159], a[236:239], a[168:171]\n"
    "v_exp_f32 v209, v209\n"
    "v_mfma_f32_16x16x32_bf16 a[172:175], v[164:167], a[236:239], a[172:175]\n"
    "ds_read_b64_tr_b16 a[226:227], v1 offset:4096\n"
    "v_cvt_pk_bf16_f32 v126, v208, v209\n"
    "v_mfma_f32_16x16x32_bf16 a[172:175], v[160:163], a[232:235], a[172:175]\n"
    "v_exp_f32 v210, v210\n"
    "v_mfma_f32_16x16x32_bf16 a[176:179], v[168:171], a[232:235], a[176:179]\n"
    "ds_read_b128 a[200:203], v0 offset:512\n"
    "v_mfma_f32_16x16x32_bf16 a[176:179], v[172:175], a[236:239], a[176:179]\n"
    "v_exp_f32 v211, v211\n"
    "v_mfma_f32_16x16x32_bf16 a[180:183], v[180:183], a[236:239], a[180:183]\n"
    "ds_read_b64_tr_b16 a[228:229], v1 offset:8192\n"
    "v_cvt_pk_bf16_f32 v127, v210, v211\n"
    "v_mfma_f32_16x16x32_bf16 a[180:183], v[176:179], a[232:235], a[180:183]\n"
    "v_exp_f32 v200, v200\n"
    "v_mfma_f32_16x16x32_bf16 v[204:207], a[208:211], v[72:75], v[228:231]\n"
    "ds_read_b128 a[204:207], v0 offset:768\n"
    "v_mfma_f32_16x16x32_bf16 v[204:207], a[212:215], v[76:79], v[204:207]\n"
    "v_exp_f32 v201, v201\n"
    "v_mfma_f32_16x16x32_bf16 v[204:207], a[216:219], v[80:83], v[204:207]\n"
    "ds_read_b64_tr_b16 a[230:231], v1 offset:12288\n"
    "v_cvt_pk_bf16_f32 v134, v200, v201\n"
    "v_mfma_f32_16x16x32_bf16 v[204:207], a[220:223], v[84:87], v[204:207]\n"
    "s_nop 0\n"
    "v_exp_f32 v202, v202\n"
    "v_mfma_f32_16x16x32_bf16 v[208:211], a[220:223], v[100:103], v[232:235]\n"
    "v_mfma_f32_16x16x32_bf16 v[208:211], a[216:219], v[96:99], v[208:211]\n"
    "v_exp_f32 v203, v203\n"
    "v_mfma_f32_16x16x32_bf16 v[208:211], a[212:215], v[92:95], v[208:211]\n"
    "v_cvt_pk_bf16_f32 v135, v202, v203\n"
    "v_mfma_f32_16x16x32_bf16 v[208:211], a[208:211], v[88:91], v[208:211]\n"
    "v_exp_f32 v204, v204\n"
    "v_mfma_f32_16x16x32_bf16 a[184:187], v[184:187], a[232:235], a[184:187]\n"
    "v_mfma_f32_16x16x32_bf16 a[184:187], v[188:191], a[236:239], a[184:187]\n"
    "v_exp_f32 v205, v205\n"
    "v_mfma_f32_16x16x32_bf16 a[188:191], v[196:199], a[236:239], a[188:191]\n"
    "v_cvt_pk_bf16_f32 v142, v204, v205\n"
    "v_mfma_f32_16x16x32_bf16 a[188:191], v[192:195], a[232:235], a[188:191]\n"
    "s_waitcnt lgkmcnt(0)\n"
    "v_exp_f32 v206, v206\n"
    "v_smfmac_f32_16x16x64_bf16 v[244:247], a[240:243], v[192:199], v253\n"
    "v_mfma_f32_16x16x32_bf16 a[0:3], v[104:107], a[224:227], a[0:3]\n"
    "ds_read_b128 a[208:211], v0 offset:4096\n"
    "s_bitcmp1_b32 s58, 0\n"
    "v_mfma_f32_16x16x32_bf16 a[0:3], v[108:111], a[228:231], a[0:3]\n"
    "s_cselect_b64 s[48:49], s[44:45], s[40:41]\n"
    "v_exp_f32 v207, v207\n"
    "v_mfma_f32_16x16x32_bf16 a[4:7], v[116:119], a[228:231], a[4:7]\n"
    "ds_read_b64_tr_b16 a[232:233], v2 offset:0\n"
    "v_cvt_pk_bf16_f32 v143, v206, v207\n"
    "v_mfma_f32_16x16x32_bf16 a[4:7], v[112:115], a[224:227], a[4:7]\n"
    "s_lshl_b32 s75, s58, 13\n"
    "v_exp_f32 v208, v208\n"
    "v_mfma_f32_16x16x32_bf16 v[200:203], a[192:195], v[8:11], v[212:215]\n"
    "s_and_b32 s75, s75, 0x3c000\n"
    "s_add_u32 s63, s59, s75\n"
    "s_lshl_b32 s76, s58, 12\n"
    "v_mfma_f32_16x16x32_bf16 v[200:203], a[196:199], v[12:15], v[200:203]\n"
    "s_add_u32 s76, s60, s76\n"
    "v_exp_f32 v209, v209\n"
    "v_mfma_f32_16x16x32_bf16 v[200:203], a[200:203], v[16:19], v[200:203]\n"
    "ds_read_b64_tr_b16 a[234:235], v2 offset:4096\n"
    "v_cvt_pk_bf16_f32 v150, v208, v209\n"
    "v_mfma_f32_16x16x32_bf16 v[200:203], a[204:207], v[20:23], v[200:203]\n"
    "s_cmp_lt_i32 s58, 0\n"
    "v_exp_f32 v210, v210\n"
    "v_mfma_f32_16x16x32_bf16 v[204:207], a[204:207], v[36:39], v[216:219]\n"
    "ds_read_b128 a[212:215], v0 offset:4352\n"
    "s_cselect_b64 s[48:49], s[36:37], s[48:49]\n"
    "v_mfma_f32_16x16x32_bf16 v[204:207], a[200:203], v[32:35], v[204:207]\n"
    "s_cselect_b32 s50, s38, s42\n"
    "v_exp_f32 v211, v211\n"
    "v_mfma_f32_16x16x32_bf16 v[204:207], a[196:199], v[28:31], v[204:207]\n"
    "ds_read_b64_tr_b16 a[236:237], v2 offset:8192\n"
    "v_cvt_pk_bf16_f32 v151, v210, v211\n"
    "v_mfma_f32_16x16x32_bf16 v[204:207], a[192:195], v[24:27], v[204:207]\n"
    "s_cselect_b32 s63, s76, s63\n"
    "v_exp_f32 v200, v200\n"
    "v_mfma_f32_16x16x32_bf16 a[8:11], v[120:123], a[224:227], a[8:11]\n"
    "s_add_u32 s65, s61, s62\n"
    "s_add_u32 s61, s61, 16384\n"
    "s_cmp_eq_u32 s61, 163840\n"
    "v_mfma_f32_16x16x32_bf16 a[8:11], v[124:127], a[228:231], a[8:11]\n"
    "s_cselect_b32 s61, s79, s61\n"
    "v_exp_f32 v201, v201\n"
    "v_mfma_f32_16x16x32_bf16 a[12:15], v[132:135], a[228:231], a[12:15]\n"
    "ds_read_b64_tr_b16 a[238:239], v2 offset:12288\n"
    "v_cvt_pk_bf16_f32 v152, v200, v201\n"
    "v_mfma_f32_16x16x32_bf16 a[12:15], v[128:131], a[224:227], a[12:15]\n"
    "s_add_i32 s58, s58, 1\n"
    "v_exp_f32 v202, v202\n"
    "v_mfma_f32_16x16x32_bf16 a[16:19], v[136:139], a[224:227], a[16:19]\n"
    "ds_read_b128 a[216:219], v0 offset:4608\n"
    "s_cmp_eq_u32 s58, 32\n"
    "v_mfma_f32_16x16x32_bf16 a[16:19], v[140:143], a[228:231], a[16:19]\n"
    "s_cselect_b32 s58, -6, s58\n"
    "v_exp_f32 v203, v203\n"
    "v_mfma_f32_16x16x32_bf16 a[20:23], v[148:151], a[228:231], a[20:23]\n"
    "s_cselect_b32 s75, 67108864, 0\n"
    "v_cvt_pk_bf16_f32 v153, v202, v203\n"
    "s_cselect_b32 s76, 25165824, 0\n"
    "v_mfma_f32_16x16x32_bf16 a[20:23], v[144:147], a[224:227], a[20:23]\n"
    "s_add_u32 s59, s59, s75\n"
    "v_exp_f32 v204, v204\n"
    "v_mfma_f32_16x16x32_bf16 v[208:211], a[192:195], v[40:43], v[220:223]\n"
    "s_add_u32 s60, s60, s76\n"
    "s_mov_b32 m0, s65\n"
    "v_mfma_f32_16x16x32_bf16 v[208:211], a[196:199], v[44:47], v[208:211]\n"
    "v_exp_f32 v205, v205\n"
    "v_mfma_f32_16x16x32_bf16 v[208:211], a[200:203], v[48:51], v[208:211]\n"
    "v_cvt_pk_bf16_f32 v160, v204, v205\n"
    "v_mfma_f32_16x16x32_bf16 v[208:211], a[204:207], v[52:55], v[208:211]\n"
    "v_exp_f32 v206, v206\n"
    "v_mfma_f32_16x16x32_bf16 v[200:203], a[204:207], v[68:71], v[224:227]\n"
    "ds_read_b128 a[220:223], v0 offset:4864\n"
    "v_mfma_f32_16x16x32_bf16 v[200:203], a[200:203], v[64:67], v[200:203]\n"
    "v_exp_f32 v207, v207\n"
    "v_mfma_f32_16x16x32_bf16 v[200:203], a[196:199], v[60:63], v[200:203]\n"
    "ds_read_b64_tr_b16 a[224:225], v1 offset:256\n"
    "v_cvt_pk_bf16_f32 v161, v206, v207\n"
    "v_mfma_f32_16x16x32_bf16 v[200:203], a[192:195], v[56:59], v[200:203]\n"
    "s_waitcnt lgkmcnt(3)\n"
    "v_exp_f32 v208, v208\n"
    "v_mfma_f32_16x16x32_bf16 a[24:27], v[104:107], a[232:235], a[24:27]\n"
    "buffer_load_dwordx4 v5, s[48:51], s63 offen nt lds\n"
    "v_mfma_f32_16x16x32_bf16 a[24:27], v[108:111], a[236:239], a[24:27]\n"
    "v_exp_f32 v209, v209\n"
    "v_mfma_f32_16x16x32_bf16 a[28:31], v[116:119], a[236:239], a[28:31]\n"
    "ds_read_b64_tr_b16 a[226:227], v1 offset:4352\n"
    "v_cvt_pk_bf16_f32 v168, v208, v209\n"
    "v_mfma_f32_16x16x32_bf16 a[28:31], v[112:115], a[232:235], a[28:31]\n"
    "v_exp_f32 v210, v210\n"
    "v_smfmac_f32_16x16x64_bf16 a[244:247], a[240:243], v[104:111], v253\n"
    "v_mfma_f32_16x16x32_bf16 a[32:35], v[120:123], a[232:235], a[32:35]\n"
    "v_mfma_f32_16x16x32_bf16 a[32:35], v[124:127], a[236:239], a[32:35]\n"
    "v_exp_f32 v211, v211\n"
    "v_mfma_f32_16x16x32_bf16 a[36:39], v[132:135], a[236:239], a[36:39]\n"
    "ds_read_b64_tr_b16 a[228:229], v1 offset:8448\n"
    "v_cvt_pk_bf16_f32 v169, v210, v211\n"
    "v_mfma_f32_16x16x32_bf16 a[36:39], v[128:131], a[232:235], a[36:39]\n"
    "v_exp_f32 v200, v200\n"
    "v_mfma_f32_16x16x32_bf16 v[204:207], a[192:195], v[72:75], v[228:231]\n"
    "buffer_load_dwordx4 v6, s[48:51], s63 offen offset:1024 nt lds\n"
    "v_mfma_f32_16x16x32_bf16 v[204:207], a[196:199], v[76:79], v[204:207]\n"
    "v_exp_f32 v201, v201\n"
    "v_mfma_f32_16x16x32_bf16 v[204:207], a[200:203], v[80:83], v[204:207]\n"
    "ds_read_b64_tr_b16 a[230:231], v1 offset:12544\n"
    "v_cvt_pk_bf16_f32 v176, v200, v201\n"
    "v_mfma_f32_16x16x32_bf16 v[204:207], a[204:207], v[84:87], v[204:207]\n"
    "s_nop 0\n"
    "v_exp_f32 v202, v202\n"
    "v_mfma_f32_16x16x32_bf16 v[208:211], a[204:207], v[100:103], v[232:235]\n"
    "v_mfma_f32_16x16x32_bf16 v[208:211], a[200:203], v[96:99], v[208:211]\n"
    "v_exp_f32 v203, v203\n"
    "v_mfma_f32_16x16x32_bf16 v[208:211], a[196:199], v[92:95], v[208:211]\n"
    "v_cvt_pk_bf16_f32 v177, v202, v203\n"
    "v_mfma_f32_16x16x32_bf16 v[208:211], a[192:195], v[88:91], v[208:211]\n"
    "v_exp_f32 v204, v204\n"
    "v_mfma_f32_16x16x32_bf16 a[40:43], v[136:139], a[232:235], a[40:43]\n"
    "buffer_load_dwordx4 v7, s[48:51], s63 offen offset:2048 nt lds\n"
    "v_mfma_f32_16x16x32_bf16 a[40:43], v[140:143], a[236:239], a[40:43]\n"
    "v_exp_f32 v205, v205\n"
    "v_mfma_f32_16x16x32_bf16 a[44:47], v[148:151], a[236:239], a[44:47]\n"
    "s_waitcnt lgkmcnt(0)\n"
    "v_cvt_pk_bf16_f32 v184, v204, v205\n"
    "v_mfma_f32_16x16x32_bf16 a[44:47], v[144:147], a[232:235], a[44:47]\n"
    "s_waitcnt lgkmcnt(0)\n"
    "v_exp_f32 v206, v206\n"
    "v_mfma_f32_16x16x32_bf16 a[48:51], v[104:107], a[224:227], a[48:51]\n"
    "ds_read_b128 a[192:195], v0 offset:8192\n"
    "v_mfma_f32_16x16x32_bf16 a[48:51], v[108:111], a[228:231], a[48:51]\n"
    "v_exp_f32 v207, v207\n"
    "v_mfma_f32_16x16x32_bf16 a[52:55], v[116:119], a[228:231], a[52:55]\n"
    "ds_read_b64_tr_b16 a[232:233], v2 offset:256\n"
    "v_cvt_pk_bf16_f32 v185, v206, v207\n"
    "v_mfma_f32_16x16x32_bf16 a[52:55], v[112:115], a[224:227], a[52:55]\n"
    "v_exp_f32 v208, v208\n"
    "v_mfma_f32_16x16x32_bf16 v[200:203], a[208:211], v[8:11], v[212:215]\n"
    "v_mfma_f32_16x16x32_bf16 v[200:203], a[212:215], v[12:15], v[200:203]\n"
    "v_exp_f32 v209, v209\n"
    "v_mfma_f32_16x16x32_bf16 v[200:203], a[216:219], v[16:19], v[200:203]\n"
    "ds_read_b64_tr_b16 a[234:235], v2 offset:4352\n"
    "v_cvt_pk_bf16_f32 v192, v208, v209\n"
    "v_mfma_f32_16x16x32_bf16 v[200:203], a[220:223], v[20:23], v[200:203]\n"
    "v_exp_f32 v210, v210\n"
    "v_mfma_f32_16x16x32_bf16 v[204:207], a[220:223], v[36:39], v[216:219]\n"
    "ds_read_b128 a[196:199], v0 offset:8448\n"
    "v_mfma_f32_16x16x32_bf16 v[204:207], a[216:219], v[32:35], v[204:207]\n"
    "v_exp_f32 v211, v211\n"
    "v_mfma_f32_16x16x32_bf16 v[204:207], a[212:215], v[28:31], v[204:207]\n"
    "ds_read_b64_tr_b16 a[236:237], v2 offset:8448\n"
    "v_cvt_pk_bf16_f32 v193, v210, v211\n"
    "v_mfma_f32_16x16x32_bf16 v[204:207], a[208:211], v[24:27], v[204:207]\n"
    "v_exp_f32 v200, v200\n"
    "v_mfma_f32_16x16x32_bf16 a[56:59], v[120:123], a[224:227], a[56:59]\n"
    "buffer_load_dwordx4 v255, s[48:51], s63 offen offset:3072 nt lds\n"
    "v_mfma_f32_16x16x32_bf16 a[56:59], v[124:127], a[228:231], a[56:59]\n"
    "s_mov_b32 s68, s66\n"
    "v_exp_f32 v201, v201\n"
    "v_mfma_f32_16x16x32_bf16 a[60:63], v[132:135], a[228:231], a[60:63]\n"
    "ds_read_b64_tr_b16 a[238:239], v2 offset:12544\n"
    "v_cvt_pk_bf16_f32 v154, v200, v201\n"
    "v_mfma_f32_16x16x32_bf16 a[60:63], v[128:131], a[224:227], a[60:63]\n"
    "s_add_u32 s66, s66, 16384\n"
    "v_exp_f32 v202, v202\n"
    "v_smfmac_f32_16x16x64_bf16 a[248:251], a[240:243], v[112:119], v253\n"
    "s_cmp_eq_u32 s66, 163840\n"
    "s_cselect_b32 s66, s79, s66\n"
    "s_mov_b32 s67, s66\n"
    "v_mfma_f32_16x16x32_bf16 a[64:67], v[136:139], a[224:227], a[64:67]\n"
    "ds_read_b128 a[200:203], v0 offset:8704\n"
    "s_add_u32 s66, s66, 16384\n"
    "v_mfma_f32_16x16x32_bf16 a[64:67], v[140:143], a[228:231], a[64:67]\n"
    "s_cmp_eq_u32 s66, 163840\n"
    "v_exp_f32 v203, v203\n"
    "v_mfma_f32_16x16x32_bf16 a[68:71], v[148:151], a[228:231], a[68:71]\n"
    "s_cselect_b32 s66, s79, s66\n"
    "v_cvt_pk_bf16_f32 v155, v202, v203\n"
    "s_bitcmp1_b32 s58, 0\n"
    "v_mfma_f32_16x16x32_bf16 a[68:71], v[144:147], a[224:227], a[68:71]\n"
    "s_cselect_b64 s[48:49], s[44:45], s[40:41]\n"
    "v_exp_f32 v204, v204\n"
    "v_mfma_f32_16x16x32_bf16 v[208:211], a[208:211], v[40:43], v[220:223]\n"
    "s_lshl_b32 s75, s58, 13\n"
    "s_and_b32 s75, s75, 0x3c000\n"
    "s_add_u32 s63, s59, s75\n"
    "v_mfma_f32_16x16x32_bf16 v[208:211], a[212:215], v[44:47], v[208:211]\n"
    "s_lshl_b32 s76, s58, 12\n"
    "v_exp_f32 v205, v205\n"
    "v_mfma_f32_16x16x32_bf16 v[208:211], a[216:219], v[48:51], v[208:211]\n"
    "s_add_u32 s76, s60, s76\n"
    "v_cvt_pk_bf16_f32 v162, v204, v205\n"
    "s_cmp_lt_i32 s58, 0\n"
    "v_mfma_f32_16x16x32_bf16 v[208:211], a[220:223], v[52:55], v[208:211]\n"
    "s_cselect_b64 s[48:49], s[36:37], s[48:49]\n"
    "v_exp_f32 v206, v206\n"
    "v_mfma_f32_16x16x32_bf16 v[200:203], a[220:223], v[68:71], v[224:227]\n"
    "ds_read_b128 a[204:207], v0 offset:8960\n"
    "s_cselect_b32 s50, s38, s42\n"
    "v_mfma_f32_16x16x32_bf16 v[200:203], a[216:219], v[64:67], v[200:203]\n"
    "s_cselect_b32 s63, s76, s63\n"
    "v_exp_f32 v207, v207\n"
    "v_mfma_f32_16x16x32_bf16 v[200:203], a[212:215], v[60:63], v[200:203]\n"
    "ds_read_b64_tr_b16 a[224:225], v1 offset:512\n"
    "v_cvt_pk_bf16_f32 v163, v206, v207\n"
    "v_mfma_f32_16x16x32_bf16 v[200:203], a[208:211], v[56:59], v[200:203]\n"
    "s_waitcnt lgkmcnt(3)\n"
    "v_exp_f32 v208, v208\n"
    "v_mfma_f32_16x16x32_bf16 a[72:75], v[104:107], a[232:235], a[72:75]\n"
    "s_add_u32 s65, s61, s62\n"
    "s_add_u32 s61, s61, 16384\n"
    "s_cmp_eq_u32 s61, 163840\n"
    "v_mfma_f32_16x16x32_bf16 a[72:75], v[108:111], a[236:239], a[72:75]\n"
    "s_cselect_b32 s61, s79, s61\n"
    "v_exp_f32 v209, v209\n"
    "v_mfma_f32_16x16x32_bf16 a[76:79], v[116:119], a[236:239], a[76:79]\n"
    "ds_read_b64_tr_b16 a[226:227], v1 offset:4608\n"
    "v_cvt_pk_bf16_f32 v170, v208, v209\n"
    "v_mfma_f32_16x16x32_bf16 a[76:79], v[112:115], a[232:235], a[76:79]\n"
    "s_add_i32 s58, s58, 1\n"
    "v_exp_f32 v210, v210\n"
    "v_mfma_f32_16x16x32_bf16 a[80:83], v[120:123], a[232:235], a[80:83]\n"
    "s_cmp_eq_u32 s58, 32\n"
    "s_cselect_b32 s58, -6, s58\n"
    "s_cselect_b32 s75, 67108864, 0\n"
    "v_mfma_f32_16x16x32_bf16 a[80:83], v[124:127], a[236:239], a[80:83]\n"
    "s_cselect_b32 s76, 25165824, 0\n"
    "v_exp_f32 v211, v211\n"
    "v_mfma_f32_16x16x32_bf16 a[84:87], v[132:135], a[236:239], a[84:87]\n"
    "ds_read_b64_tr_b16 a[228:229], v1 offset:8704\n"
    "v_cvt_pk_bf16_f32 v171, v210, v211\n"
    "v_mfma_f32_16x16x32_bf16 a[84:87], v[128:131], a[232:235], a[84:87]\n"
    "s_add_u32 s59, s59, s75\n"
    "v_exp_f32 v200, v200\n"
    "v_mfma_f32_16x16x32_bf16 v[204:207], a[208:211], v[72:75], v[228:231]\n"
    "s_add_u32 s60, s60, s76\n"
    "s_mov_b32 m0, s65\n"
    "v_mfma_f32_16x16x32_bf16 v[204:207], a[212:215], v[76:79], v[204:207]\n"
    "v_exp_f32 v201, v201\n"
    "v_mfma_f32_16x16x32_bf16 v[204:207], a[216:219], v[80:83], v[204:207]\n"
    "ds_read_b64_tr_b16 a[230:231], v1 offset:12800\n"
    "v_cvt_pk_bf16_f32 v178, v200, v201\n"
    "v_mfma_f32_16x16x32_bf16 v[204:207], a[220:223], v[84:87], v[204:207]\n"
    "v_exp_f32 v202, v202\n"
    "v_mfma_f32_16x16x32_bf16 v[208:211], a[220:223], v[100:103], v[232:235]\n"
    "buffer_load_dwordx4 v5, s[48:51], s63 offen nt lds\n"
    "v_mfma_f32_16x16x32_bf16 v[208:211], a[216:219], v[96:99], v[208:211]\n"
    "v_exp_f32 v203, v203\n"
    "v_mfma_f32_16x16x32_bf16 v[208:211], a[212:215], v[92:95], v[208:211]\n"
    "v_cvt_pk_bf16_f32 v179, v202, v203\n"
    "v_mfma_f32_16x16x32_bf16 v[208:211], a[208:211], v[88:91], v[208:211]\n"
    "v_exp_f32 v204, v204\n"
    "v_mfma_f32_16x16x32_bf16 a[88:91], v[136:139], a[232:235], a[88:91]\n"
    "v_mfma_f32_16x16x32_bf16 a[88:91], v[140:143], a[236:239], a[88:91]\n"
    "v_exp_f32 v205, v205\n"
    "v_mfma_f32_16x16x32_bf16 a[92:95], v[148:151], a[236:239], a[92:95]\n"
    "v_cvt_pk_bf16_f32 v186, v204, v205\n"
    "v_mfma_f32_16x16x32_bf16 a[92:95], v[144:147], a[232:235], a[92:95]\n"
    "s_waitcnt lgkmcnt(0)\n"
    "v_exp_f32 v206, v206\n"
    "v_smfmac_f32_16x16x64_bf16 a[252:255], a[240:243], v[120:127], v253\n"
    "s_waitcnt lgkmcnt(0)\n"
    "v_mfma_f32_16x16x32_bf16 a[96:99], v[104:107], a[224:227], a[96:99]\n"
    "ds_read_b128 a[208:211], v0 offset:12288\n"
    "v_mfma_f32_16x16x32_bf16 a[96:99], v[108:111], a[228:231], a[96:99]\n"
    "v_exp_f32 v207, v207\n"
    "v_mfma_f32_16x16x32_bf16 a[100:103], v[116:119], a[228:231], a[100:103]\n"
    "ds_read_b64_tr_b16 a[232:233], v2 offset:512\n"
    "v_cvt_pk_bf16_f32 v187, v206, v207\n"
    "v_mfma_f32_16x16x32_bf16 a[100:103], v[112:115], a[224:227], a[100:103]\n"
    "v_exp_f32 v208, v208\n"
    "v_mfma_f32_16x16x32_bf16 v[200:203], a[192:195], v[8:11], v[212:215]\n"
    "buffer_load_dwordx4 v6, s[48:51], s63 offen offset:1024 nt lds\n"
    "v_mfma_f32_16x16x32_bf16 v[200:203], a[196:199], v[12:15], v[200:203]\n"
    "v_exp_f32 v209, v209\n"
    "v_mfma_f32_16x16x32_bf16 v[200:203], a[200:203], v[16:19], v[200:203]\n"
    "ds_read_b64_tr_b16 a[234:235], v2 offset:4608\n"
    "v_cvt_pk_bf16_f32 v194, v208, v209\n"
    "v_mfma_f32_16x16x32_bf16 v[200:203], a[204:207], v[20:23], v[200:203]\n"
    "v_exp_f32 v210, v210\n"
    "v_mfma_f32_16x16x32_bf16 v[204:207], a[204:207], v[36:39], v[216:219]\n"
    "ds_read_b128 a[212:215], v0 offset:12544\n"
    "v_mfma_f32_16x16x32_bf16 v[204:207], a[200:203], v[32:35], v[204:207]\n"
    "v_exp_f32 v211, v211\n"
    "v_mfma_f32_16x16x32_bf16 v[204:207], a[196:199], v[28:31], v[204:207]\n"
    "ds_read_b64_tr_b16 a[236:237], v2 offset:8704\n"
    "v_cvt_pk_bf16_f32 v195, v210, v211\n"
    "v_mfma_f32_16x16x32_bf16 v[204:207], a[192:195], v[24:27], v[204:207]\n"
    "v_exp_f32 v200, v200\n"
    "v_mfma_f32_16x16x32_bf16 a[104:107], v[120:123], a[224:227], a[104:107]\n"
    "buffer_load_dwordx4 v7, s[48:51], s63 offen offset:2048 nt lds\n"
    "v_mfma_f32_16x16x32_bf16 a[104:107], v[124:127], a[228:231], a[104:107]\n"
    "v_exp_f32 v201, v201\n"
    "v_mfma_f32_16x16x32_bf16 a[108:111], v[132:135], a[228:231], a[108:111]\n"
    "ds_read_b64_tr_b16 a[238:239], v2 offset:12800\n"
    "v_cvt_pk_bf16_f32 v156, v200, v201\n"
    "v_mfma_f32_16x16x32_bf16 a[108:111], v[128:131], a[224:227], a[108:111]\n"
    "v_exp_f32 v202, v202\n"
    "v_mfma_f32_16x16x32_bf16 a[112:115], v[136:139], a[224:227], a[112:115]\n"
    "ds_read_b128 a[216:219], v0 offset:12800\n"
    "v_mfma_f32_16x16x32_bf16 a[112:115], v[140:143], a[228:231], a[112:115]\n"
    "v_exp_f32 v203, v203\n"
    "v_mfma_f32_16x16x32_bf16 a[116:119], v[148:151], a[228:231], a[116:119]\n"
    "v_cvt_pk_bf16_f32 v157, v202, v203\n"
    "v_mfma_f32_16x16x32_bf16 a[116:119], v[144:147], a[224:227], a[116:119]\n"
    "v_exp_f32 v204, v204\n"
    "v_mfma_f32_16x16x32_bf16 v[208:211], a[192:195], v[40:43], v[220:223]\n"
    "v_mfma_f32_16x16x32_bf16 v[208:211], a[196:199], v[44:47], v[208:211]\n"
    "v_exp_f32 v205, v205\n"
    "v_mfma_f32_16x16x32_bf16 v[208:211], a[200:203], v[48:51], v[208:211]\n"
    "v_cvt_pk_bf16_f32 v164, v204, v205\n"
    "v_mfma_f32_16x16x32_bf16 v[208:211], a[204:207], v[52:55], v[208:211]\n"
    "v_exp_f32 v206, v206\n"
    "v_mfma_f32_16x16x32_bf16 v[200:203], a[204:207], v[68:71], v[224:227]\n"
    "ds_read_b128 a[220:223], v0 offset:13056\n"
    "v_mfma_f32_16x16x32_bf16 v[200:203], a[200:203], v[64:67], v[200:203]\n"
    "v_exp_f32 v207, v207\n"
    "v_mfma_f32_16x16x32_bf16 v[200:203], a[196:199], v[60:63], v[200:203]\n"
    "ds_read_b64_tr_b16 a[224:225], v1 offset:768\n"
    "v_cvt_pk_bf16_f32 v165, v206, v207\n"
    "v_mfma_f32_16x16x32_bf16 v[200:203], a[192:195], v[56:59], v[200:203]\n"
    "s_waitcnt lgkmcnt(3)\n"
    "v_exp_f32 v208, v208\n"
    "v_mfma_f32_16x16x32_bf16 a[120:123], v[104:107], a[232:235], a[120:123]\n"
    "v_mfma_f32_16x16x32_bf16 a[120:123], v[108:111], a[236:239], a[120:123]\n"
    "v_exp_f32 v209, v209\n"
    "v_mfma_f32_16x16x32_bf16 a[124:127], v[116:119], a[236:239], a[124:127]\n"
    "ds_read_b64_tr_b16 a[226:227], v1 offset:4864\n"
    "v_cvt_pk_bf16_f32 v172, v208, v209\n"
    "v_mfma_f32_16x16x32_bf16 a[124:127], v[112:115], a[232:235], a[124:127]\n"
    "v_exp_f32 v210, v210\n"
    "v_smfmac_f32_16x16x64_bf16 v[236:239], a[240:243], v[128:135], v253\n"
    "buffer_load_dwordx4 v255, s[48:51], s63 offen offset:3072 nt lds\n"
    "v_mfma_f32_16x16x32_bf16 a[128:131], v[120:123], a[232:235], a[128:131]\n"
    "v_mfma_f32_16x16x32_bf16 a[128:131], v[124:127], a[236:239], a[128:131]\n"
    "v_exp_f32 v211, v211\n"
    "v_mfma_f32_16x16x32_bf16 a[132:135], v[132:135], a[236:239], a[132:135]\n"
    "ds_read_b64_tr_b16 a[228:229], v1 offset:8960\n"
    "v_cvt_pk_bf16_f32 v173, v210, v211\n"
    "v_mfma_f32_16x16x32_bf16 a[132:135], v[128:131], a[232:235], a[132:135]\n"
    "v_exp_f32 v200, v200\n"
    "v_mfma_f32_16x16x32_bf16 v[204:207], a[192:195], v[72:75], v[228:231]\n"
    "v_mfma_f32_16x16x32_bf16 v[204:207], a[196:199], v[76:79], v[204:207]\n"
    "v_exp_f32 v201, v201\n"
    "v_mfma_f32_16x16x32_bf16 v[204:207], a[200:203], v[80:83], v[204:207]\n"
    "ds_read_b64_tr_b16 a[230:231], v1 offset:13056\n"
    "v_cvt_pk_bf16_f32 v180, v200, v201\n"
    "v_mfma_f32_16x16x32_bf16 v[204:207], a[204:207], v[84:87], v[204:207]\n"
    "s_nop 0\n"
    "v_exp_f32 v202, v202\n"
    "v_mfma_f32_16x16x32_bf16 v[208:211], a[204:207], v[100:103], v[232:235]\n"
    "v_mfma_f32_16x16x32_bf16 v[208:211], a[200:203], v[96:99], v[208:211]\n"
    "v_exp_f32 v203, v203\n"
    "v_mfma_f32_16x16x32_bf16 v[208:211], a[196:199], v[92:95], v[208:211]\n"
    "v_cvt_pk_bf16_f32 v181, v202, v203\n"
    "v_mfma_f32_16x16x32_bf16 v[208:211], a[192:195], v[88:91], v[208:211]\n"
    "v_exp_f32 v204, v204\n"
    "v_mfma_f32_16x16x32_bf16 a[136:139], v[136:139], a[232:235], a[136:139]\n"
    "v_mfma_f32_16x16x32_bf16 a[136:139], v[140:143], a[236:239], a[136:139]\n"
    "v_exp_f32 v205, v205\n"
    "v_mfma_f32_16x16x32_bf16 a[140:143], v[148:151], a[236:239], a[140:143]\n"
    "s_waitcnt lgkmcnt(0)\n"
    "v_cvt_pk_bf16_f32 v188, v204, v205\n"
    "v_mfma_f32_16x16x32_bf16 a[140:143], v[144:147], a[232:235], a[140:143]\n"
    "s_waitcnt lgkmcnt(0)\n"
    "v_exp_f32 v206, v206\n"
    "v_mfma_f32_16x16x32_bf16 a[144:147], v[104:107], a[224:227], a[144:147]\n"
    "v_mfma_f32_16x16x32_bf16 a[144:147], v[108:111], a[228:231], a[144:147]\n"
    "v_exp_f32 v207, v207\n"
    "v_mfma_f32_16x16x32_bf16 a[148:151], v[116:119], a[228:231], a[148:151]\n"
    "ds_read_b64_tr_b16 a[232:233], v2 offset:768\n"
    "v_cvt_pk_bf16_f32 v189, v206, v207\n"
    "v_mfma_f32_16x16x32_bf16 a[148:151], v[112:115], a[224:227], a[148:151]\n"
    "v_exp_f32 v208, v208\n"
    "v_mfma_f32_16x16x32_bf16 v[200:203], a[208:211], v[8:11], v[212:215]\n"
    "v_mfma_f32_16x16x32_bf16 v[200:203], a[212:215], v[12:15], v[200:203]\n"
    "v_exp_f32 v209, v209\n"
    "v_mfma_f32_16x16x32_bf16 v[200:203], a[216:219], v[16:19], v[200:203]\n"
    "ds_read_b64_tr_b16 a[234:235], v2 offset:4864\n"
    "v_cvt_pk_bf16_f32 v196, v208, v209\n"
    "v_mfma_f32_16x16x32_bf16 v[200:203], a[220:223], v[20:23], v[200:203]\n"
    "v_exp_f32 v210, v210\n"
    "v_mfma_f32_16x16x32_bf16 v[204:207], a[220:223], v[36:39], v[216:219]\n"
    "v_mfma_f32_16x16x32_bf16 v[204:207], a[216:219], v[32:35], v[204:207]\n"
    "v_exp_f32 v211, v211\n"
    "v_mfma_f32_16x16x32_bf16 v[204:207], a[212:215], v[28:31], v[204:207]\n"
    "ds_read_b64_tr_b16 a[236:237], v2 offset:8960\n"
    "v_cvt_pk_bf16_f32 v197, v210, v211\n"
    "v_mfma_f32_16x16x32_bf16 v[204:207], a[208:211], v[24:27], v[204:207]\n"
    "v_exp_f32 v200, v200\n"
    "v_mfma_f32_16x16x32_bf16 a[152:155], v[120:123], a[224:227], a[152:155]\n"
    "v_mfma_f32_16x16x32_bf16 a[152:155], v[124:127], a[228:231], a[152:155]\n"
    "v_exp_f32 v201, v201\n"
    "v_mfma_f32_16x16x32_bf16 a[156:159], v[132:135], a[228:231], a[156:159]\n"
    "ds_read_b64_tr_b16 a[238:239], v2 offset:13056\n"
    "v_cvt_pk_bf16_f32 v158, v200, v201\n"
    "v_mfma_f32_16x16x32_bf16 a[156:159], v[128:131], a[224:227], a[156:159]\n"
    "v_exp_f32 v202, v202\n"
    "v_smfmac_f32_16x16x64_bf16 v[240:243], a[240:243], v[136:143], v253\n"
    "v_mfma_f32_16x16x32_bf16 a[160:163], v[136:139], a[224:227], a[160:163]\n"
    "v_add_u32 v0, s67, v3\n"
    "v_add_u32 v1, s68, v4\n"
    "v_mfma_f32_16x16x32_bf16 a[160:163], v[140:143], a[228:231], a[160:163]\n"
    "v_exp_f32 v203, v203\n"
    "v_mfma_f32_16x16x32_bf16 a[164:167], v[148:151], a[228:231], a[164:167]\n"
    "v_cvt_pk_bf16_f32 v159, v202, v203\n"
    "v_mfma_f32_16x16x32_bf16 a[164:167], v[144:147], a[224:227], a[164:167]\n"
    "s_waitcnt lgkmcnt(0)\n"
    "v_exp_f32 v204, v204\n"
    "v_mfma_f32_16x16x32_bf16 v[208:211], a[208:211], v[40:43], v[220:223]\n"
    "v_xor_b32 v2, 32, v1\n"
    "v_mfma_f32_16x16x32_bf16 v[208:211], a[212:215], v[44:47], v[208:211]\n"
    "s_waitcnt vmcnt(12)\n"
    "v_exp_f32 v205, v205\n"
    "v_mfma_f32_16x16x32_bf16 v[208:211], a[216:219], v[48:51], v[208:211]\n"
    "v_cvt_pk_bf16_f32 v166, v204, v205\n"
    "v_mfma_f32_16x16x32_bf16 v[208:211], a[220:223], v[52:55], v[208:211]\n"
    "s_barrier\n"
    "v_exp_f32 v206, v206\n"
    "v_mfma_f32_16x16x32_bf16 v[200:203], a[220:223], v[68:71], v[224:227]\n"
    "ds_read_b128 a[192:195], v0 offset:0\n"
    "v_mfma_f32_16x16x32_bf16 v[200:203], a[216:219], v[64:67], v[200:203]\n"
    "v_exp_f32 v207, v207\n"
    "v_mfma_f32_16x16x32_bf16 v[200:203], a[212:215], v[60:63], v[200:203]\n"
    "ds_read_b64_tr_b16 a[224:225], v1 offset:0\n"
    "v_cvt_pk_bf16_f32 v167, v206, v207\n"
    "v_mfma_f32_16x16x32_bf16 v[200:203], a[208:211], v[56:59], v[200:203]\n"
    "v_exp_f32 v208, v208\n"
    "v_mfma_f32_16x16x32_bf16 a[168:171], v[104:107], a[232:235], a[168:171]\n"
    "ds_read_b128 a[196:199], v0 offset:256\n"
    "v_mfma_f32_16x16x32_bf16 a[168:171], v[108:111], a[236:239], a[168:171]\n"
    "v_exp_f32 v209, v209\n"
    "v_mfma_f32_16x16x32_bf16 a[172:175], v[116:119], a[236:239], a[172:175]\n"
    "ds_read_b64_tr_b16 a[226:227], v1 offset:4096\n"
    "v_cvt_pk_bf16_f32 v174, v208, v209\n"
    "v_mfma_f32_16x16x32_bf16 a[172:175], v[112:115], a[232:235], a[172:175]\n"
    "v_exp_f32 v210, v210\n"
    "v_mfma_f32_16x16x32_bf16 a[176:179], v[120:123], a[232:235], a[176:179]\n"
    "ds_read_b128 a[200:203], v0 offset:512\n"
    "s_sub_u32 s69, s69, 1\n"
    "v_mfma_f32_16x16x32_bf16 a[176:179], v[124:127], a[236:239], a[176:179]\n"
    "s_cmp_lg_u32 s69, 0\n"
    "v_exp_f32 v211, v211\n"
    "v_mfma_f32_16x16x32_bf16 a[180:183], v[132:135], a[236:239], a[180:183]\n"
    "ds_read_b64_tr_b16 a[228:229], v1 offset:8192\n"
    "v_cvt_pk_bf16_f32 v175, v210, v211\n"
    "v_mfma_f32_16x16x32_bf16 a[180:183], v[128:131], a[232:235], a[180:183]\n"
    "v_exp_f32 v200, v200\n"
    "v_mfma_f32_16x16x32_bf16 v[204:207], a[208:211], v[72:75], v[228:231]\n"
    "ds_read_b128 a[204:207], v0 offset:768\n"
    "v_mfma_f32_16x16x32_bf16 v[204:207], a[212:215], v[76:79], v[204:207]\n"
    "v_exp_f32 v201, v201\n"
    "v_mfma_f32_16x16x32_bf16 v[204:207], a[216:219], v[80:83], v[204:207]\n"
    "ds_read_b64_tr_b16 a[230:231], v1 offset:12288\n"
    "v_cvt_pk_bf16_f32 v182, v200, v201\n"
    "v_mfma_f32_16x16x32_bf16 v[204:207], a[220:223], v[84:87], v[204:207]\n"
    "s_nop 0\n"
    "v_exp_f32 v202, v202\n"
    "v_mfma_f32_16x16x32_bf16 v[208:211], a[220:223], v[100:103], v[232:235]\n"
    "v_mfma_f32_16x16x32_bf16 v[208:211], a[216:219], v[96:99], v[208:211]\n"
    "v_exp_f32 v203, v203\n"
    "v_mfma_f32_16x16x32_bf16 v[208:211], a[212:215], v[92:95], v[208:211]\n"
    "v_cvt_pk_bf16_f32 v183, v202, v203\n"
    "v_mfma_f32_16x16x32_bf16 v[208:211], a[208:211], v[88:91], v[208:211]\n"
    "v_exp_f32 v204, v204\n"
    "v_mfma_f32_16x16x32_bf16 a[184:187], v[136:139], a[232:235], a[184:187]\n"
    "v_mfma_f32_16x16x32_bf16 a[184:187], v[140:143], a[236:239], a[184:187]\n"
    "v_exp_f32 v205, v205\n"
    "v_mfma_f32_16x16x32_bf16 a[188:191], v[148:151], a[236:239], a[188:191]\n"
    "v_cvt_pk_bf16_f32 v190, v204, v205\n"
    "v_mfma_f32_16x16x32_bf16 a[188:191], v[144:147], a[232:235], a[188:191]\n"
    "s_waitcnt lgkmcnt(0)\n"
    "v_exp_f32 v206, v206\n"
    "v_smfmac_f32_16x16x64_bf16 v[244:247], a[240:243], v[144:151], v253\n"
    "s_cbranch_scc1 Lloop_%=\n"
    "; ---- seam: PV(15), epilogue of the first head, Q prescale and reference scores of the second\n"
    "v_smfmac_f32_16x16x64_bf16 a[244:247], a[240:243], v[152:159], v253\n"
    "s_mov_b64 s[70:71], 0\n"
    "ds_read_b64_tr_b16 a[232:233], v2 offset:0\n"
    "v_mfma_f32_16x16x32_bf16 a[0:3], v[152:155], a[224:227], a[0:3]\n"
    "ds_read_b64_tr_b16 a[234:235], v2 offset:4096\n"
    "ds_read_b64_tr_b16 a[236:237], v2 offset:8192\n"
    "v_mfma_f32_16x16x32_bf16 a[0:3], v[156:159], a[228:231], a[0:3]\n"
    "ds_read_b64_tr_b16 a[238:239], v2 offset:12288\n"
    "ds_read_b64_tr_b16 v[104:105], v1 offset:256\n"
    "v_smfmac_f32_16x16x64_bf16 a[248:251], a[240:243], v[160:167], v253\n"
    "ds_read_b64_tr_b16 v[106:107], v1 offset:4352\n"
    "ds_read_b64_tr_b16 v[108:109], v1 offset:8448\n"
    "v_mfma_f32_16x16x32_bf16 a[4:7], v[160:163], a[224:227], a[4:7]\n"
    "ds_read_b64_tr_b16 v[110:111], v1 offset:12544\n"
    "ds_read_b64_tr_b16 v[112:113], v2 offset:256\n"
    "v_mfma_f32_16x16x32_bf16 a[4:7], v[164:167], a[228:231], a[4:7]\n"
    "ds_read_b64_tr_b16 v[114:115], v2 offset:4352\n"
    "ds_read_b64_tr_b16 v[116:117], v2 offset:8448\n"
    "v_smfmac_f32_16x16x64_bf16 a[252:255], a[240:243], v[168:175], v253\n"
    "ds_read_b64_tr_b16 v[118:119], v2 offset:12544\n"
    "v_exp_f32 v207, v207\n"
    "v_mfma_f32_16x16x32_bf16 a[8:11], v[168:171], a[224:227], a[8:11]\n"
    "ds_read_b64_tr_b16 a[192:193], v1 offset:512\n"
    "v_cvt_pk_bf16_f32 v191, v206, v207\n"
    "v_mfma_f32_16x16x32_bf16 a[8:11], v[172:175], a[228:231], a[8:11]\n"
    "ds_read_b64_tr_b16 a[194:195], v1 offset:4608\n"
    "v_exp_f32 v208, v208\n"
    "s_waitcnt lgkmcnt(11)\n"
    "v_mfma_f32_16x16x32_bf16 a[24:27], v[152:155], a[232:235], a[24:27]\n"
    "ds_read_b64_tr_b16 a[196:197], v1 offset:8704\n"
    "v_exp_f32 v209, v209\n"
    "s_waitcnt lgkmcnt(10)\n"
    "v_mfma_f32_16x16x32_bf16 a[24:27], v[156:159], a[236:239], a[24:27]\n"
    "ds_read_b64_tr_b16 a[198:199], v1 offset:12800\n"
    "v_exp_f32 v210, v210\n"
    "s_waitcnt lgkmcnt(8)\n"
    "v_mfma_f32_16x16x32_bf16 a[48:51], v[152:155], v[104:107], a[48:51]\n"
    "ds_read_b64_tr_b16 a[200:201], v2 offset:512\n"
    "v_exp_f32 v211, v211\n"
    "v_mfma_f32_16x16x32_bf16 a[48:51], v[156:159], v[108:111], a[48:51]\n"
    "ds_read_b64_tr_b16 a[202:203], v2 offset:4608\n"
    "v_cvt_pk_bf16_f32 v198, v208, v209\n"
    "s_waitcnt lgkmcnt(6)\n"
    "v_mfma_f32_16x16x32_bf16 a[72:75], v[152:155], v[112:115], a[72:75]\n"
    "ds_read_b64_tr_b16 a[204:205], v2 offset:8704\n"
    "v_cvt_pk_bf16_f32 v199, v210, v211\n"
    "v_mfma_f32_16x16x32_bf16 a[72:75], v[156:159], v[116:119], a[72:75]\n"
    "ds_read_b64_tr_b16 a[206:207], v2 offset:12800\n"
    "v_mbcnt_lo_u32_b32 v213, -1, 0\n"
    "s_waitcnt lgkmcnt(6)\n"
    "v_mfma_f32_16x16x32_bf16 a[96:99], v[152:155], a[192:195], a[96:99]\n"
    "ds_read_b64_tr_b16 a[208:209], v1 offset:768\n"
    "v_mbcnt_hi_u32_b32 v213, -1, v213\n"
    "v_mfma_f32_16x16x32_bf16 a[28:31], v[160:163], a[232:235], a[28:31]\n"
    "ds_read_b64_tr_b16 a[210:211], v1 offset:4864\n"
    "v_lshrrev_b32 v212, 4, v213\n"
    "v_mfma_f32_16x16x32_bf16 a[28:31], v[164:167], a[236:239], a[28:31]\n"
    "ds_read_b64_tr_b16 a[212:213], v1 offset:8960\n"
    "v_lshlrev_b32 v212, 10, v212\n"
    "s_waitcnt lgkmcnt(7)\n"
    "v_mfma_f32_16x16x32_bf16 a[96:99], v[156:159], a[196:199], a[96:99]\n"
    "ds_read_b64_tr_b16 a[214:215], v1 offset:13056\n"
    "v_and_b32 v214, 15, v213\n"
    "v_mfma_f32_16x16x32_bf16 a[52:55], v[160:163], v[104:107], a[52:55]\n"
    "ds_read_b64_tr_b16 a[216:217], v2 offset:768\n"
    "v_lshl_add_u32 v212, v214, 3, v212\n"
    "v_mfma_f32_16x16x32_bf16 a[52:55], v[164:167], v[108:111], a[52:55]\n"
    "ds_read_b64_tr_b16 a[218:219], v2 offset:4864\n"
    "v_add_u32 v248, s62, v212\n"
    "s_waitcnt lgkmcnt(8)\n"
    "v_mfma_f32_16x16x32_bf16 a[120:123], v[152:155], a[200:203], a[120:123]\n"
    "ds_read_b64_tr_b16 a[220:221], v2 offset:8960\n"
    "v_sub_u32 v214, 15, v213\n"
    "v_mfma_f32_16x16x32_bf16 a[76:79], v[160:163], v[112:115], a[76:79]\n"
    "ds_read_b64_tr_b16 a[222:223], v2 offset:13056\n"
    "v_ashrrev_i32 v214, 31, v214\n"
    "s_waitcnt lgkmcnt(8)\n"
    "v_mfma_f32_16x16x32_bf16 a[120:123], v[156:159], a[204:207], a[120:123]\n"
    "s_waitcnt vmcnt(8)\n"
    "v_not_b32 v214, v214\n"
    "v_and_b32 v249, 1.0, v214\n"
    "v_add_u32 v0, s84, v3\n"
    "s_waitcnt lgkmcnt(6)\n"
    "v_mfma_f32_16x16x32_bf16 a[144:147], v[152:155], a[208:211], a[144:147]\n"
    "ds_read_b128 v[40:43], v0 offset:0\n"
    "v_add_u32 v215, 131072, v0\n"
    "v_mfma_f32_16x16x32_bf16 a[76:79], v[164:167], v[116:119], a[76:79]\n"
    "ds_read_b128 v[8:11], v215 offset:0\n"
    "ds_read_b128 v[12:15], v215 offset:256\n"
    "s_waitcnt lgkmcnt(7)\n"
    "v_mfma_f32_16x16x32_bf16 a[144:147], v[156:159], a[212:215], a[144:147]\n"
    "ds_read_b128 v[16:19], v215 offset:512\n"
    "ds_read_b128 v[20:23], v215 offset:768\n"
    "v_mfma_f32_16x16x32_bf16 a[100:103], v[160:163], a[192:195], a[100:103]\n"
    "ds_read_b128 v[24:27], v215 offset:16384\n"
    "ds_read_b128 v[28:31], v215 offset:16640\n"
    "s_waitcnt lgkmcnt(9)\n"
    "v_mfma_f32_16x16x32_bf16 a[168:171], v[152:155], a[216:219], a[168:171]\n"
    "ds_read_b128 v[32:35], v215 offset:16896\n"
    "ds_read_b128 v[36:39], v215 offset:17152\n"
    "s_waitcnt lgkmcnt(9)\n"
    "v_mfma_f32_16x16x32_bf16 a[168:171], v[156:159], a[220:223], a[168:171]\n"
    "ds_read_b128 v[44:47], v0 offset:256\n"
    "ds_read_b128 v[48:51], v0 offset:512\n"
    "v_mfma_f32_16x16x32_bf16 a[100:103], v[164:167], a[196:199], a[100:103]\n"
    "ds_read_b128 v[52:55], v0 offset:768\n"
    "ds_read_b128 v[56:59], v0 offset:16384\n"
    "v_mfma_f32_16x16x32_bf16 a[124:127], v[160:163], a[200:203], a[124:127]\n"
    "ds_read_b128 v[60:63], v0 offset:16640\n"
    "s_waitcnt lgkmcnt(12)\n"
    "v_lshlrev_b32 v220, 16, v8\n"
    "v_mfma_f32_16x16x32_bf16 a[124:127], v[164:167], a[204:207], a[124:127]\n"
    "ds_read_b128 v[64:67], v0 offset:16896\n"
    "v_and_b32 v221, s74, v8\n"
    "v_mfma_f32_16x16x32_bf16 a[148:151], v[160:163], a[208:211], a[148:151]\n"
    "ds_read_b128 v[68:71], v0 offset:17152\n"
    "v_lshlrev_b32 v222, 16, v9\n"
    "v_mfma_f32_16x16x32_bf16 a[148:151], v[164:167], a[212:215], a[148:151]\n"
    "ds_read_b128 v[72:75], v0 offset:32768\n"
    "v_and_b32 v223, s74, v9\n"
    "v_mfma_f32_16x16x32_bf16 a[172:175], v[160:163], a[216:219], a[172:175]\n"
    "ds_read_b128 v[76:79], v0 offset:33024\n"
    "v_lshlrev_b32 v224, 16, v10\n"
    "v_mfma_f32_16x16x32_bf16 a[172:175], v[164:167], a[220:223], a[172:175]\n"
    "ds_read_b128 v[80:83], v0 offset:33280\n"
    "v_and_b32 v225, s74, v10\n"
    "ds_read_b128 v[84:87], v0 offset:33536\n"
    "v_lshlrev_b32 v226, 16, v11\n"
    "v_pk_mul_f32 v[220:221], v[220:221], s[72:73]\n"
    "v_pk_mul_f32 v[222:223], v[222:223], s[72:73]\n"
    "v_pk_mul_f32 v[224:225], v[224:225], s[72:73]\n"
    "v_and_b32 v227, s74, v11\n"
    "v_pk_mul_f32 v[226:227], v[226:227], s[72:73]\n"
    "v_cvt_pk_bf16_f32 v8, v220, v221\n"
    "v_mfma_f32_16x16x32_bf16 a[32:35], v[168:171], a[232:235], a[32:35]\n"
    "s_waitcnt lgkmcnt(0)\n"
    "v_cvt_pk_bf16_f32 v9, v222, v223\n"
    "v_cvt_pk_bf16_f32 v10, v224, v225\n"
    "v_mfma_f32_16x16x32_bf16 a[32:35], v[172:175], a[236:239], a[32:35]\n"
    "s_waitcnt vmcnt(0)\n"
    "v_cvt_pk_bf16_f32 v11, v226, v227\n"
    "v_lshlrev_b32 v220, 16, v12\n"
    "v_mfma_f32_16x16x32_bf16 a[56:59], v[168:171], v[104:107], a[56:59]\n"
    "s_barrier\n"
    "v_and_b32 v221, s74, v12\n"
    "v_lshlrev_b32 v222, 16, v13\n"
    "v_mfma_f32_16x16x32_bf16 a[56:59], v[172:175], v[108:111], a[56:59]\n"
    "ds_read_b128 v[88:91], v0 offset:49152\n"
    "v_and_b32 v223, s74, v13\n"
    "v_mfma_f32_16x16x32_bf16 a[80:83], v[168:171], v[112:115], a[80:83]\n"
    "ds_read_b128 v[92:95], v0 offset:49408\n"
    "v_lshlrev_b32 v224, 16, v14\n"
    "v_mfma_f32_16x16x32_bf16 a[80:83], v[172:175], v[116:119], a[80:83]\n"
    "ds_read_b128 v[96:99], v0 offset:49664\n"
    "v_and_b32 v225, s74, v14\n"
    "v_lshlrev_b32 v226, 16, v15\n"
    "v_and_b32 v227, s74, v15\n"
    "ds_read_b128 v[100:103], v0 offset:49920\n"
    "v_pk_mul_f32 v[220:221], v[220:221], s[72:73]\n"
    "v_pk_mul_f32 v[222:223], v[222:223], s[72:73]\n"
    "v_pk_mul_f32 v[224:225], v[224:225], s[72:73]\n"
    "v_pk_mul_f32 v[226:227], v[226:227], s[72:73]\n"
    "v_cvt_pk_bf16_f32 v12, v220, v221\n"
    "v_cvt_pk_bf16_f32 v13, v222, v223\n"
    "v_mfma_f32_16x16x32_bf16 a[104:107], v[168:171], a[192:195], a[104:107]\n"
    "v_cvt_pk_bf16_f32 v14, v224, v225\n"
    "v_cvt_pk_bf16_f32 v15, v226, v227\n"
    "v_lshlrev_b32 v220, 16, v16\n"
    "v_and_b32 v221, s74, v16\n"
    "v_lshlrev_b32 v222, 16, v17\n"
    "v_pk_mul_f32 v[220:221], v[220:221], s[72:73]\n"
    "v_and_b32 v223, s74, v17\n"
    "v_pk_mul_f32 v[222:223], v[222:223], s[72:73]\n"
    "v_lshlrev_b32 v224, 16, v18\n"
    "v_and_b32 v225, s74, v18\n"
    "v_pk_mul_f32 v[224:225], v[224:225], s[72:73]\n"
    "v_lshlrev_b32 v226, 16, v19\n"
    "v_mfma_f32_16x16x32_bf16 a[104:107], v[172:175], a[196:199], a[104:107]\n"
    "v_and_b32 v227, s74, v19\n"
    "v_cvt_pk_bf16_f32 v16, v220, v221\n"
    "v_cvt_pk_bf16_f32 v17, v222, v223\n"
    "v_cvt_pk_bf16_f32 v18, v224, v225\n"
    "v_lshlrev_b32 v220, 16, v20\n"
    "v_pk_mul_f32 v[226:227], v[226:227], s[72:73]\n"
    "v_cvt_pk_bf16_f32 v19, v226, v227\n"
    "v_and_b32 v221, s74, v20\n"
    "v_pk_mul_f32 v[220:221], v[220:221], s[72:73]\n"
    "v_lshlrev_b32 v222, 16, v21\n"
    "v_and_b32 v223, s74, v21\n"
    "v_pk_mul_f32 v[222:223], v[222:223], s[72:73]\n"
    "v_lshlrev_b32 v224, 16, v22\n"
    "v_mfma_f32_16x16x32_bf16 a[128:131], v[168:171], a[200:203], a[128:131]\n"
    "v_and_b32 v225, s74, v22\n"
    "v_lshlrev_b32 v226, 16, v23\n"
    "v_and_b32 v227, s74, v23\n"
    "v_cvt_pk_bf16_f32 v20, v220, v221\n"
    "v_cvt_pk_bf16_f32 v21, v222, v223\n"
    "v_pk_mul_f32 v[224:225], v[224:225], s[72:73]\n"
    "v_pk_mul_f32 v[226:227], v[226:227], s[72:73]\n"
    "v_cvt_pk_bf16_f32 v22, v224, v225\n"
    "v_cvt_pk_bf16_f32 v23, v226, v227\n"
    "v_and_b32 v212, 48, v213\n"
    "v_add_u32 v212, s85, v212\n"
    "v_add_u32 v212, 65536, v212\n"
    "v_mfma_f32_16x16x32_bf16 a[128:131], v[172:175], a[204:207], a[128:131]\n"
    "ds_read_b128 v[120:123], v212 offset:0\n"
    "ds_read_b128 v[124:127], v212 offset:256\n"
    "v_mfma_f32_16x16x32_bf16 a[152:155], v[168:171], a[208:211], a[152:155]\n"
    "ds_read_b128 v[128:131], v212 offset:512\n"
    "ds_read_b128 v[132:135], v212 offset:768\n"
    "v_accvgpr_read_b32 v152, a244\n"
    "v_accvgpr_read_b32 v153, a245\n"
    "v_add_f32 v153, v152, v153\n"
    "v_rcp_f32 v152, v153\n"
    "v_accvgpr_read_b32 v200, a0\n"
    "v_max_f32 v153, v152, v153\n"
    "v_mfma_f32_16x16x4_f32 v[156:159], v152, v249, 0\n"
    "v_cmp_nge_f32 vcc, s80, v153\n"
    "v_accvgpr_read_b32 v201, a1\n"
    "v_accvgpr_read_b32 v202, a24\n"
    "v_accvgpr_read_b32 v203, a25\n"
    "v_accvgpr_read_b32 v204, a48\n"
    "v_accvgpr_read_b32 v205, a49\n"
    "v_mfma_f32_16x16x32_bf16 a[152:155], v[172:175], a[212:215], a[152:155]\n"
    "s_or_b64 s[70:71], s[70:71], vcc\n"
    "v_accvgpr_read_b32 v206, a72\n"
    "v_accvgpr_read_b32 v207, a73\n"
    "v_accvgpr_read_b32 v228, a2\n"
    "v_accvgpr_read_b32 v229, a3\n"
    "v_accvgpr_read_b32 v230, a26\n"
    "v_pk_mul_f32 v[200:201], v[200:201], v[156:157]\n"
    "v_pk_mul_f32 v[202:203], v[202:203], v[156:157]\n"
    "v_pk_mul_f32 v[204:205], v[204:205], v[156:157]\n"
    "v_pk_mul_f32 v[206:207], v[206:207], v[156:157]\n"
    "v_pk_mul_f32 v[228:229], v[228:229], v[158:159]\n"
    "v_cvt_pk_bf16_f32 v200, v200, v202\n"
    "v_mfma_f32_16x16x32_bf16 a[176:179], v[168:171], a[216:219], a[176:179]\n"
    "s_add_u32 s88, s82, 0\n"
    "v_cvt_pk_bf16_f32 v202, v201, v203\n"
    "v_cvt_pk_bf16_f32 v201, v204, v206\n"
    "v_mfma_f32_16x16x32_bf16 a[176:179], v[172:175], a[220:223], a[176:179]\n"
    "ds_write_b64 v248, v[200:201] offset:32768\n"
    "v_cvt_pk_bf16_f32 v203, v205, v207\n"
    "s_waitcnt lgkmcnt(1)\n"
    "v_mfma_f32_16x16x32_bf16 v[212:215], v[120:123], v[8:11], 0\n"
    "ds_write_b64 v248, v[202:203] offset:33024\n"
    "v_accvgpr_read_b32 v231, a27\n"
    "v_accvgpr_read_b32 v232, a50\n"
    "v_pk_mul_f32 v[230:231], v[230:231], v[158:159]\n"
    "v_accvgpr_read_b32 v233, a51\n"
    "v_pk_mul_f32 v[232:233], v[232:233], v[158:159]\n"
    "v_accvgpr_read_b32 v234, a74\n"
    "v_accvgpr_read_b32 v235, a75\n"
    "v_pk_mul_f32 v[234:235], v[234:235], v[158:159]\n"
    "v_cvt_pk_bf16_f32 v228, v228, v230\n"
    "v_mfma_f32_16x16x32_bf16 v[212:215], v[124:127], v[12:15], v[212:215]\n"
    "v_cvt_pk_bf16_f32 v230, v229, v231\n"
    "v_cvt_pk_bf16_f32 v229, v232, v234\n"
    "v_mfma_f32_16x16x32_bf16 v[212:215], v[128:131], v[16:19], v[212:215]\n"
    "ds_write_b64 v248, v[228:229] offset:33280\n"
    "v_cvt_pk_bf16_f32 v231, v233, v235\n"
    "v_mfma_f32_16x16x32_bf16 v[212:215], v[132:135], v[20:23], v[212:215]\n"
    "ds_write_b64 v248, v[230:231] offset:33536\n"
    "v_accvgpr_read_b32 v200, a96\n"
    "v_accvgpr_read_b32 v201, a97\n"
    "v_pk_mul_f32 v[200:201], v[200:201], v[156:157]\n"
    "v_accvgpr_read_b32 v202, a120\n"
    "v_accvgpr_read_b32 v203, a121\n"
    "v_pk_mul_f32 v[202:203], v[202:203], v[156:157]\n"
    "v_accvgpr_read_b32 v204, a144\n"
    "v_accvgpr_read_b32 v205, a145\n"
    "v_pk_mul_f32 v[204:205], v[204:205], v[156:157]\n"
    "v_accvgpr_read_b32 v206, a168\n"
    "v_smfmac_f32_16x16x64_bf16 v[236:239], a[240:243], v[176:183], v253\n"
    "v_accvgpr_read_b32 v207, a169\n"
    "v_cvt_pk_bf16_f32 v200, v200, v202\n"
    "v_cvt_pk_bf16_f32 v202, v201, v203\n"
    "v_accvgpr_read_b32 v228, a98\n"
    "v_pk_mul_f32 v[206:207], v[206:207], v[156:157]\n"
    "v_cvt_pk_bf16_f32 v201, v204, v206\n"
    "v_mfma_f32_16x16x32_bf16 a[12:15], v[176:179], a[224:227], a[12:15]\n"
    "ds_write_b64 v248, v[200:201] offset:32896\n"
    "v_cvt_pk_bf16_f32 v203, v205, v207\n"
    "v_mfma_f32_16x16x32_bf16 a[12:15], v[180:183], a[228:231], a[12:15]\n"
    "ds_write_b64 v248, v[202:203] offset:33152\n"
    "v_accvgpr_read_b32 v229, a99\n"
    "v_accvgpr_read_b32 v230, a122\n"
    "v_pk_mul_f32 v[228:229], v[228:229], v[158:159]\n"
    "v_accvgpr_read_b32 v231, a123\n"
    "v_pk_mul_f32 v[230:231], v[230:231], v[158:159]\n"
    "v_accvgpr_read_b32 v232, a146\n"
    "v_accvgpr_read_b32 v233, a147\n"
    "v_pk_mul_f32 v[232:233], v[232:233], v[158:159]\n"
    "v_accvgpr_read_b32 v234, a170\n"
    "v_accvgpr_read_b32 v235, a171\n"
    "v_pk_mul_f32 v[234:235], v[234:235], v[158:159]\n"
    "v_cvt_pk_bf16_f32 v228, v228, v230\n"
    "v_mfma_f32_16x16x32_bf16 a[36:39], v[176:179], a[232:235], a[36:39]\n"
    "v_cvt_pk_bf16_f32 v230, v229, v231\n"
    "v_cvt_pk_bf16_f32 v229, v232, v234\n"
    "v_mfma_f32_16x16x32_bf16 a[36:39], v[180:183], a[236:239], a[36:39]\n"
    "ds_write_b64 v248, v[228:229] offset:33408\n"
    "v_cvt_pk_bf16_f32 v231, v233, v235\n"
    "v_mfma_f32_16x16x32_bf16 a[60:63], v[176:179], v[104:107], a[60:63]\n"
    "ds_write_b64 v248, v[230:231] offset:33664\n"
    "v_mfma_f32_16x16x32_bf16 a[60:63], v[180:183], v[108:111], a[60:63]\n"
    "ds_read_b64_tr_b16 v[136:137], v254 offset:32768\n"
    "v_lshlrev_b32 v220, 16, v24\n"
    "v_mfma_f32_16x16x32_bf16 a[84:87], v[176:179], v[112:115], a[84:87]\n"
    "ds_read_b64_tr_b16 v[138:139], v254 offset:32800\n"
    "v_and_b32 v221, s74, v24\n"
    "v_mfma_f32_16x16x32_bf16 a[84:87], v[180:183], v[116:119], a[84:87]\n"
    "ds_read_b64_tr_b16 v[140:141], v254 offset:33792\n"
    "v_lshlrev_b32 v222, 16, v25\n"
    "v_mfma_f32_16x16x32_bf16 a[108:111], v[176:179], a[192:195], a[108:111]\n"
    "ds_read_b64_tr_b16 v[142:143], v254 offset:33824\n"
    "v_and_b32 v223, s74, v25\n"
    "v_mfma_f32_16x16x32_bf16 a[108:111], v[180:183], a[196:199], a[108:111]\n"
    "ds_read_b64_tr_b16 v[144:145], v254 offset:34816\n"
    "v_lshlrev_b32 v224, 16, v26\n"
    "v_mfma_f32_16x16x32_bf16 a[132:135], v[176:179], a[200:203], a[132:135]\n"
    "ds_read_b64_tr_b16 v[146:147], v254 offset:34848\n"
    "v_and_b32 v225, s74, v26\n"
    "ds_read_b64_tr_b16 v[148:149], v254 offset:35840\n"
    "ds_read_b64_tr_b16 v[150:151], v254 offset:35872\n"
    "v_pk_mul_f32 v[220:221], v[220:221], s[72:73]\n"
    "v_pk_mul_f32 v[222:223], v[222:223], s[72:73]\n"
    "v_pk_mul_f32 v[224:225], v[224:225], s[72:73]\n"
    "v_lshlrev_b32 v226, 16, v27\n"
    "v_and_b32 v227, s74, v27\n"
    "v_pk_mul_f32 v[226:227], v[226:227], s[72:73]\n"
    "v_cvt_pk_bf16_f32 v24, v220, v221\n"
    "v_cvt_pk_bf16_f32 v25, v222, v223\n"
    "v_mfma_f32_16x16x32_bf16 a[132:135], v[180:183], a[204:207], a[132:135]\n"
    "v_cvt_pk_bf16_f32 v26, v224, v225\n"
    "v_cvt_pk_bf16_f32 v27, v226, v227\n"
    "v_mfma_f32_16x16x32_bf16 a[156:159], v[176:179], a[208:211], a[156:159]\n"
    "s_waitcnt lgkmcnt(6)\n"
    "buffer_store_dwordx4 v[136:139], v252, s[52:55], s88 offen offset:0 nt\n"
    "v_lshlrev_b32 v220, 16, v28\n"
    "v_and_b32 v221, s74, v28\n"
    "v_lshlrev_b32 v222, 16, v29\n"
    "v_pk_mul_f32 v[220:221], v[220:221], s[72:73]\n"
    "v_and_b32 v223, s74, v29\n"
    "v_pk_mul_f32 v[222:223], v[222:223], s[72:73]\n"
    "v_lshlrev_b32 v224, 16, v30\n"
    "v_and_b32 v225, s74, v30\n"
    "v_pk_mul_f32 v[224:225], v[224:225], s[72:73]\n"
    "v_lshlrev_b32 v226, 16, v31\n"
    "v_and_b32 v227, s74, v31\n"
    "v_pk_mul_f32 v[226:227], v[226:227], s[72:73]\n"
    "v_cvt_pk_bf16_f32 v28, v220, v221\n"
    "v_mfma_f32_16x16x32_bf16 a[156:159], v[180:183], a[212:215], a[156:159]\n"
    "v_cvt_pk_bf16_f32 v29, v222, v223\n"
    "s_waitcnt lgkmcnt(1)\n"
    "buffer_store_dwordx4 v[140:143], v252, s[52:55], s88 offen offset:1024 nt\n"
    "v_cvt_pk_bf16_f32 v30, v224, v225\n"
    "v_cvt_pk_bf16_f32 v31, v226, v227\n"
    "v_lshlrev_b32 v220, 16, v32\n"
    "v_and_b32 v221, s74, v32\n"
    "v_pk_mul_f32 v[220:221], v[220:221], s[72:73]\n"
    "v_lshlrev_b32 v222, 16, v33\n"
    "v_and_b32 v223, s74, v33\n"
    "v_pk_mul_f32 v[222:223], v[222:223], s[72:73]\n"
    "v_lshlrev_b32 v224, 16, v34\n"
    "v_and_b32 v225, s74, v34\n"
    "v_pk_mul_f32 v[224:225], v[224:225], s[72:73]\n"
    "v_lshlrev_b32 v226, 16, v35\n"
    "v_mfma_f32_16x16x32_bf16 a[180:183], v[176:179], a[216:219], a[180:183]\n"
    "v_and_b32 v227, s74, v35\n"
    "v_cvt_pk_bf16_f32 v32, v220, v221\n"
    "v_mfma_f32_16x16x32_bf16 a[180:183], v[180:183], a[220:223], a[180:183]\n"
    "buffer_store_dwordx4 v[144:147], v252, s[52:55], s88 offen offset:2048 nt\n"
    "v_cvt_pk_bf16_f32 v33, v222, v223\n"
    "v_cvt_pk_bf16_f32 v34, v224, v225\n"
    "v_lshlrev_b32 v220, 16, v36\n"
    "v_and_b32 v221, s74, v36\n"
    "v_pk_mul_f32 v[226:227], v[226:227], s[72:73]\n"
    "v_pk_mul_f32 v[220:221], v[220:221], s[72:73]\n"
    "v_cvt_pk_bf16_f32 v35, v226, v227\n"
    "v_lshlrev_b32 v222, 16, v37\n"
    "v_and_b32 v223, s74, v37\n"
    "v_pk_mul_f32 v[222:223], v[222:223], s[72:73]\n"
    "v_lshlrev_b32 v224, 16, v38\n"
    "v_and_b32 v225, s74, v38\n"
    "v_pk_mul_f32 v[224:225], v[224:225], s[72:73]\n"
    "v_lshlrev_b32 v226, 16, v39\n"
    "v_mfma_f32_16x16x32_bf16 v[216:219], v[120:123], v[24:27], 0\n"
    "s_waitcnt lgkmcnt(0)\n"
    "buffer_store_dwordx4 v[148:151], v252, s[52:55], s88 offen offset:3072 nt\n"
    "v_mfma_f32_16x16x32_bf16 v[216:219], v[124:127], v[28:31], v[216:219]\n"
    "s_bitcmp1_b32 s58, 0\n"
    "v_and_b32 v227, s74, v39\n"
    "v_cvt_pk_bf16_f32 v36, v220, v221\n"
    "v_mfma_f32_16x16x32_bf16 v[216:219], v[128:131], v[32:35], v[216:219]\n"
    "s_cselect_b64 s[48:49], s[44:45], s[40:41]\n"
    "v_cvt_pk_bf16_f32 v37, v222, v223\n"
    "v_cvt_pk_bf16_f32 v38, v224, v225\n"
    "v_smfmac_f32_16x16x64_bf16 v[240:243], a[240:243], v[184:191], v253\n"
    "s_lshl_b32 s75, s58, 13\n"
    "s_and_b32 s75, s75, 0x3c000\n"
    "v_accvgpr_read_b32 v160, a248\n"
    "v_mfma_f32_16x16x32_bf16 a[16:19], v[184:187], a[224:227], a[16:19]\n"
    "s_add_u32 s63, s59, s75\n"
    "v_accvgpr_read_b32 v161, a249\n"
    "v_add_f32 v161, v160, v161\n"
    "v_mfma_f32_16x16x32_bf16 a[16:19], v[188:191], a[228:231], a[16:19]\n"
    "s_mov_b32 s50, s42\n"
    "v_rcp_f32 v160, v161\n"
    "v_mfma_f32_16x16x32_bf16 a[40:43], v[184:187], a[232:235], a[40:43]\n"
    "s_add_u32 s65, s61, s62\n"
    "v_max_f32 v161, v160, v161\n"
    "v_cmp_nge_f32 vcc, s80, v161\n"
    "v_mfma_f32_16x16x4_f32 v[164:167], v160, v249, 0\n"
    "s_add_u32 s61, s61, 16384\n"
    "v_accvgpr_read_b32 v200, a4\n"
    "s_cmp_eq_u32 s61, 163840\n"
    "v_accvgpr_read_b32 v201, a5\n"
    "s_cselect_b32 s61, s79, s61\n"
    "v_accvgpr_read_b32 v202, a28\n"
    "s_add_i32 s58, s58, 1\n"
    "v_mfma_f32_16x16x32_bf16 a[40:43], v[188:191], a[236:239], a[40:43]\n"
    "s_mov_b32 m0, s65\n"
    "v_accvgpr_read_b32 v203, a29\n"
    "v_accvgpr_read_b32 v204, a52\n"
    "buffer_load_dwordx4 v5, s[48:51], s63 offen nt lds\n"
    "v_accvgpr_read_b32 v205, a53\n"
    "v_pk_mul_f32 v[226:227], v[226:227], s[72:73]\n"
    "v_pk_mul_f32 v[200:201], v[200:201], v[164:165]\n"
    "v_pk_mul_f32 v[202:203], v[202:203], v[164:165]\n"
    "v_pk_mul_f32 v[204:205], v[204:205], v[164:165]\n"
    "v_cvt_pk_bf16_f32 v39, v226, v227\n"
    "v_accvgpr_read_b32 v206, a76\n"
    "v_accvgpr_read_b32 v207, a77\n"
    "v_pk_mul_f32 v[206:207], v[206:207], v[164:165]\n"
    "v_cvt_pk_bf16_f32 v200, v200, v202\n"
    "v_cvt_pk_bf16_f32 v202, v201, v203\n"
    "v_cvt_pk_bf16_f32 v201, v204, v206\n"
    "v_mfma_f32_16x16x32_bf16 v[216:219], v[132:135], v[36:39], v[216:219]\n"
    "ds_write_b64 v248, v[200:201] offset:49152\n"
    "v_mfma_f32_16x16x32_bf16 a[64:67], v[184:187], v[104:107], a[64:67]\n"
    "buffer_load_dwordx4 v6, s[48:51], s63 offen offset:1024 nt lds\n"
    "v_cvt_pk_bf16_f32 v203, v205, v207\n"
    "v_mfma_f32_16x16x32_bf16 a[64:67], v[188:191], v[108:111], a[64:67]\n"
    "ds_write_b64 v248, v[202:203] offset:49408\n"
    "v_accvgpr_read_b32 v228, a6\n"
    "v_accvgpr_read_b32 v229, a7\n"
    "v_pk_mul_f32 v[228:229], v[228:229], v[166:167]\n"
    "v_accvgpr_read_b32 v230, a30\n"
    "v_accvgpr_read_b32 v231, a31\n"
    "v_pk_mul_f32 v[230:231], v[230:231], v[166:167]\n"
    "v_accvgpr_read_b32 v232, a54\n"
    "v_accvgpr_read_b32 v233, a55\n"
    "v_pk_mul_f32 v[232:233], v[232:233], v[166:167]\n"
    "v_accvgpr_read_b32 v234, a78\n"
    "v_mfma_f32_16x16x32_bf16 a[88:91], v[184:187], v[112:115], a[88:91]\n"
    "buffer_load_dwordx4 v7, s[48:51], s63 offen offset:2048 nt lds\n"
    "v_accvgpr_read_b32 v235, a79\n"
    "v_cvt_pk_bf16_f32 v228, v228, v230\n"
    "v_cvt_pk_bf16_f32 v230, v229, v231\n"
    "v_accvgpr_read_b32 v200, a100\n"
    "v_pk_mul_f32 v[234:235], v[234:235], v[166:167]\n"
    "v_cvt_pk_bf16_f32 v229, v232, v234\n"
    "v_mfma_f32_16x16x32_bf16 a[88:91], v[188:191], v[116:119], a[88:91]\n"
    "ds_write_b64 v248, v[228:229] offset:49664\n"
    "v_cvt_pk_bf16_f32 v231, v233, v235\n"
    "v_mfma_f32_16x16x32_bf16 a[112:115], v[184:187], a[192:195], a[112:115]\n"
    "ds_write_b64 v248, v[230:231] offset:49920\n"
    "v_mfma_f32_16x16x32_bf16 a[112:115], v[188:191], a[196:199], a[112:115]\n"
    "buffer_load_dwordx4 v255, s[48:51], s63 offen offset:3072 nt lds\n"
    "s_or_b64 s[70:71], s[70:71], vcc\n"
    "v_mfma_f32_16x16x32_bf16 a[136:139], v[184:187], a[200:203], a[136:139]\n"
    "s_add_u32 s88, s82, 4096\n"
    "v_accvgpr_read_b32 v201, a101\n"
    "v_accvgpr_read_b32 v202, a124\n"
    "v_accvgpr_read_b32 v203, a125\n"
    "v_accvgpr_read_b32 v204, a148\n"
    "v_accvgpr_read_b32 v205, a149\n"
    "v_pk_mul_f32 v[200:201], v[200:201], v[164:165]\n"
    "v_pk_mul_f32 v[202:203], v[202:203], v[164:165]\n"
    "v_pk_mul_f32 v[204:205], v[204:205], v[164:165]\n"
    "v_accvgpr_read_b32 v206, a172\n"
    "v_accvgpr_read_b32 v207, a173\n"
    "v_pk_mul_f32 v[206:207], v[206:207], v[164:165]\n"
    "v_cvt_pk_bf16_f32 v200, v200, v202\n"
    "v_cvt_pk_bf16_f32 v202, v201, v203\n"
    "v_cvt_pk_bf16_f32 v201, v204, v206\n"
    "v_mfma_f32_16x16x32_bf16 a[136:139], v[188:191], a[204:207], a[136:139]\n"
    "ds_write_b64 v248, v[200:201] offset:49280\n"
    "v_cvt_pk_bf16_f32 v203, v205, v207\n"
    "v_mfma_f32_16x16x32_bf16 a[160:163], v[184:187], a[208:211], a[160:163]\n"
    "ds_write_b64 v248, v[202:203] offset:49536\n"
    "v_accvgpr_read_b32 v228, a102\n"
    "v_accvgpr_read_b32 v229, a103\n"
    "v_pk_mul_f32 v[228:229], v[228:229], v[166:167]\n"
    "v_accvgpr_read_b32 v230, a126\n"
    "v_accvgpr_read_b32 v231, a127\n"
    "v_pk_mul_f32 v[230:231], v[230:231], v[166:167]\n"
    "v_accvgpr_read_b32 v232, a150\n"
    "v_accvgpr_read_b32 v233, a151\n"
    "v_pk_mul_f32 v[232:233], v[232:233], v[166:167]\n"
    "v_accvgpr_read_b32 v234, a174\n"
    "v_accvgpr_read_b32 v235, a175\n"
    "v_pk_mul_f32 v[234:235], v[234:235], v[166:167]\n"
    "v_cvt_pk_bf16_f32 v228, v228, v230\n"
    "v_cvt_pk_bf16_f32 v230, v229, v231\n"
    "v_cvt_pk_bf16_f32 v229, v232, v234\n"
    "v_mfma_f32_16x16x32_bf16 a[160:163], v[188:191], a[212:215], a[160:163]\n"
    "ds_write_b64 v248, v[228:229] offset:49792\n"
    "v_cvt_pk_bf16_f32 v231, v233, v235\n"
    "v_mfma_f32_16x16x32_bf16 a[184:187], v[184:187], a[216:219], a[184:187]\n"
    "ds_write_b64 v248, v[230:231] offset:50048\n"
    "v_mfma_f32_16x16x32_bf16 a[184:187], v[188:191], a[220:223], a[184:187]\n"
    "ds_read_b64_tr_b16 v[136:137], v254 offset:49152\n"
    "v_lshlrev_b32 v220, 16, v40\n"
    "v_smfmac_f32_16x16x64_bf16 v[244:247], a[240:243], v[192:199], v253\n"
    "ds_read_b64_tr_b16 v[138:139], v254 offset:49184\n"
    "v_and_b32 v221, s74, v40\n"
    "v_mfma_f32_16x16x32_bf16 a[20:23], v[192:195], a[224:227], a[20:23]\n"
    "ds_read_b64_tr_b16 v[140:141], v254 offset:50176\n"
    "v_lshlrev_b32 v222, 16, v41\n"
    "v_mfma_f32_16x16x32_bf16 a[20:23], v[196:199], a[228:231], a[20:23]\n"
    "ds_read_b64_tr_b16 v[142:143], v254 offset:50208\n"
    "v_and_b32 v223, s74, v41\n"
    "v_mfma_f32_16x16x32_bf16 a[44:47], v[192:195], a[232:235], a[44:47]\n"
    "ds_read_b64_tr_b16 v[144:145], v254 offset:51200\n"
    "v_lshlrev_b32 v224, 16, v42\n"
    "v_mfma_f32_16x16x32_bf16 a[44:47], v[196:199], a[236:239], a[44:47]\n"
    "ds_read_b64_tr_b16 v[146:147], v254 offset:51232\n"
    "v_and_b32 v225, s74, v42\n"
    "ds_read_b64_tr_b16 v[148:149], v254 offset:52224\n"
    "ds_read_b64_tr_b16 v[150:151], v254 offset:52256\n"
    "v_pk_mul_f32 v[220:221], v[220:221], s[72:73]\n"
    "v_pk_mul_f32 v[222:223], v[222:223], s[72:73]\n"
    "v_pk_mul_f32 v[224:225], v[224:225], s[72:73]\n"
    "v_lshlrev_b32 v226, 16, v43\n"
    "v_and_b32 v227, s74, v43\n"
    "v_pk_mul_f32 v[226:227], v[226:227], s[72:73]\n"
    "v_cvt_pk_bf16_f32 v40, v220, v221\n"
    "v_cvt_pk_bf16_f32 v41, v222, v223\n"
    "v_cvt_pk_bf16_f32 v42, v224, v225\n"
    "v_cvt_pk_bf16_f32 v43, v226, v227\n"
    "v_lshlrev_b32 v220, 16, v44\n"
    "v_and_b32 v221, s74, v44\n"
    "v_pk_mul_f32 v[220:221], v[220:221], s[72:73]\n"
    "s_waitcnt lgkmcnt(6)\n"
    "buffer_store_dwordx4 v[136:139], v252, s[52:55], s88 offen offset:0 nt\n"
    "v_mfma_f32_16x16x32_bf16 a[68:71], v[192:195], v[104:107], a[68:71]\n"
    "v_lshlrev_b32 v222, 16, v45\n"
    "v_and_b32 v223, s74, v45\n"
    "v_lshlrev_b32 v224, 16, v46\n"
    "v_and_b32 v225, s74, v46\n"
    "v_lshlrev_b32 v226, 16, v47\n"
    "v_pk_mul_f32 v[222:223], v[222:223], s[72:73]\n"
    "v_pk_mul_f32 v[224:225], v[224:225], s[72:73]\n"
    "v_and_b32 v227, s74, v47\n"
    "v_pk_mul_f32 v[226:227], v[226:227], s[72:73]\n"
    "v_cvt_pk_bf16_f32 v44, v220, v221\n"
    "v_cvt_pk_bf16_f32 v45, v222, v223\n"
    "v_cvt_pk_bf16_f32 v46, v224, v225\n"
    "v_cvt_pk_bf16_f32 v47, v226, v227\n"
    "v_lshlrev_b32 v220, 16, v48\n"
    "v_mfma_f32_16x16x32_bf16 a[68:71], v[196:199], v[108:111], a[68:71]\n"
    "s_waitcnt lgkmcnt(0)\n"
    "buffer_store_dwordx4 v[140:143], v252, s[52:55], s88 offen offset:1024 nt\n"
    "v_and_b32 v221, s74, v48\n"
    "v_lshlrev_b32 v222, 16, v49\n"
    "v_and_b32 v223, s74, v49\n"
    "v_pk_mul_f32 v[220:221], v[220:221], s[72:73]\n"
    "v_pk_mul_f32 v[222:223], v[222:223], s[72:73]\n"
    "v_lshlrev_b32 v224, 16, v50\n"
    "v_and_b32 v225, s74, v50\n"
    "v_pk_mul_f32 v[224:225], v[224:225], s[72:73]\n"
    "v_lshlrev_b32 v226, 16, v51\n"
    "v_and_b32 v227, s74, v51\n"
    "v_pk_mul_f32 v[226:227], v[226:227], s[72:73]\n"
    "v_cvt_pk_bf16_f32 v48, v220, v221\n"
    "v_cvt_pk_bf16_f32 v49, v222, v223\n"
    "v_cvt_pk_bf16_f32 v50, v224, v225\n"
    "v_cvt_pk_bf16_f32 v51, v226, v227\n"
    "v_lshlrev_b32 v220, 16, v52\n"
    "v_mfma_f32_16x16x32_bf16 a[92:95], v[192:195], v[112:115], a[92:95]\n"
    "buffer_store_dwordx4 v[144:147], v252, s[52:55], s88 offen offset:2048 nt\n"
    "v_and_b32 v221, s74, v52\n"
    "v_lshlrev_b32 v222, 16, v53\n"
    "v_and_b32 v223, s74, v53\n"
    "v_lshlrev_b32 v224, 16, v54\n"
    "v_pk_mul_f32 v[220:221], v[220:221], s[72:73]\n"
    "v_pk_mul_f32 v[222:223], v[222:223], s[72:73]\n"
    "v_and_b32 v225, s74, v54\n"
    "v_pk_mul_f32 v[224:225], v[224:225], s[72:73]\n"
    "v_lshlrev_b32 v226, 16, v55\n"
    "v_and_b32 v227, s74, v55\n"
    "v_pk_mul_f32 v[226:227], v[226:227], s[72:73]\n"
    "v_cvt_pk_bf16_f32 v52, v220, v221\n"
    "v_cvt_pk_bf16_f32 v53, v222, v223\n"
    "v_cvt_pk_bf16_f32 v54, v224, v225\n"
    "v_cvt_pk_bf16_f32 v55, v226, v227\n"
    "v_sub_f32 v212, s83, v212\n"
    "v_mfma_f32_16x16x32_bf16 v[220:223], v[120:123], v[40:43], 0\n"
    "buffer_store_dwordx4 v[148:151], v252, s[52:55], s88 offen offset:3072 nt\n"
    "v_sub_f32 v213, s83, v213\n"
    "v_sub_f32 v214, s83, v214\n"
    "v_sub_f32 v215, s83, v215\n"
    "v_accvgpr_read_b32 v168, a252\n"
    "v_accvgpr_read_b32 v169, a253\n"
    "v_add_f32 v169, v168, v169\n"
    "v_rcp_f32 v168, v169\n"
    "v_accvgpr_read_b32 v200, a8\n"
    "v_max_f32 v169, v168, v169\n"
    "v_mfma_f32_16x16x4_f32 v[172:175], v168, v249, 0\n"
    "v_cmp_nge_f32 vcc, s80, v169\n"
    "v_accvgpr_read_b32 v201, a9\n"
    "v_accvgpr_read_b32 v202, a32\n"
    "v_accvgpr_read_b32 v203, a33\n"
    "v_accvgpr_read_b32 v204, a56\n"
    "v_accvgpr_read_b32 v205, a57\n"
    "v_mfma_f32_16x16x32_bf16 v[220:223], v[124:127], v[44:47], v[220:223]\n"
    "s_or_b64 s[70:71], s[70:71], vcc\n"
    "v_accvgpr_read_b32 v206, a80\n"
    "v_accvgpr_read_b32 v207, a81\n"
    "v_accvgpr_read_b32 v228, a10\n"
    "v_accvgpr_read_b32 v229, a11\n"
    "v_accvgpr_read_b32 v230, a34\n"
    "v_pk_mul_f32 v[200:201], v[200:201], v[172:173]\n"
    "v_pk_mul_f32 v[202:203], v[202:203], v[172:173]\n"
    "v_pk_mul_f32 v[204:205], v[204:205], v[172:173]\n"
    "v_pk_mul_f32 v[206:207], v[206:207], v[172:173]\n"
    "v_pk_mul_f32 v[228:229], v[228:229], v[174:175]\n"
    "v_cvt_pk_bf16_f32 v200, v200, v202\n"
    "v_mfma_f32_16x16x32_bf16 v[220:223], v[128:131], v[48:51], v[220:223]\n"
    "s_bitcmp1_b32 s58, 0\n"
    "v_cvt_pk_bf16_f32 v202, v201, v203\n"
    "v_cvt_pk_bf16_f32 v201, v204, v206\n"
    "v_mfma_f32_16x16x32_bf16 v[220:223], v[132:135], v[52:55], v[220:223]\n"
    "ds_write_b64 v248, v[200:201] offset:0\n"
    "v_mfma_f32_16x16x32_bf16 a[92:95], v[196:199], v[116:119], a[92:95]\n"
    "s_cselect_b64 s[48:49], s[44:45], s[40:41]\n"
    "v_cvt_pk_bf16_f32 v203, v205, v207\n"
    "v_accvgpr_read_b32 v231, a35\n"
    "v_mfma_f32_16x16x32_bf16 a[116:119], v[192:195], a[192:195], a[116:119]\n"
    "ds_write_b64 v248, v[202:203] offset:256\n"
    "v_mfma_f32_16x16x32_bf16 a[116:119], v[196:199], a[196:199], a[116:119]\n"
    "s_lshl_b32 s75, s58, 13\n"
    "v_accvgpr_read_b32 v232, a58\n"
    "v_accvgpr_read_b32 v233, a59\n"
    "v_mfma_f32_16x16x32_bf16 a[140:143], v[192:195], a[200:203], a[140:143]\n"
    "s_and_b32 s75, s75, 0x3c000\n"
    "v_accvgpr_read_b32 v234, a82\n"
    "v_accvgpr_read_b32 v235, a83\n"
    "v_accvgpr_read_b32 v200, a104\n"
    "v_accvgpr_read_b32 v201, a105\n"
    "v_accvgpr_read_b32 v202, a128\n"
    "v_pk_mul_f32 v[230:231], v[230:231], v[174:175]\n"
    "v_pk_mul_f32 v[232:233], v[232:233], v[174:175]\n"
    "v_pk_mul_f32 v[234:235], v[234:235], v[174:175]\n"
    "v_pk_mul_f32 v[200:201], v[200:201], v[172:173]\n"
    "v_cvt_pk_bf16_f32 v228, v228, v230\n"
    "v_mfma_f32_16x16x32_bf16 a[140:143], v[196:199], a[204:207], a[140:143]\n"
    "s_add_u32 s63, s59, s75\n"
    "v_cvt_pk_bf16_f32 v230, v229, v231\n"
    "v_cvt_pk_bf16_f32 v229, v232, v234\n"
    "v_mfma_f32_16x16x32_bf16 a[164:167], v[192:195], a[208:211], a[164:167]\n"
    "ds_write_b64 v248, v[228:229] offset:512\n"
    "v_mfma_f32_16x16x32_bf16 a[164:167], v[196:199], a[212:215], a[164:167]\n"
    "s_mov_b32 s50, s42\n"
    "v_cvt_pk_bf16_f32 v231, v233, v235\n"
    "v_accvgpr_read_b32 v203, a129\n"
    "v_mfma_f32_16x16x32_bf16 a[188:191], v[192:195], a[216:219], a[188:191]\n"
    "ds_write_b64 v248, v[230:231] offset:768\n"
    "v_mfma_f32_16x16x32_bf16 a[188:191], v[196:199], a[220:223], a[188:191]\n"
    "s_add_u32 s65, s61, s62\n"
    "v_accvgpr_read_b32 v204, a152\n"
    "v_accvgpr_read_b32 v205, a153\n"
    "v_accvgpr_read_b32 v206, a176\n"
    "v_accvgpr_read_b32 v207, a177\n"
    "v_accvgpr_read_b32 v228, a106\n"
    "v_pk_mul_f32 v[202:203], v[202:203], v[172:173]\n"
    "v_pk_mul_f32 v[204:205], v[204:205], v[172:173]\n"
    "v_pk_mul_f32 v[206:207], v[206:207], v[172:173]\n"
    "v_cvt_pk_bf16_f32 v200, v200, v202\n"
    "v_cvt_pk_bf16_f32 v202, v201, v203\n"
    "v_cvt_pk_bf16_f32 v201, v204, v206\n"
    "v_cvt_pk_bf16_f32 v203, v205, v207\n"
    "ds_write_b64 v248, v[200:201] offset:128\n"
    "ds_write_b64 v248, v[202:203] offset:384\n"
    "v_accvgpr_read_b32 v229, a107\n"
    "v_pk_mul_f32 v[228:229], v[228:229], v[174:175]\n"
    "v_accvgpr_read_b32 v230, a130\n"
    "v_accvgpr_read_b32 v231, a131\n"
    "v_pk_mul_f32 v[230:231], v[230:231], v[174:175]\n"
    "v_accvgpr_read_b32 v232, a154\n"
    "v_accvgpr_read_b32 v233, a155\n"
    "v_pk_mul_f32 v[232:233], v[232:233], v[174:175]\n"
    "v_accvgpr_read_b32 v234, a178\n"
    "v_accvgpr_read_b32 v235, a179\n"
    "v_pk_mul_f32 v[234:235], v[234:235], v[174:175]\n"
    "v_cvt_pk_bf16_f32 v228, v228, v230\n"
    "v_cvt_pk_bf16_f32 v230, v229, v231\n"
    "v_cvt_pk_bf16_f32 v229, v232, v234\n"
    "v_cvt_pk_bf16_f32 v231, v233, v235\n"
    "ds_write_b64 v248, v[228:229] offset:640\n"
    "ds_write_b64 v248, v[230:231] offset:896\n"
    "v_lshlrev_b32 v160, 16, v56\n"
    "v_and_b32 v161, s74, v56\n"
    "v_pk_mul_f32 v[160:161], v[160:161], s[72:73]\n"
    "v_lshlrev_b32 v162, 16, v57\n"
    "v_and_b32 v163, s74, v57\n"
    "v_pk_mul_f32 v[162:163], v[162:163], s[72:73]\n"
    "v_lshlrev_b32 v164, 16, v58\n"
    "v_and_b32 v165, s74, v58\n"
    "v_pk_mul_f32 v[164:165], v[164:165], s[72:73]\n"
    "v_lshlrev_b32 v166, 16, v59\n"
    "v_and_b32 v167, s74, v59\n"
    "v_pk_mul_f32 v[166:167], v[166:167], s[72:73]\n"
    "v_cvt_pk_bf16_f32 v56, v160, v161\n"
    "v_cvt_pk_bf16_f32 v57, v162, v163\n"
    "v_cvt_pk_bf16_f32 v58, v164, v165\n"
    "v_cvt_pk_bf16_f32 v59, v166, v167\n"
    "v_lshlrev_b32 v160, 16, v60\n"
    "v_and_b32 v161, s74, v60\n"
    "v_pk_mul_f32 v[160:161], v[160:161], s[72:73]\n"
    "v_lshlrev_b32 v162, 16, v61\n"
    "v_mfma_f32_16x16x32_bf16 v[224:227], v[120:123], v[56:59], 0\n"
    "s_add_u32 s61, s61, 16384\n"
    "v_and_b32 v163, s74, v61\n"
    "v_lshlrev_b32 v164, 16, v62\n"
    "v_and_b32 v165, s74, v62\n"
    "v_lshlrev_b32 v166, 16, v63\n"
    "v_and_b32 v167, s74, v63\n"
    "v_pk_mul_f32 v[162:163], v[162:163], s[72:73]\n"
    "v_pk_mul_f32 v[164:165], v[164:165], s[72:73]\n"
    "v_pk_mul_f32 v[166:167], v[166:167], s[72:73]\n"
    "v_cvt_pk_bf16_f32 v60, v160, v161\n"
    "v_cvt_pk_bf16_f32 v61, v162, v163\n"
    "v_cvt_pk_bf16_f32 v62, v164, v165\n"
    "v_cvt_pk_bf16_f32 v63, v166, v167\n"
    "v_lshlrev_b32 v160, 16, v64\n"
    "v_and_b32 v161, s74, v64\n"
    "v_pk_mul_f32 v[160:161], v[160:161], s[72:73]\n"
    "v_lshlrev_b32 v162, 16, v65\n"
    "v_mfma_f32_16x16x32_bf16 v[224:227], v[124:127], v[60:63], v[224:227]\n"
    "s_cmp_eq_u32 s61, 163840\n"
    "v_and_b32 v163, s74, v65\n"
    "v_lshlrev_b32 v164, 16, v66\n"
    "v_and_b32 v165, s74, v66\n"
    "v_lshlrev_b32 v166, 16, v67\n"
    "v_and_b32 v167, s74, v67\n"
    "v_pk_mul_f32 v[162:163], v[162:163], s[72:73]\n"
    "v_pk_mul_f32 v[164:165], v[164:165], s[72:73]\n"
    "v_pk_mul_f32 v[166:167], v[166:167], s[72:73]\n"
    "v_cvt_pk_bf16_f32 v64, v160, v161\n"
    "v_cvt_pk_bf16_f32 v65, v162, v163\n"
    "v_cvt_pk_bf16_f32 v66, v164, v165\n"
    "v_cvt_pk_bf16_f32 v67, v166, v167\n"
    "v_lshlrev_b32 v160, 16, v68\n"
    "v_and_b32 v161, s74, v68\n"
    "v_pk_mul_f32 v[160:161], v[160:161], s[72:73]\n"
    "v_lshlrev_b32 v162, 16, v69\n"
    "v_mfma_f32_16x16x32_bf16 v[224:227], v[128:131], v[64:67], v[224:227]\n"
    "s_cselect_b32 s61, s79, s61\n"
    "v_and_b32 v163, s74, v69\n"
    "v_lshlrev_b32 v164, 16, v70\n"
    "v_and_b32 v165, s74, v70\n"
    "v_lshlrev_b32 v166, 16, v71\n"
    "v_and_b32 v167, s74, v71\n"
    "v_pk_mul_f32 v[162:163], v[162:163], s[72:73]\n"
    "v_pk_mul_f32 v[164:165], v[164:165], s[72:73]\n"
    "v_pk_mul_f32 v[166:167], v[166:167], s[72:73]\n"
    "v_cvt_pk_bf16_f32 v68, v160, v161\n"
    "v_cvt_pk_bf16_f32 v69, v162, v163\n"
    "v_cvt_pk_bf16_f32 v70, v164, v165\n"
    "v_cvt_pk_bf16_f32 v71, v166, v167\n"
    "v_sub_f32 v216, s83, v216\n"
    "v_sub_f32 v217, s83, v217\n"
    "v_mfma_f32_16x16x32_bf16 v[224:227], v[132:135], v[68:71], v[224:227]\n"
    "s_add_i32 s58, s58, 1\n"
    "v_sub_f32 v218, s83, v218\n"
    "v_sub_f32 v219, s83, v219\n"
    "s_mov_b32 m0, s65\n"
    "v_add_f32 v177, v236, v237\n"
    "buffer_load_dwordx4 v5, s[48:51], s63 offen nt lds\n"
    "v_rcp_f32 v176, v177\n"
    "v_accvgpr_read_b32 v200, a12\n"
    "v_max_f32 v177, v176, v177\n"
    "v_mfma_f32_16x16x4_f32 v[180:183], v176, v249, 0\n"
    "v_cmp_nge_f32 vcc, s80, v177\n"
    "v_accvgpr_read_b32 v201, a13\n"
    "v_accvgpr_read_b32 v202, a36\n"
    "v_accvgpr_read_b32 v203, a37\n"
    "v_accvgpr_read_b32 v204, a60\n"
    "v_accvgpr_read_b32 v205, a61\n"
    "v_accvgpr_read_b32 v206, a84\n"
    "v_accvgpr_read_b32 v207, a85\n"
    "v_accvgpr_read_b32 v152, a14\n"
    "v_accvgpr_read_b32 v153, a15\n"
    "buffer_load_dwordx4 v6, s[48:51], s63 offen offset:1024 nt lds\n"
    "v_accvgpr_read_b32 v154, a38\n"
    "v_pk_mul_f32 v[200:201], v[200:201], v[180:181]\n"
    "v_pk_mul_f32 v[202:203], v[202:203], v[180:181]\n"
    "v_pk_mul_f32 v[204:205], v[204:205], v[180:181]\n"
    "v_pk_mul_f32 v[206:207], v[206:207], v[180:181]\n"
    "v_pk_mul_f32 v[152:153], v[152:153], v[182:183]\n"
    "v_cvt_pk_bf16_f32 v200, v200, v202\n"
    "v_cvt_pk_bf16_f32 v202, v201, v203\n"
    "v_cvt_pk_bf16_f32 v201, v204, v206\n"
    "v_cvt_pk_bf16_f32 v203, v205, v207\n"
    "ds_write_b64 v248, v[200:201] offset:16384\n"
    "ds_write_b64 v248, v[202:203] offset:16640\n"
    "buffer_load_dwordx4 v7, s[48:51], s63 offen offset:2048 nt lds\n"
    "v_accvgpr_read_b32 v155, a39\n"
    "v_pk_mul_f32 v[154:155], v[154:155], v[182:183]\n"
    "v_accvgpr_read_b32 v156, a62\n"
    "v_accvgpr_read_b32 v157, a63\n"
    "v_pk_mul_f32 v[156:157], v[156:157], v[182:183]\n"
    "v_accvgpr_read_b32 v158, a86\n"
    "v_accvgpr_read_b32 v159, a87\n"
    "v_pk_mul_f32 v[158:159], v[158:159], v[182:183]\n"
    "v_cvt_pk_bf16_f32 v152, v152, v154\n"
    "v_cvt_pk_bf16_f32 v154, v153, v155\n"
    "v_cvt_pk_bf16_f32 v153, v156, v158\n"
    "v_cvt_pk_bf16_f32 v155, v157, v159\n"
    "ds_write_b64 v248, v[152:153] offset:16896\n"
    "ds_write_b64 v248, v[154:155] offset:17152\n"
    "buffer_load_dwordx4 v255, s[48:51], s63 offen offset:3072 nt lds\n"
    "s_or_b64 s[70:71], s[70:71], vcc\n"
    "v_accvgpr_read_b32 v200, a108\n"
    "v_accvgpr_read_b32 v201, a109\n"
    "v_pk_mul_f32 v[200:201], v[200:201], v[180:181]\n"
    "v_accvgpr_read_b32 v202, a132\n"
    "v_accvgpr_read_b32 v203, a133\n"
    "v_pk_mul_f32 v[202:203], v[202:203], v[180:181]\n"
    "v_accvgpr_read_b32 v204, a156\n"
    "v_accvgpr_read_b32 v205, a157\n"
    "v_pk_mul_f32 v[204:205], v[204:205], v[180:181]\n"
    "v_accvgpr_read_b32 v206, a180\n"
    "v_accvgpr_read_b32 v207, a181\n"
    "v_pk_mul_f32 v[206:207], v[206:207], v[180:181]\n"
    "v_cvt_pk_bf16_f32 v200, v200, v202\n"
    "v_cvt_pk_bf16_f32 v202, v201, v203\n"
    "v_cvt_pk_bf16_f32 v201, v204, v206\n"
    "v_cvt_pk_bf16_f32 v203, v205, v207\n"
    "ds_write_b64 v248, v[200:201] offset:16512\n"
    "ds_write_b64 v248, v[202:203] offset:16768\n"
    "v_accvgpr_read_b32 v152, a110\n"
    "v_accvgpr_read_b32 v153, a111\n"
    "v_pk_mul_f32 v[152:153], v[152:153], v[182:183]\n"
    "v_accvgpr_read_b32 v154, a134\n"
    "v_accvgpr_read_b32 v155, a135\n"
    "v_pk_mul_f32 v[154:155], v[154:155], v[182:183]\n"
    "v_accvgpr_read_b32 v156, a158\n"
    "v_accvgpr_read_b32 v157, a159\n"
    "v_pk_mul_f32 v[156:157], v[156:157], v[182:183]\n"
    "v_accvgpr_read_b32 v158, a182\n"
    "v_accvgpr_read_b32 v159, a183\n"
    "v_pk_mul_f32 v[158:159], v[158:159], v[182:183]\n"
    "v_cvt_pk_bf16_f32 v152, v152, v154\n"
    "v_cvt_pk_bf16_f32 v154, v153, v155\n"
    "v_cvt_pk_bf16_f32 v153, v156, v158\n"
    "v_cvt_pk_bf16_f32 v155, v157, v159\n"
    "ds_write_b64 v248, v[152:153] offset:17024\n"
    "ds_write_b64 v248, v[154:155] offset:17280\n"
    "v_lshlrev_b32 v160, 16, v72\n"
    "v_and_b32 v161, s74, v72\n"
    "v_pk_mul_f32 v[160:161], v[160:161], s[72:73]\n"
    "v_lshlrev_b32 v162, 16, v73\n"
    "v_and_b32 v163, s74, v73\n"
    "v_pk_mul_f32 v[162:163], v[162:163], s[72:73]\n"
    "v_lshlrev_b32 v164, 16, v74\n"
    "v_and_b32 v165, s74, v74\n"
    "v_pk_mul_f32 v[164:165], v[164:165], s[72:73]\n"
    "v_lshlrev_b32 v166, 16, v75\n"
    "v_and_b32 v167, s74, v75\n"
    "v_pk_mul_f32 v[166:167], v[166:167], s[72:73]\n"
    "v_cvt_pk_bf16_f32 v72, v160, v161\n"
    "v_cvt_pk_bf16_f32 v73, v162, v163\n"
    "v_cvt_pk_bf16_f32 v74, v164, v165\n"
    "v_cvt_pk_bf16_f32 v75, v166, v167\n"
    "v_lshlrev_b32 v160, 16, v76\n"
    "v_and_b32 v161, s74, v76\n"
    "v_pk_mul_f32 v[160:161], v[160:161], s[72:73]\n"
    "v_lshlrev_b32 v162, 16, v77\n"
    "v_mfma_f32_16x16x32_bf16 v[228:231], v[120:123], v[72:75], 0\n"
    "v_and_b32 v163, s74, v77\n"
    "v_lshlrev_b32 v164, 16, v78\n"
    "v_and_b32 v165, s74, v78\n"
    "v_lshlrev_b32 v166, 16, v79\n"
    "v_and_b32 v167, s74, v79\n"
    "v_pk_mul_f32 v[162:163], v[162:163], s[72:73]\n"
    "v_pk_mul_f32 v[164:165], v[164:165], s[72:73]\n"
    "v_pk_mul_f32 v[166:167], v[166:167], s[72:73]\n"
    "v_cvt_pk_bf16_f32 v76, v160, v161\n"
    "v_cvt_pk_bf16_f32 v77, v162, v163\n"
    "v_cvt_pk_bf16_f32 v78, v164, v165\n"
    "v_cvt_pk_bf16_f32 v79, v166, v167\n"
    "v_lshlrev_b32 v160, 16, v80\n"
    "v_and_b32 v161, s74, v80\n"
    "v_pk_mul_f32 v[160:161], v[160:161], s[72:73]\n"
    "v_lshlrev_b32 v162, 16, v81\n"
    "v_and_b32 v163, s74, v81\n"
    "v_pk_mul_f32 v[162:163], v[162:163], s[72:73]\n"
    "v_lshlrev_b32 v164, 16, v82\n"
    "v_and_b32 v165, s74, v82\n"
    "v_pk_mul_f32 v[164:165], v[164:165], s[72:73]\n"
    "v_lshlrev_b32 v166, 16, v83\n"
    "v_and_b32 v167, s74, v83\n"
    "v_pk_mul_f32 v[166:167], v[166:167], s[72:73]\n"
    "v_cvt_pk_bf16_f32 v80, v160, v161\n"
    "v_cvt_pk_bf16_f32 v81, v162, v163\n"
    "v_cvt_pk_bf16_f32 v82, v164, v165\n"
    "v_cvt_pk_bf16_f32 v83, v166, v167\n"
    "v_lshlrev_b32 v160, 16, v84\n"
    "v_and_b32 v161, s74, v84\n"
    "v_pk_mul_f32 v[160:161], v[160:161], s[72:73]\n"
    "v_lshlrev_b32 v162, 16, v85\n"
    "v_and_b32 v163, s74, v85\n"
    "v_pk_mul_f32 v[162:163], v[162:163], s[72:73]\n"
    "v_lshlrev_b32 v164, 16, v86\n"
    "v_mfma_f32_16x16x32_bf16 v[228:231], v[124:127], v[76:79], v[228:231]\n"
    "v_and_b32 v165, s74, v86\n"
    "v_lshlrev_b32 v166, 16, v87\n"
    "v_and_b32 v167, s74, v87\n"
    "v_cvt_pk_bf16_f32 v84, v160, v161\n"
    "v_cvt_pk_bf16_f32 v85, v162, v163\n"
    "v_pk_mul_f32 v[164:165], v[164:165], s[72:73]\n"
    "v_pk_mul_f32 v[166:167], v[166:167], s[72:73]\n"
    "v_cvt_pk_bf16_f32 v86, v164, v165\n"
    "v_cvt_pk_bf16_f32 v87, v166, v167\n"
    "v_sub_f32 v220, s83, v220\n"
    "v_sub_f32 v221, s83, v221\n"
    "v_sub_f32 v222, s83, v222\n"
    "v_sub_f32 v223, s83, v223\n"
    "v_add_f32 v185, v240, v241\n"
    "v_rcp_f32 v184, v185\n"
    "v_accvgpr_read_b32 v200, a16\n"
    "v_max_f32 v185, v184, v185\n"
    "v_mfma_f32_16x16x4_f32 v[188:191], v184, v249, 0\n"
    "v_cmp_nge_f32 vcc, s80, v185\n"
    "v_accvgpr_read_b32 v201, a17\n"
    "v_accvgpr_read_b32 v202, a40\n"
    "v_accvgpr_read_b32 v203, a41\n"
    "v_accvgpr_read_b32 v204, a64\n"
    "v_accvgpr_read_b32 v205, a65\n"
    "v_mfma_f32_16x16x32_bf16 v[228:231], v[128:131], v[80:83], v[228:231]\n"
    "s_or_b64 s[70:71], s[70:71], vcc\n"
    "v_accvgpr_read_b32 v206, a88\n"
    "v_accvgpr_read_b32 v207, a89\n"
    "v_accvgpr_read_b32 v152, a18\n"
    "v_accvgpr_read_b32 v153, a19\n"
    "v_accvgpr_read_b32 v154, a42\n"
    "v_pk_mul_f32 v[200:201], v[200:201], v[188:189]\n"
    "v_pk_mul_f32 v[202:203], v[202:203], v[188:189]\n"
    "v_pk_mul_f32 v[204:205], v[204:205], v[188:189]\n"
    "v_pk_mul_f32 v[206:207], v[206:207], v[188:189]\n"
    "v_pk_mul_f32 v[152:153], v[152:153], v[190:191]\n"
    "v_cvt_pk_bf16_f32 v200, v200, v202\n"
    "v_cvt_pk_bf16_f32 v202, v201, v203\n"
    "v_cvt_pk_bf16_f32 v201, v204, v206\n"
    "v_mfma_f32_16x16x32_bf16 v[228:231], v[132:135], v[84:87], v[228:231]\n"
    "ds_write_b64 v248, v[200:201] offset:32768\n"
    "v_cvt_pk_bf16_f32 v203, v205, v207\n"
    "ds_write_b64 v248, v[202:203] offset:33024\n"
    "v_accvgpr_read_b32 v155, a43\n"
    "v_pk_mul_f32 v[154:155], v[154:155], v[190:191]\n"
    "v_accvgpr_read_b32 v156, a66\n"
    "v_accvgpr_read_b32 v157, a67\n"
    "v_pk_mul_f32 v[156:157], v[156:157], v[190:191]\n"
    "v_accvgpr_read_b32 v158, a90\n"
    "v_accvgpr_read_b32 v159, a91\n"
    "v_pk_mul_f32 v[158:159], v[158:159], v[190:191]\n"
    "v_cvt_pk_bf16_f32 v152, v152, v154\n"
    "v_cvt_pk_bf16_f32 v154, v153, v155\n"
    "v_cvt_pk_bf16_f32 v153, v156, v158\n"
    "v_cvt_pk_bf16_f32 v155, v157, v159\n"
    "ds_write_b64 v248, v[152:153] offset:33280\n"
    "ds_write_b64 v248, v[154:155] offset:33536\n"
    "v_accvgpr_read_b32 v200, a112\n"
    "v_accvgpr_read_b32 v201, a113\n"
    "v_pk_mul_f32 v[200:201], v[200:201], v[188:189]\n"
    "v_accvgpr_read_b32 v202, a136\n"
    "v_accvgpr_read_b32 v203, a137\n"
    "v_pk_mul_f32 v[202:203], v[202:203], v[188:189]\n"
    "v_accvgpr_read_b32 v204, a160\n"
    "v_accvgpr_read_b32 v205, a161\n"
    "v_pk_mul_f32 v[204:205], v[204:205], v[188:189]\n"
    "v_accvgpr_read_b32 v206, a184\n"
    "v_accvgpr_read_b32 v207, a185\n"
    "v_pk_mul_f32 v[206:207], v[206:207], v[188:189]\n"
    "v_cvt_pk_bf16_f32 v200, v200, v202\n"
    "v_cvt_pk_bf16_f32 v202, v201, v203\n"
    "v_cvt_pk_bf16_f32 v201, v204, v206\n"
    "v_cvt_pk_bf16_f32 v203, v205, v207\n"
    "ds_write_b64 v248, v[200:201] offset:32896\n"
    "ds_write_b64 v248, v[202:203] offset:33152\n"
    "v_accvgpr_read_b32 v152, a114\n"
    "v_accvgpr_read_b32 v153, a115\n"
    "v_pk_mul_f32 v[152:153], v[152:153], v[190:191]\n"
    "v_accvgpr_read_b32 v154, a138\n"
    "v_accvgpr_read_b32 v155, a139\n"
    "v_pk_mul_f32 v[154:155], v[154:155], v[190:191]\n"
    "v_accvgpr_read_b32 v156, a162\n"
    "v_accvgpr_read_b32 v157, a163\n"
    "v_pk_mul_f32 v[156:157], v[156:157], v[190:191]\n"
    "v_accvgpr_read_b32 v158, a186\n"
    "v_accvgpr_read_b32 v159, a187\n"
    "v_pk_mul_f32 v[158:159], v[158:159], v[190:191]\n"
    "v_cvt_pk_bf16_f32 v152, v152, v154\n"
    "v_cvt_pk_bf16_f32 v154, v153, v155\n"
    "v_cvt_pk_bf16_f32 v153, v156, v158\n"
    "v_cvt_pk_bf16_f32 v155, v157, v159\n"
    "ds_write_b64 v248, v[152:153] offset:33408\n"
    "ds_write_b64 v248, v[154:155] offset:33664\n"
    "v_lshlrev_b32 v160, 16, v88\n"
    "v_and_b32 v161, s74, v88\n"
    "v_pk_mul_f32 v[160:161], v[160:161], s[72:73]\n"
    "v_lshlrev_b32 v162, 16, v89\n"
    "v_and_b32 v163, s74, v89\n"
    "v_pk_mul_f32 v[162:163], v[162:163], s[72:73]\n"
    "v_lshlrev_b32 v164, 16, v90\n"
    "v_and_b32 v165, s74, v90\n"
    "v_pk_mul_f32 v[164:165], v[164:165], s[72:73]\n"
    "v_lshlrev_b32 v166, 16, v91\n"
    "v_and_b32 v167, s74, v91\n"
    "v_pk_mul_f32 v[166:167], v[166:167], s[72:73]\n"
    "v_cvt_pk_bf16_f32 v88, v160, v161\n"
    "v_cvt_pk_bf16_f32 v89, v162, v163\n"
    "v_cvt_pk_bf16_f32 v90, v164, v165\n"
    "v_cvt_pk_bf16_f32 v91, v166, v167\n"
    "v_lshlrev_b32 v160, 16, v92\n"
    "v_and_b32 v161, s74, v92\n"
    "v_pk_mul_f32 v[160:161], v[160:161], s[72:73]\n"
    "v_lshlrev_b32 v162, 16, v93\n"
    "v_mfma_f32_16x16x32_bf16 v[232:235], v[120:123], v[88:91], 0\n"
    "v_and_b32 v163, s74, v93\n"
    "v_lshlrev_b32 v164, 16, v94\n"
    "v_and_b32 v165, s74, v94\n"
    "v_lshlrev_b32 v166, 16, v95\n"
    "v_and_b32 v167, s74, v95\n"
    "v_pk_mul_f32 v[162:163], v[162:163], s[72:73]\n"
    "v_pk_mul_f32 v[164:165], v[164:165], s[72:73]\n"
    "v_pk_mul_f32 v[166:167], v[166:167], s[72:73]\n"
    "v_cvt_pk_bf16_f32 v92, v160, v161\n"
    "v_cvt_pk_bf16_f32 v93, v162, v163\n"
    "v_cvt_pk_bf16_f32 v94, v164, v165\n"
    "v_cvt_pk_bf16_f32 v95, v166, v167\n"
    "v_lshlrev_b32 v160, 16, v96\n"
    "v_and_b32 v161, s74, v96\n"
    "v_pk_mul_f32 v[160:161], v[160:161], s[72:73]\n"
    "v_lshlrev_b32 v162, 16, v97\n"
    "v_and_b32 v163, s74, v97\n"
    "v_pk_mul_f32 v[162:163], v[162:163], s[72:73]\n"
    "v_lshlrev_b32 v164, 16, v98\n"
    "v_and_b32 v165, s74, v98\n"
    "v_pk_mul_f32 v[164:165], v[164:165], s[72:73]\n"
    "v_lshlrev_b32 v166, 16, v99\n"
    "v_and_b32 v167, s74, v99\n"
    "v_pk_mul_f32 v[166:167], v[166:167], s[72:73]\n"
    "v_cvt_pk_bf16_f32 v96, v160, v161\n"
    "v_cvt_pk_bf16_f32 v97, v162, v163\n"
    "v_cvt_pk_bf16_f32 v98, v164, v165\n"
    "v_cvt_pk_bf16_f32 v99, v166, v167\n"
    "v_lshlrev_b32 v160, 16, v100\n"
    "v_and_b32 v161, s74, v100\n"
    "v_pk_mul_f32 v[160:161], v[160:161], s[72:73]\n"
    "v_lshlrev_b32 v162, 16, v101\n"
    "v_and_b32 v163, s74, v101\n"
    "v_pk_mul_f32 v[162:163], v[162:163], s[72:73]\n"
    "v_lshlrev_b32 v164, 16, v102\n"
    "v_and_b32 v165, s74, v102\n"
    "v_pk_mul_f32 v[164:165], v[164:165], s[72:73]\n"
    "v_lshlrev_b32 v166, 16, v103\n"
    "v_mfma_f32_16x16x32_bf16 v[232:235], v[124:127], v[92:95], v[232:235]\n"
    "v_and_b32 v167, s74, v103\n"
    "v_cvt_pk_bf16_f32 v100, v160, v161\n"
    "v_cvt_pk_bf16_f32 v101, v162, v163\n"
    "v_cvt_pk_bf16_f32 v102, v164, v165\n"
    "v_sub_f32 v224, s83, v224\n"
    "v_pk_mul_f32 v[166:167], v[166:167], s[72:73]\n"
    "v_cvt_pk_bf16_f32 v103, v166, v167\n"
    "v_sub_f32 v225, s83, v225\n"
    "v_sub_f32 v226, s83, v226\n"
    "v_sub_f32 v227, s83, v227\n"
    "v_add_f32 v193, v244, v245\n"
    "v_rcp_f32 v192, v193\n"
    "v_accvgpr_read_b32 v200, a20\n"
    "v_max_f32 v193, v192, v193\n"
    "v_mfma_f32_16x16x4_f32 v[196:199], v192, v249, 0\n"
    "v_cmp_nge_f32 vcc, s80, v193\n"
    "v_accvgpr_read_b32 v201, a21\n"
    "v_accvgpr_read_b32 v202, a44\n"
    "v_accvgpr_read_b32 v203, a45\n"
    "v_accvgpr_read_b32 v204, a68\n"
    "v_accvgpr_read_b32 v205, a69\n"
    "v_mfma_f32_16x16x32_bf16 v[232:235], v[128:131], v[96:99], v[232:235]\n"
    "s_or_b64 s[70:71], s[70:71], vcc\n"
    "v_accvgpr_read_b32 v206, a92\n"
    "v_accvgpr_read_b32 v207, a93\n"
    "v_accvgpr_read_b32 v152, a22\n"
    "v_accvgpr_read_b32 v153, a23\n"
    "v_accvgpr_read_b32 v154, a46\n"
    "v_pk_mul_f32 v[200:201], v[200:201], v[196:197]\n"
    "v_pk_mul_f32 v[202:203], v[202:203], v[196:197]\n"
    "v_pk_mul_f32 v[204:205], v[204:205], v[196:197]\n"
    "v_pk_mul_f32 v[206:207], v[206:207], v[196:197]\n"
    "v_pk_mul_f32 v[152:153], v[152:153], v[198:199]\n"
    "v_cvt_pk_bf16_f32 v200, v200, v202\n"
    "v_mfma_f32_16x16x32_bf16 v[232:235], v[132:135], v[100:103], v[232:235]\n"
    "s_cmp_lg_u64 s[70:71], 0\n"
    "v_cvt_pk_bf16_f32 v202, v201, v203\n"
    "v_cvt_pk_bf16_f32 v201, v204, v206\n"
    "v_cvt_pk_bf16_f32 v203, v205, v207\n"
    "ds_write_b64 v248, v[200:201] offset:49152\n"
    "ds_write_b64 v248, v[202:203] offset:49408\n"
    "v_accvgpr_read_b32 v155, a47\n"
    "v_pk_mul_f32 v[154:155], v[154:155], v[198:199]\n"
    "v_accvgpr_read_b32 v156, a70\n"
    "v_accvgpr_read_b32 v157, a71\n"
    "v_pk_mul_f32 v[156:157], v[156:157], v[198:199]\n"
    "v_accvgpr_read_b32 v158, a94\n"
    "v_accvgpr_read_b32 v159, a95\n"
    "v_pk_mul_f32 v[158:159], v[158:159], v[198:199]\n"
    "v_cvt_pk_bf16_f32 v152, v152, v154\n"
    "v_cvt_pk_bf16_f32 v154, v153, v155\n"
    "v_cvt_pk_bf16_f32 v153, v156, v158\n"
    "v_cvt_pk_bf16_f32 v155, v157, v159\n"
    "ds_write_b64 v248, v[152:153] offset:49664\n"
    "ds_write_b64 v248, v[154:155] offset:49920\n"
    "v_accvgpr_read_b32 v200, a116\n"
    "v_accvgpr_read_b32 v201, a117\n"
    "v_pk_mul_f32 v[200:201], v[200:201], v[196:197]\n"
    "v_accvgpr_read_b32 v202, a140\n"
    "v_accvgpr_read_b32 v203, a141\n"
    "v_pk_mul_f32 v[202:203], v[202:203], v[196:197]\n"
    "v_accvgpr_read_b32 v204, a164\n"
    "v_accvgpr_read_b32 v205, a165\n"
    "v_pk_mul_f32 v[204:205], v[204:205], v[196:197]\n"
    "v_accvgpr_read_b32 v206, a188\n"
    "v_accvgpr_read_b32 v207, a189\n"
    "v_pk_mul_f32 v[206:207], v[206:207], v[196:197]\n"
    "v_cvt_pk_bf16_f32 v200, v200, v202\n"
    "v_cvt_pk_bf16_f32 v202, v201, v203\n"
    "v_cvt_pk_bf16_f32 v201, v204, v206\n"
    "v_cvt_pk_bf16_f32 v203, v205, v207\n"
    "ds_write_b64 v248, v[200:201] offset:49280\n"
    "ds_write_b64 v248, v[202:203] offset:49536\n"
    "v_accvgpr_read_b32 v152, a118\n"
    "v_accvgpr_read_b32 v153, a119\n"
    "v_pk_mul_f32 v[152:153], v[152:153], v[198:199]\n"
    "v_accvgpr_read_b32 v154, a142\n"
    "v_accvgpr_read_b32 v155, a143\n"
    "v_pk_mul_f32 v[154:155], v[154:155], v[198:199]\n"
    "v_accvgpr_read_b32 v156, a166\n"
    "v_accvgpr_read_b32 v157, a167\n"
    "v_pk_mul_f32 v[156:157], v[156:157], v[198:199]\n"
    "v_accvgpr_read_b32 v158, a190\n"
    "v_accvgpr_read_b32 v159, a191\n"
    "v_pk_mul_f32 v[158:159], v[158:159], v[198:199]\n"
    "v_cvt_pk_bf16_f32 v152, v152, v154\n"
    "v_cvt_pk_bf16_f32 v154, v153, v155\n"
    "v_cvt_pk_bf16_f32 v153, v156, v158\n"
    "v_cvt_pk_bf16_f32 v155, v157, v159\n"
    "ds_write_b64 v248, v[152:153] offset:49792\n"
    "ds_write_b64 v248, v[154:155] offset:50048\n"
    "v_sub_f32 v228, s83, v228\n"
    "v_sub_f32 v229, s83, v229\n"
    "v_sub_f32 v230, s83, v230\n"
    "v_sub_f32 v231, s83, v231\n"
    "v_sub_f32 v232, s83, v232\n"
    "v_sub_f32 v233, s83, v233\n"
    "v_sub_f32 v234, s83, v234\n"
    "v_sub_f32 v235, s83, v235\n"
    "s_cselect_b32 s75, 1, 0\n"
    "s_or_b32 s81, s81, s75\n"
    "v_add_u32 v0, 65536, v3\n"
    "ds_read_b128 a[192:195], v0 offset:0\n"
    "ds_read_b128 a[196:199], v0 offset:256\n"
    "ds_read_b128 a[200:203], v0 offset:512\n"
    "ds_read_b128 a[204:207], v0 offset:768\n"
    "s_mov_b32 s66, 81920\n"
    "s_add_u32 s64, s82, 8192\n"
    "s_mov_b32 s79, 65536\n"
    "s_add_u32 s82, s82, 25165824\n"
    "s_mov_b32 s57, 1\n"
    "s_mov_b32 s91, 1\n"
    "s_mov_b32 s38, 0\n"
    "s_mov_b32 s90, 0\n"
    "s_waitcnt lgkmcnt(0)\n"
    "s_nop 3\n"
    "; ---- second head, tiles 0 and 1\n"
    "v_mfma_f32_16x16x32_bf16 v[152:155], a[192:195], v[8:11], v[212:215]\n"
    "ds_read_b128 a[208:211], v0 offset:4096\n"
    "s_bitcmp1_b32 s58, 0\n"
    "v_mfma_f32_16x16x32_bf16 v[152:155], a[196:199], v[12:15], v[152:155]\n"
    "s_cselect_b64 s[48:49], s[44:45], s[40:41]\n"
    "v_accvgpr_write_b32 a244, 0\n"
    "v_accvgpr_write_b32 a245, 0\n"
    "v_mfma_f32_16x16x32_bf16 v[152:155], a[200:203], v[16:19], v[152:155]\n"
    "s_lshl_b32 s75, s58, 13\n"
    "v_accvgpr_write_b32 a248, 0\n"
    "v_accvgpr_write_b32 a249, 0\n"
    "v_mfma_f32_16x16x32_bf16 v[152:155], a[204:207], v[20:23], v[152:155]\n"
    "s_and_b32 s75, s75, 0x3c000\n"
    "v_accvgpr_write_b32 a252, 0\n"
    "v_accvgpr_write_b32 a253, 0\n"
    "v_mfma_f32_16x16x32_bf16 v[156:159], a[204:207], v[36:39], v[216:219]\n"
    "ds_read_b128 a[212:215], v0 offset:4352\n"
    "s_add_u32 s63, s59, s75\n"
    "v_mfma_f32_16x16x32_bf16 v[156:159], a[200:203], v[32:35], v[156:159]\n"
    "s_mov_b32 s50, s42\n"
    "v_mov_b32 v236, 0\n"
    "v_mov_b32 v237, 0\n"
    "v_mfma_f32_16x16x32_bf16 v[156:159], a[196:199], v[28:31], v[156:159]\n"
    "s_add_u32 s65, s61, s62\n"
    "v_mov_b32 v240, 0\n"
    "v_mov_b32 v241, 0\n"
    "v_mfma_f32_16x16x32_bf16 v[156:159], a[192:195], v[24:27], v[156:159]\n"
    "s_add_u32 s61, s61, 16384\n"
    "v_mov_b32 v244, 0\n"
    "v_mov_b32 v245, 0\n"
    "v_mfma_f32_16x16x32_bf16 v[160:163], a[192:195], v[40:43], v[220:223]\n"
    "ds_read_b128 a[216:219], v0 offset:4608\n"
    "v_exp_f32 v152, v152\n"
    "v_mfma_f32_16x16x32_bf16 v[160:163], a[196:199], v[44:47], v[160:163]\n"
    "s_cmp_eq_u32 s61, 163840\n"
    "v_exp_f32 v153, v153\n"
    "v_mfma_f32_16x16x32_bf16 v[160:163], a[200:203], v[48:51], v[160:163]\n"
    "s_cselect_b32 s61, s79, s61\n"
    "v_exp_f32 v154, v154\n"
    "v_mfma_f32_16x16x32_bf16 v[160:163], a[204:207], v[52:55], v[160:163]\n"
    "s_add_i32 s58, s58, 1\n"
    "v_exp_f32 v155, v155\n"
    "v_cvt_pk_bf16_f32 v104, v152, v153\n"
    "v_mfma_f32_16x16x32_bf16 v[164:167], a[204:207], v[68:71], v[224:227]\n"
    "ds_read_b128 a[220:223], v0 offset:4864\n"
    "v_exp_f32 v156, v156\n"
    "v_mfma_f32_16x16x32_bf16 v[164:167], a[200:203], v[64:67], v[164:167]\n"
    "s_mov_b32 m0, s65\n"
    "v_cvt_pk_bf16_f32 v105, v154, v155\n"
    "v_exp_f32 v157, v157\n"
    "v_mfma_f32_16x16x32_bf16 v[164:167], a[196:199], v[60:63], v[164:167]\n"
    "buffer_load_dwordx4 v5, s[48:51], s63 offen nt lds\n"
    "v_exp_f32 v158, v158\n"
    "v_mfma_f32_16x16x32_bf16 v[164:167], a[192:195], v[56:59], v[164:167]\n"
    "v_exp_f32 v159, v159\n"
    "v_cvt_pk_bf16_f32 v112, v156, v157\n"
    "v_mfma_f32_16x16x32_bf16 v[152:155], a[192:195], v[72:75], v[228:231]\n"
    "v_exp_f32 v160, v160\n"
    "v_mfma_f32_16x16x32_bf16 v[152:155], a[196:199], v[76:79], v[152:155]\n"
    "v_cvt_pk_bf16_f32 v113, v158, v159\n"
    "v_exp_f32 v161, v161\n"
    "v_mfma_f32_16x16x32_bf16 v[152:155], a[200:203], v[80:83], v[152:155]\n"
    "buffer_load_dwordx4 v6, s[48:51], s63 offen offset:1024 nt lds\n"
    "v_exp_f32 v162, v162\n"
    "v_mfma_f32_16x16x32_bf16 v[152:155], a[204:207], v[84:87], v[152:155]\n"
    "v_exp_f32 v163, v163\n"
    "v_cvt_pk_bf16_f32 v120, v160, v161\n"
    "v_mfma_f32_16x16x32_bf16 v[156:159], a[204:207], v[100:103], v[232:235]\n"
    "v_exp_f32 v164, v164\n"
    "v_mfma_f32_16x16x32_bf16 v[156:159], a[200:203], v[96:99], v[156:159]\n"
    "v_cvt_pk_bf16_f32 v121, v162, v163\n"
    "v_exp_f32 v165, v165\n"
    "v_mfma_f32_16x16x32_bf16 v[156:159], a[196:199], v[92:95], v[156:159]\n"
    "buffer_load_dwordx4 v7, s[48:51], s63 offen offset:2048 nt lds\n"
    "v_exp_f32 v166, v166\n"
    "v_mfma_f32_16x16x32_bf16 v[156:159], a[192:195], v[88:91], v[156:159]\n"
    "s_waitcnt lgkmcnt(0)\n"
    "v_exp_f32 v167, v167\n"
    "v_cvt_pk_bf16_f32 v128, v164, v165\n"
    "v_mfma_f32_16x16x32_bf16 v[160:163], a[208:211], v[8:11], v[212:215]\n"
    "ds_read_b128 a[192:195], v0 offset:8192\n"
    "v_exp_f32 v152, v152\n"
    "v_mfma_f32_16x16x32_bf16 v[160:163], a[212:215], v[12:15], v[160:163]\n"
    "v_cvt_pk_bf16_f32 v129, v166, v167\n"
    "v_exp_f32 v153, v153\n"
    "v_mfma_f32_16x16x32_bf16 v[160:163], a[216:219], v[16:19], v[160:163]\n"
    "v_exp_f32 v154, v154\n"
    "v_mfma_f32_16x16x32_bf16 v[160:163], a[220:223], v[20:23], v[160:163]\n"
    "v_exp_f32 v155, v155\n"
    "v_cvt_pk_bf16_f32 v136, v152, v153\n"
    "v_mfma_f32_16x16x32_bf16 v[164:167], a[220:223], v[36:39], v[216:219]\n"
    "ds_read_b128 a[196:199], v0 offset:8448\n"
    "v_exp_f32 v156, v156\n"
    "v_mfma_f32_16x16x32_bf16 v[164:167], a[216:219], v[32:35], v[164:167]\n"
    "buffer_load_dwordx4 v255, s[48:51], s63 offen offset:3072 nt lds\n"
    "v_cvt_pk_bf16_f32 v137, v154, v155\n"
    "v_exp_f32 v157, v157\n"
    "v_mfma_f32_16x16x32_bf16 v[164:167], a[212:215], v[28:31], v[164:167]\n"
    "s_mov_b32 s68, s66\n"
    "v_exp_f32 v158, v158\n"
    "v_mfma_f32_16x16x32_bf16 v[164:167], a[208:211], v[24:27], v[164:167]\n"
    "s_add_u32 s66, s66, 16384\n"
    "v_exp_f32 v159, v159\n"
    "v_cvt_pk_bf16_f32 v144, v156, v157\n"
    "v_mfma_f32_16x16x32_bf16 v[152:155], a[208:211], v[40:43], v[220:223]\n"
    "ds_read_b128 a[200:203], v0 offset:8704\n"
    "v_exp_f32 v160, v160\n"
    "v_mfma_f32_16x16x32_bf16 v[152:155], a[212:215], v[44:47], v[152:155]\n"
    "s_cmp_eq_u32 s66, 163840\n"
    "v_cvt_pk_bf16_f32 v145, v158, v159\n"
    "v_exp_f32 v161, v161\n"
    "v_mfma_f32_16x16x32_bf16 v[152:155], a[216:219], v[48:51], v[152:155]\n"
    "s_cselect_b32 s66, s79, s66\n"
    "v_exp_f32 v162, v162\n"
    "v_mfma_f32_16x16x32_bf16 v[152:155], a[220:223], v[52:55], v[152:155]\n"
    "s_mov_b32 s67, s66\n"
    "v_exp_f32 v163, v163\n"
    "v_cvt_pk_bf16_f32 v106, v160, v161\n"
    "v_mfma_f32_16x16x32_bf16 v[156:159], a[220:223], v[68:71], v[224:227]\n"
    "ds_read_b128 a[204:207], v0 offset:8960\n"
    "v_exp_f32 v164, v164\n"
    "v_mfma_f32_16x16x32_bf16 v[156:159], a[216:219], v[64:67], v[156:159]\n"
    "s_add_u32 s66, s66, 16384\n"
    "v_cvt_pk_bf16_f32 v107, v162, v163\n"
    "v_exp_f32 v165, v165\n"
    "v_mfma_f32_16x16x32_bf16 v[156:159], a[212:215], v[60:63], v[156:159]\n"
    "s_cmp_eq_u32 s66, 163840\n"
    "v_exp_f32 v166, v166\n"
    "v_mfma_f32_16x16x32_bf16 v[156:159], a[208:211], v[56:59], v[156:159]\n"
    "s_cselect_b32 s66, s79, s66\n"
    "v_exp_f32 v167, v167\n"
    "v_cvt_pk_bf16_f32 v114, v164, v165\n"
    "v_mfma_f32_16x16x32_bf16 v[160:163], a[208:211], v[72:75], v[228:231]\n"
    "s_bitcmp1_b32 s58, 0\n"
    "v_exp_f32 v152, v152\n"
    "v_mfma_f32_16x16x32_bf16 v[160:163], a[212:215], v[76:79], v[160:163]\n"
    "s_cselect_b64 s[48:49], s[44:45], s[40:41]\n"
    "v_cvt_pk_bf16_f32 v115, v166, v167\n"
    "v_exp_f32 v153, v153\n"
    "v_mfma_f32_16x16x32_bf16 v[160:163], a[216:219], v[80:83], v[160:163]\n"
    "s_lshl_b32 s75, s58, 13\n"
    "v_exp_f32 v154, v154\n"
    "v_mfma_f32_16x16x32_bf16 v[160:163], a[220:223], v[84:87], v[160:163]\n"
    "s_and_b32 s75, s75, 0x3c000\n"
    "v_exp_f32 v155, v155\n"
    "v_cvt_pk_bf16_f32 v122, v152, v153\n"
    "v_mfma_f32_16x16x32_bf16 v[164:167], a[220:223], v[100:103], v[232:235]\n"
    "s_add_u32 s63, s59, s75\n"
    "v_exp_f32 v156, v156\n"
    "v_mfma_f32_16x16x32_bf16 v[164:167], a[216:219], v[96:99], v[164:167]\n"
    "s_mov_b32 s50, s42\n"
    "v_cvt_pk_bf16_f32 v123, v154, v155\n"
    "v_exp_f32 v157, v157\n"
    "v_mfma_f32_16x16x32_bf16 v[164:167], a[212:215], v[92:95], v[164:167]\n"
    "s_add_u32 s65, s61, s62\n"
    "v_exp_f32 v158, v158\n"
    "v_mfma_f32_16x16x32_bf16 v[164:167], a[208:211], v[88:91], v[164:167]\n"
    "s_waitcnt lgkmcnt(0)\n"
    "v_exp_f32 v159, v159\n"
    "v_cvt_pk_bf16_f32 v130, v156, v157\n"
    "v_mfma_f32_16x16x32_bf16 v[152:155], a[192:195], v[8:11], v[212:215]\n"
    "ds_read_b128 a[208:211], v0 offset:12288\n"
    "v_exp_f32 v160, v160\n"
    "v_mfma_f32_16x16x32_bf16 v[152:155], a[196:199], v[12:15], v[152:155]\n"
    "s_add_u32 s61, s61, 16384\n"
    "v_cvt_pk_bf16_f32 v131, v158, v159\n"
    "v_exp_f32 v161, v161\n"
    "v_mfma_f32_16x16x32_bf16 v[152:155], a[200:203], v[16:19], v[152:155]\n"
    "s_cmp_eq_u32 s61, 163840\n"
    "v_exp_f32 v162, v162\n"
    "v_mfma_f32_16x16x32_bf16 v[152:155], a[204:207], v[20:23], v[152:155]\n"
    "s_cselect_b32 s61, s79, s61\n"
    "v_exp_f32 v163, v163\n"
    "v_cvt_pk_bf16_f32 v138, v160, v161\n"
    "v_mfma_f32_16x16x32_bf16 v[156:159], a[204:207], v[36:39], v[216:219]\n"
    "ds_read_b128 a[212:215], v0 offset:12544\n"
    "v_exp_f32 v164, v164\n"
    "v_mfma_f32_16x16x32_bf16 v[156:159], a[200:203], v[32:35], v[156:159]\n"
    "s_add_i32 s58, s58, 1\n"
    "v_cvt_pk_bf16_f32 v139, v162, v163\n"
    "v_exp_f32 v165, v165\n"
    "v_mfma_f32_16x16x32_bf16 v[156:159], a[196:199], v[28:31], v[156:159]\n"
    "s_mov_b32 m0, s65\n"
    "v_exp_f32 v166, v166\n"
    "v_mfma_f32_16x16x32_bf16 v[156:159], a[192:195], v[24:27], v[156:159]\n"
    "buffer_load_dwordx4 v5, s[48:51], s63 offen nt lds\n"
    "v_exp_f32 v167, v167\n"
    "v_cvt_pk_bf16_f32 v146, v164, v165\n"
    "v_mfma_f32_16x16x32_bf16 v[160:163], a[192:195], v[40:43], v[220:223]\n"
    "ds_read_b128 a[216:219], v0 offset:12800\n"
    "v_exp_f32 v152, v152\n"
    "v_mfma_f32_16x16x32_bf16 v[160:163], a[196:199], v[44:47], v[160:163]\n"
    "v_cvt_pk_bf16_f32 v147, v166, v167\n"
    "v_exp_f32 v153, v153\n"
    "v_mfma_f32_16x16x32_bf16 v[160:163], a[200:203], v[48:51], v[160:163]\n"
    "v_exp_f32 v154, v154\n"
    "v_mfma_f32_16x16x32_bf16 v[160:163], a[204:207], v[52:55], v[160:163]\n"
    "buffer_load_dwordx4 v6, s[48:51], s63 offen offset:1024 nt lds\n"
    "v_exp_f32 v155, v155\n"
    "v_cvt_pk_bf16_f32 v108, v152, v153\n"
    "v_mfma_f32_16x16x32_bf16 v[164:167], a[204:207], v[68:71], v[224:227]\n"
    "ds_read_b128 a[220:223], v0 offset:13056\n"
    "v_exp_f32 v156, v156\n"
    "v_mfma_f32_16x16x32_bf16 v[164:167], a[200:203], v[64:67], v[164:167]\n"
    "v_cvt_pk_bf16_f32 v109, v154, v155\n"
    "v_exp_f32 v157, v157\n"
    "v_mfma_f32_16x16x32_bf16 v[164:167], a[196:199], v[60:63], v[164:167]\n"
    "v_exp_f32 v158, v158\n"
    "v_mfma_f32_16x16x32_bf16 v[164:167], a[192:195], v[56:59], v[164:167]\n"
    "buffer_load_dwordx4 v7, s[48:51], s63 offen offset:2048 nt lds\n"
    "v_exp_f32 v159, v159\n"
    "v_cvt_pk_bf16_f32 v116, v156, v157\n"
    "v_mfma_f32_16x16x32_bf16 v[152:155], a[192:195], v[72:75], v[228:231]\n"
    "v_exp_f32 v160, v160\n"
    "v_mfma_f32_16x16x32_bf16 v[152:155], a[196:199], v[76:79], v[152:155]\n"
    "v_cvt_pk_bf16_f32 v117, v158, v159\n"
    "v_exp_f32 v161, v161\n"
    "v_mfma_f32_16x16x32_bf16 v[152:155], a[200:203], v[80:83], v[152:155]\n"
    "v_exp_f32 v162, v162\n"
    "v_mfma_f32_16x16x32_bf16 v[152:155], a[204:207], v[84:87], v[152:155]\n"
    "buffer_load_dwordx4 v255, s[48:51], s63 offen offset:3072 nt lds\n"
    "v_exp_f32 v163, v163\n"
    "v_cvt_pk_bf16_f32 v124, v160, v161\n"
    "v_mfma_f32_16x16x32_bf16 v[156:159], a[204:207], v[100:103], v[232:235]\n"
    "v_exp_f32 v164, v164\n"
    "v_mfma_f32_16x16x32_bf16 v[156:159], a[200:203], v[96:99], v[156:159]\n"
    "v_cvt_pk_bf16_f32 v125, v162, v163\n"
    "v_exp_f32 v165, v165\n"
    "v_mfma_f32_16x16x32_bf16 v[156:159], a[196:199], v[92:95], v[156:159]\n"
    "v_exp_f32 v166, v166\n"
    "v_mfma_f32_16x16x32_bf16 v[156:159], a[192:195], v[88:91], v[156:159]\n"
    "s_waitcnt lgkmcnt(0)\n"
    "v_exp_f32 v167, v167\n"
    "v_cvt_pk_bf16_f32 v132, v164, v165\n"
    "v_mfma_f32_16x16x32_bf16 v[160:163], a[208:211], v[8:11], v[212:215]\n"
    "v_exp_f32 v152, v152\n"
    "v_mfma_f32_16x16x32_bf16 v[160:163], a[212:215], v[12:15], v[160:163]\n"
    "v_cvt_pk_bf16_f32 v133, v166, v167\n"
    "v_exp_f32 v153, v153\n"
    "v_mfma_f32_16x16x32_bf16 v[160:163], a[216:219], v[16:19], v[160:163]\n"
    "v_exp_f32 v154, v154\n"
    "v_mfma_f32_16x16x32_bf16 v[160:163], a[220:223], v[20:23], v[160:163]\n"
    "v_exp_f32 v155, v155\n"
    "v_cvt_pk_bf16_f32 v140, v152, v153\n"
    "v_mfma_f32_16x16x32_bf16 v[164:167], a[220:223], v[36:39], v[216:219]\n"
    "v_exp_f32 v156, v156\n"
    "v_mfma_f32_16x16x32_bf16 v[164:167], a[216:219], v[32:35], v[164:167]\n"
    "v_cvt_pk_bf16_f32 v141, v154, v155\n"
    "v_exp_f32 v157, v157\n"
    "v_mfma_f32_16x16x32_bf16 v[164:167], a[212:215], v[28:31], v[164:167]\n"
    "v_exp_f32 v158, v158\n"
    "v_mfma_f32_16x16x32_bf16 v[164:167], a[208:211], v[24:27], v[164:167]\n"
    "v_exp_f32 v159, v159\n"
    "v_cvt_pk_bf16_f32 v148, v156, v157\n"
    "v_mfma_f32_16x16x32_bf16 v[152:155], a[208:211], v[40:43], v[220:223]\n"
    "v_exp_f32 v160, v160\n"
    "v_add_u32 v0, s67, v3\n"
    "v_add_u32 v1, s68, v4\n"
    "v_xor_b32 v2, 32, v1\n"
    "v_mfma_f32_16x16x32_bf16 v[152:155], a[212:215], v[44:47], v[152:155]\n"
    "s_waitcnt lgkmcnt(0)\n"
    "v_cvt_pk_bf16_f32 v149, v158, v159\n"
    "v_exp_f32 v161, v161\n"
    "v_mfma_f32_16x16x32_bf16 v[152:155], a[216:219], v[48:51], v[152:155]\n"
    "s_waitcnt vmcnt(8)\n"
    "v_exp_f32 v162, v162\n"
    "v_mfma_f32_16x16x32_bf16 v[152:155], a[220:223], v[52:55], v[152:155]\n"
    "s_barrier\n"
    "v_exp_f32 v163, v163\n"
    "v_cvt_pk_bf16_f32 v110, v160, v161\n"
    "v_mfma_f32_16x16x32_bf16 v[200:203], a[220:223], v[68:71], v[224:227]\n"
    "ds_read_b128 a[192:195], v0 offset:0\n"
    "v_exp_f32 v164, v164\n"
    "v_mfma_f32_16x16x32_bf16 v[200:203], a[216:219], v[64:67], v[200:203]\n"
    "ds_read_b64_tr_b16 a[224:225], v1 offset:0\n"
    "v_cvt_pk_bf16_f32 v111, v162, v163\n"
    "v_exp_f32 v165, v165\n"
    "v_mfma_f32_16x16x32_bf16 v[200:203], a[212:215], v[60:63], v[200:203]\n"
    "ds_read_b64_tr_b16 a[226:227], v1 offset:4096\n"
    "v_exp_f32 v166, v166\n"
    "v_mfma_f32_16x16x32_bf16 v[200:203], a[208:211], v[56:59], v[200:203]\n"
    "ds_read_b128 a[196:199], v0 offset:256\n"
    "v_exp_f32 v167, v167\n"
    "v_cvt_pk_bf16_f32 v118, v164, v165\n"
    "v_mfma_f32_16x16x32_bf16 v[204:207], a[208:211], v[72:75], v[228:231]\n"
    "ds_read_b128 a[200:203], v0 offset:512\n"
    "v_exp_f32 v152, v152\n"
    "v_mfma_f32_16x16x32_bf16 v[204:207], a[212:215], v[76:79], v[204:207]\n"
    "ds_read_b64_tr_b16 a[228:229], v1 offset:8192\n"
    "v_cvt_pk_bf16_f32 v119, v166, v167\n"
    "v_exp_f32 v153, v153\n"
    "v_mfma_f32_16x16x32_bf16 v[204:207], a[216:219], v[80:83], v[204:207]\n"
    "ds_read_b64_tr_b16 a[230:231], v1 offset:12288\n"
    "v_exp_f32 v154, v154\n"
    "v_mfma_f32_16x16x32_bf16 v[204:207], a[220:223], v[84:87], v[204:207]\n"
    "ds_read_b128 a[204:207], v0 offset:768\n"
    "v_exp_f32 v155, v155\n"
    "v_cvt_pk_bf16_f32 v126, v152, v153\n"
    "v_mfma_f32_16x16x32_bf16 v[208:211], a[220:223], v[100:103], v[232:235]\n"
    "v_exp_f32 v200, v200\n"
    "v_mfma_f32_16x16x32_bf16 v[208:211], a[216:219], v[96:99], v[208:211]\n"
    "v_cvt_pk_bf16_f32 v127, v154, v155\n"
    "v_exp_f32 v201, v201\n"
    "v_mfma_f32_16x16x32_bf16 v[208:211], a[212:215], v[92:95], v[208:211]\n"
    "s_waitcnt lgkmcnt(0)\n"
    "v_exp_f32 v202, v202\n"
    "v_mfma_f32_16x16x32_bf16 v[208:211], a[208:211], v[88:91], v[208:211]\n"
    "v_exp_f32 v203, v203\n"
    "v_cvt_pk_bf16_f32 v134, v200, v201\n"
    "v_exp_f32 v204, v204\n"
    "v_exp_f32 v205, v205\n"
    "v_cvt_pk_bf16_f32 v135, v202, v203\n"
    "v_exp_f32 v206, v206\n"
    "v_cvt_pk_bf16_f32 v142, v204, v205\n"
    "v_mfma_f32_16x16x32_bf16 a[0:3], v[104:107], a[224:227], 0\n"
    "ds_read_b128 a[208:211], v0 offset:4096\n"
    "s_bitcmp1_b32 s58, 0\n"
    "v_mfma_f32_16x16x32_bf16 a[0:3], v[108:111], a[228:231], a[0:3]\n"
    "s_cselect_b64 s[48:49], s[44:45], s[40:41]\n"
    "v_exp_f32 v207, v207\n"
    "v_mfma_f32_16x16x32_bf16 a[4:7], v[116:119], a[228:231], 0\n"
    "ds_read_b64_tr_b16 a[232:233], v2 offset:0\n"
    "v_cvt_pk_bf16_f32 v143, v206, v207\n"
    "v_mfma_f32_16x16x32_bf16 a[4:7], v[112:115], a[224:227], a[4:7]\n"
    "s_lshl_b32 s75, s58, 13\n"
    "v_exp_f32 v208, v208\n"
    "v_mfma_f32_16x16x32_bf16 v[200:203], a[192:195], v[8:11], v[212:215]\n"
    "ds_read_b64_tr_b16 v[248:249], v254 offset:0\n"
    "s_and_b32 s75, s75, 0x3c000\n"
    "v_mfma_f32_16x16x32_bf16 v[200:203], a[196:199], v[12:15], v[200:203]\n"
    "s_add_u32 s63, s59, s75\n"
    "v_exp_f32 v209, v209\n"
    "v_mfma_f32_16x16x32_bf16 v[200:203], a[200:203], v[16:19], v[200:203]\n"
    "ds_read_b64_tr_b16 a[234:235], v2 offset:4096\n"
    "v_cvt_pk_bf16_f32 v150, v208, v209\n"
    "v_mfma_f32_16x16x32_bf16 v[200:203], a[204:207], v[20:23], v[200:203]\n"
    "s_mov_b32 s50, s42\n"
    "v_exp_f32 v210, v210\n"
    "v_mfma_f32_16x16x32_bf16 v[204:207], a[204:207], v[36:39], v[216:219]\n"
    "ds_read_b128 a[212:215], v0 offset:4352\n"
    "s_add_u32 s65, s61, s62\n"
    "v_mfma_f32_16x16x32_bf16 v[204:207], a[200:203], v[32:35], v[204:207]\n"
    "s_add_u32 s61, s61, 16384\n"
    "v_exp_f32 v211, v211\n"
    "v_mfma_f32_16x16x32_bf16 v[204:207], a[196:199], v[28:31], v[204:207]\n"
    "ds_read_b64_tr_b16 a[236:237], v2 offset:8192\n"
    "v_cvt_pk_bf16_f32 v151, v210, v211\n"
    "v_mfma_f32_16x16x32_bf16 v[204:207], a[192:195], v[24:27], v[204:207]\n"
    "s_cmp_eq_u32 s61, 163840\n"
    "v_exp_f32 v200, v200\n"
    "v_mfma_f32_16x16x32_bf16 a[8:11], v[120:123], a[224:227], 0\n"
    "ds_read_b64_tr_b16 v[250:251], v254 offset:32\n"
    "s_cselect_b32 s61, s79, s61\n"
    "v_mfma_f32_16x16x32_bf16 a[8:11], v[124:127], a[228:231], a[8:11]\n"
    "s_add_i32 s58, s58, 1\n"
    "v_exp_f32 v201, v201\n"
    "v_mfma_f32_16x16x32_bf16 a[12:15], v[132:135], a[228:231], 0\n"
    "ds_read_b64_tr_b16 a[238:239], v2 offset:12288\n"
    "v_cvt_pk_bf16_f32 v152, v200, v201\n"
    "v_mfma_f32_16x16x32_bf16 a[12:15], v[128:131], a[224:227], a[12:15]\n"
    "s_mov_b32 m0, s65\n"
    "v_exp_f32 v202, v202\n"
    "v_mfma_f32_16x16x32_bf16 a[16:19], v[136:139], a[224:227], 0\n"
    "ds_read_b128 a[216:219], v0 offset:4608\n"
    "v_mfma_f32_16x16x32_bf16 a[16:19], v[140:143], a[228:231], a[16:19]\n"
    "v_exp_f32 v203, v203\n"
    "v_mfma_f32_16x16x32_bf16 a[20:23], v[148:151], a[228:231], 0\n"
    "v_cvt_pk_bf16_f32 v153, v202, v203\n"
    "v_mfma_f32_16x16x32_bf16 a[20:23], v[144:147], a[224:227], a[20:23]\n"
    "v_exp_f32 v204, v204\n"
    "v_mfma_f32_16x16x32_bf16 v[208:211], a[192:195], v[40:43], v[220:223]\n"
    "buffer_load_dwordx4 v5, s[48:51], s63 offen nt lds\n"
    "v_mfma_f32_16x16x32_bf16 v[208:211], a[196:199], v[44:47], v[208:211]\n"
    "v_exp_f32 v205, v205\n"
    "v_mfma_f32_16x16x32_bf16 v[208:211], a[200:203], v[48:51], v[208:211]\n"
    "v_cvt_pk_bf16_f32 v160, v204, v205\n"
    "v_mfma_f32_16x16x32_bf16 v[208:211], a[204:207], v[52:55], v[208:211]\n"
    "v_exp_f32 v206, v206\n"
    "v_mfma_f32_16x16x32_bf16 v[200:203], a[204:207], v[68:71], v[224:227]\n"
    "ds_read_b128 a[220:223], v0 offset:4864\n"
    "v_mfma_f32_16x16x32_bf16 v[200:203], a[200:203], v[64:67], v[200:203]\n"
    "s_waitcnt lgkmcnt(3)\n"
    "v_exp_f32 v207, v207\n"
    "v_mfma_f32_16x16x32_bf16 v[200:203], a[196:199], v[60:63], v[200:203]\n"
    "ds_read_b64_tr_b16 a[224:225], v1 offset:256\n"
    "v_cvt_pk_bf16_f32 v161, v206, v207\n"
    "v_mfma_f32_16x16x32_bf16 v[200:203], a[192:195], v[56:59], v[200:203]\n"
    "s_waitcnt lgkmcnt(3)\n"
    "v_exp_f32 v208, v208\n"
    "v_mfma_f32_16x16x32_bf16 a[24:27], v[104:107], a[232:235], 0\n"
    "buffer_store_dwordx4 v[248:251], v252, s[52:55], s64 offen offset:0 nt\n"
    "v_mfma_f32_16x16x32_bf16 a[24:27], v[108:111], a[236:239], a[24:27]\n"
    "v_exp_f32 v209, v209\n"
    "v_mfma_f32_16x16x32_bf16 a[28:31], v[116:119], a[236:239], 0\n"
    "ds_read_b64_tr_b16 a[226:227], v1 offset:4352\n"
    "v_cvt_pk_bf16_f32 v168, v208, v209\n"
    "v_mfma_f32_16x16x32_bf16 a[28:31], v[112:115], a[232:235], a[28:31]\n"
    "v_exp_f32 v210, v210\n"
    "v_smfmac_f32_16x16x64_bf16 a[244:247], a[240:243], v[104:111], v253\n"
    "buffer_load_dwordx4 v6, s[48:51], s63 offen offset:1024 nt lds\n"
    "v_mfma_f32_16x16x32_bf16 a[32:35], v[120:123], a[232:235], 0\n"
    "v_mfma_f32_16x16x32_bf16 a[32:35], v[124:127], a[236:239], a[32:35]\n"
    "v_exp_f32 v211, v211\n"
    "v_mfma_f32_16x16x32_bf16 a[36:39], v[132:135], a[236:239], 0\n"
    "ds_read_b64_tr_b16 a[228:229], v1 offset:8448\n"
    "v_cvt_pk_bf16_f32 v169, v210, v211\n"
    "v_mfma_f32_16x16x32_bf16 a[36:39], v[128:131], a[232:235], a[36:39]\n"
    "v_exp_f32 v200, v200\n"
    "v_mfma_f32_16x16x32_bf16 v[204:207], a[192:195], v[72:75], v[228:231]\n"
    "v_mfma_f32_16x16x32_bf16 v[204:207], a[196:199], v[76:79], v[204:207]\n"
    "v_exp_f32 v201, v201\n"
    "v_mfma_f32_16x16x32_bf16 v[204:207], a[200:203], v[80:83], v[204:207]\n"
    "ds_read_b64_tr_b16 a[230:231], v1 offset:12544\n"
    "v_cvt_pk_bf16_f32 v176, v200, v201\n"
    "v_mfma_f32_16x16x32_bf16 v[204:207], a[204:207], v[84:87], v[204:207]\n"
    "s_nop 0\n"
    "v_exp_f32 v202, v202\n"
    "v_mfma_f32_16x16x32_bf16 v[208:211], a[204:207], v[100:103], v[232:235]\n"
    "v_mfma_f32_16x16x32_bf16 v[208:211], a[200:203], v[96:99], v[208:211]\n"
    "v_exp_f32 v203, v203\n"
    "v_mfma_f32_16x16x32_bf16 v[208:211], a[196:199], v[92:95], v[208:211]\n"
    "v_cvt_pk_bf16_f32 v177, v202, v203\n"
    "v_mfma_f32_16x16x32_bf16 v[208:211], a[192:195], v[88:91], v[208:211]\n"
    "v_exp_f32 v204, v204\n"
    "v_mfma_f32_16x16x32_bf16 a[40:43], v[136:139], a[232:235], 0\n"
    "buffer_load_dwordx4 v7, s[48:51], s63 offen offset:2048 nt lds\n"
    "v_mfma_f32_16x16x32_bf16 a[40:43], v[140:143], a[236:239], a[40:43]\n"
    "v_exp_f32 v205, v205\n"
    "v_mfma_f32_16x16x32_bf16 a[44:47], v[148:151], a[236:239], 0\n"
    "s_waitcnt lgkmcnt(0)\n"
    "v_cvt_pk_bf16_f32 v184, v204, v205\n"
    "v_mfma_f32_16x16x32_bf16 a[44:47], v[144:147], a[232:235], a[44:47]\n"
    "s_waitcnt lgkmcnt(0)\n"
    "v_exp_f32 v206, v206\n"
    "v_mfma_f32_16x16x32_bf16 a[48:51], v[104:107], a[224:227], 0\n"
    "ds_read_b128 a[192:195], v0 offset:8192\n"
    "v_mfma_f32_16x16x32_bf16 a[48:51], v[108:111], a[228:231], a[48:51]\n"
    "v_exp_f32 v207, v207\n"
    "v_mfma_f32_16x16x32_bf16 a[52:55], v[116:119], a[228:231], 0\n"
    "ds_read_b64_tr_b16 a[232:233], v2 offset:256\n"
    "v_cvt_pk_bf16_f32 v185, v206, v207\n"
    "v_mfma_f32_16x16x32_bf16 a[52:55], v[112:115], a[224:227], a[52:55]\n"
    "v_exp_f32 v208, v208\n"
    "v_mfma_f32_16x16x32_bf16 v[200:203], a[208:211], v[8:11], v[212:215]\n"
    "v_mfma_f32_16x16x32_bf16 v[200:203], a[212:215], v[12:15], v[200:203]\n"
    "v_exp_f32 v209, v209\n"
    "v_mfma_f32_16x16x32_bf16 v[200:203], a[216:219], v[16:19], v[200:203]\n"
    "ds_read_b64_tr_b16 a[234:235], v2 offset:4352\n"
    "v_cvt_pk_bf16_f32 v192, v208, v209\n"
    "v_mfma_f32_16x16x32_bf16 v[200:203], a[220:223], v[20:23], v[200:203]\n"
    "v_exp_f32 v210, v210\n"
    "v_mfma_f32_16x16x32_bf16 v[204:207], a[220:223], v[36:39], v[216:219]\n"
    "ds_read_b128 a[196:199], v0 offset:8448\n"
    "v_mfma_f32_16x16x32_bf16 v[204:207], a[216:219], v[32:35], v[204:207]\n"
    "v_exp_f32 v211, v211\n"
    "v_mfma_f32_16x16x32_bf16 v[204:207], a[212:215], v[28:31], v[204:207]\n"
    "ds_read_b64_tr_b16 a[236:237], v2 offset:8448\n"
    "v_cvt_pk_bf16_f32 v193, v210, v211\n"
    "v_mfma_f32_16x16x32_bf16 v[204:207], a[208:211], v[24:27], v[204:207]\n"
    "v_exp_f32 v200, v200\n"
    "v_mfma_f32_16x16x32_bf16 a[56:59], v[120:123], a[224:227], 0\n"
    "buffer_load_dwordx4 v255, s[48:51], s63 offen offset:3072 nt lds\n"
    "v_mfma_f32_16x16x32_bf16 a[56:59], v[124:127], a[228:231], a[56:59]\n"
    "s_mov_b32 s68, s66\n"
    "v_exp_f32 v201, v201\n"
    "v_mfma_f32_16x16x32_bf16 a[60:63], v[132:135], a[228:231], 0\n"
    "ds_read_b64_tr_b16 a[238:239], v2 offset:12544\n"
    "v_cvt_pk_bf16_f32 v154, v200, v201\n"
    "v_mfma_f32_16x16x32_bf16 a[60:63], v[128:131], a[224:227], a[60:63]\n"
    "s_add_u32 s66, s66, 16384\n"
    "v_exp_f32 v202, v202\n"
    "v_smfmac_f32_16x16x64_bf16 a[248:251], a[240:243], v[112:119], v253\n"
    "s_cmp_eq_u32 s66, 163840\n"
    "s_cselect_b32 s66, s79, s66\n"
    "s_mov_b32 s67, s66\n"
    "v_mfma_f32_16x16x32_bf16 a[64:67], v[136:139], a[224:227], 0\n"
    "ds_read_b128 a[200:203], v0 offset:8704\n"
    "s_add_u32 s66, s66, 16384\n"
    "v_mfma_f32_16x16x32_bf16 a[64:67], v[140:143], a[228:231], a[64:67]\n"
    "s_cmp_eq_u32 s66, 163840\n"
    "v_exp_f32 v203, v203\n"
    "v_mfma_f32_16x16x32_bf16 a[68:71], v[148:151], a[228:231], 0\n"
    "s_cselect_b32 s66, s79, s66\n"
    "v_cvt_pk_bf16_f32 v155, v202, v203\n"
    "s_bitcmp1_b32 s58, 0\n"
    "v_mfma_f32_16x16x32_bf16 a[68:71], v[144:147], a[224:227], a[68:71]\n"
    "s_cselect_b64 s[48:49], s[44:45], s[40:41]\n"
    "v_exp_f32 v204, v204\n"
    "v_mfma_f32_16x16x32_bf16 v[208:211], a[208:211], v[40:43], v[220:223]\n"
    "s_lshl_b32 s75, s58, 13\n"
    "s_and_b32 s75, s75, 0x3c000\n"
    "s_add_u32 s63, s59, s75\n"
    "v_mfma_f32_16x16x32_bf16 v[208:211], a[212:215], v[44:47], v[208:211]\n"
    "s_mov_b32 s50, s42\n"
    "v_exp_f32 v205, v205\n"
    "v_mfma_f32_16x16x32_bf16 v[208:211], a[216:219], v[48:51], v[208:211]\n"
    "s_add_u32 s65, s61, s62\n"
    "v_cvt_pk_bf16_f32 v162, v204, v205\n"
    "s_add_u32 s61, s61, 16384\n"
    "v_mfma_f32_16x16x32_bf16 v[208:211], a[220:223], v[52:55], v[208:211]\n"
    "s_cmp_eq_u32 s61, 163840\n"
    "v_exp_f32 v206, v206\n"
    "v_mfma_f32_16x16x32_bf16 v[200:203], a[220:223], v[68:71], v[224:227]\n"
    "ds_read_b128 a[204:207], v0 offset:8960\n"
    "s_cselect_b32 s61, s79, s61\n"
    "v_mfma_f32_16x16x32_bf16 v[200:203], a[216:219], v[64:67], v[200:203]\n"
    "s_add_i32 s58, s58, 1\n"
    "v_exp_f32 v207, v207\n"
    "v_mfma_f32_16x16x32_bf16 v[200:203], a[212:215], v[60:63], v[200:203]\n"
    "ds_read_b64_tr_b16 a[224:225], v1 offset:512\n"
    "v_cvt_pk_bf16_f32 v163, v206, v207\n"
    "v_mfma_f32_16x16x32_bf16 v[200:203], a[208:211], v[56:59], v[200:203]\n"
    "s_waitcnt lgkmcnt(3)\n"
    "v_exp_f32 v208, v208\n"
    "v_mfma_f32_16x16x32_bf16 a[72:75], v[104:107], a[232:235], 0\n"
    "s_mov_b32 m0, s65\n"
    "v_mfma_f32_16x16x32_bf16 a[72:75], v[108:111], a[236:239], a[72:75]\n"
    "v_exp_f32 v209, v209\n"
    "v_mfma_f32_16x16x32_bf16 a[76:79], v[116:119], a[236:239], 0\n"
    "ds_read_b64_tr_b16 a[226:227], v1 offset:4608\n"
    "v_cvt_pk_bf16_f32 v170, v208, v209\n"
    "v_mfma_f32_16x16x32_bf16 a[76:79], v[112:115], a[232:235], a[76:79]\n"
    "v_exp_f32 v210, v210\n"
    "v_mfma_f32_16x16x32_bf16 a[80:83], v[120:123], a[232:235], 0\n"
    "buffer_load_dwordx4 v5, s[48:51], s63 offen nt lds\n"
    "v_mfma_f32_16x16x32_bf16 a[80:83], v[124:127], a[236:239], a[80:83]\n"
    "v_exp_f32 v211, v211\n"
    "v_mfma_f32_16x16x32_bf16 a[84:87], v[132:135], a[236:239], 0\n"
    "ds_read_b64_tr_b16 a[228:229], v1 offset:8704\n"
    "v_cvt_pk_bf16_f32 v171, v210, v211\n"
    "v_mfma_f32_16x16x32_bf16 a[84:87], v[128:131], a[232:235], a[84:87]\n"
    "v_exp_f32 v200, v200\n"
    "v_mfma_f32_16x16x32_bf16 v[204:207], a[208:211], v[72:75], v[228:231]\n"
    "ds_read_b64_tr_b16 v[248:249], v254 offset:1024\n"
    "v_mfma_f32_16x16x32_bf16 v[204:207], a[212:215], v[76:79], v[204:207]\n"
    "v_exp_f32 v201, v201\n"
    "v_mfma_f32_16x16x32_bf16 v[204:207], a[216:219], v[80:83], v[204:207]\n"
    "ds_read_b64_tr_b16 a[230:231], v1 offset:12800\n"
    "v_cvt_pk_bf16_f32 v178, v200, v201\n"
    "v_mfma_f32_16x16x32_bf16 v[204:207], a[220:223], v[84:87], v[204:207]\n"
    "v_exp_f32 v202, v202\n"
    "v_mfma_f32_16x16x32_bf16 v[208:211], a[220:223], v[100:103], v[232:235]\n"
    "ds_read_b64_tr_b16 v[250:251], v254 offset:1056\n"
    "v_mfma_f32_16x16x32_bf16 v[208:211], a[216:219], v[96:99], v[208:211]\n"
    "v_exp_f32 v203, v203\n"
    "v_mfma_f32_16x16x32_bf16 v[208:211], a[212:215], v[92:95], v[208:211]\n"
    "v_cvt_pk_bf16_f32 v179, v202, v203\n"
    "v_mfma_f32_16x16x32_bf16 v[208:211], a[208:211], v[88:91], v[208:211]\n"
    "v_exp_f32 v204, v204\n"
    "v_mfma_f32_16x16x32_bf16 a[88:91], v[136:139], a[232:235], 0\n"
    "buffer_load_dwordx4 v6, s[48:51], s63 offen offset:1024 nt lds\n"
    "v_mfma_f32_16x16x32_bf16 a[88:91], v[140:143], a[236:239], a[88:91]\n"
    "v_exp_f32 v205, v205\n"
    "v_mfma_f32_16x16x32_bf16 a[92:95], v[148:151], a[236:239], 0\n"
    "v_cvt_pk_bf16_f32 v186, v204, v205\n"
    "v_mfma_f32_16x16x32_bf16 a[92:95], v[144:147], a[232:235], a[92:95]\n"
    "s_waitcnt lgkmcnt(1)\n"
    "v_exp_f32 v206, v206\n"
    "v_smfmac_f32_16x16x64_bf16 a[252:255], a[240:243], v[120:127], v253\n"
    "s_waitcnt lgkmcnt(1)\n"
    "v_mfma_f32_16x16x32_bf16 a[96:99], v[104:107], a[224:227], 0\n"
    "ds_read_b128 a[208:211], v0 offset:12288\n"
    "v_mfma_f32_16x16x32_bf16 a[96:99], v[108:111], a[228:231], a[96:99]\n"
    "v_exp_f32 v207, v207\n"
    "v_mfma_f32_16x16x32_bf16 a[100:103], v[116:119], a[228:231], 0\n"
    "ds_read_b64_tr_b16 a[232:233], v2 offset:512\n"
    "v_cvt_pk_bf16_f32 v187, v206, v207\n"
    "v_mfma_f32_16x16x32_bf16 a[100:103], v[112:115], a[224:227], a[100:103]\n"
    "s_waitcnt lgkmcnt(2)\n"
    "v_exp_f32 v208, v208\n"
    "v_mfma_f32_16x16x32_bf16 v[200:203], a[192:195], v[8:11], v[212:215]\n"
    "buffer_store_dwordx4 v[248:251], v252, s[52:55], s64 offen offset:1024 nt\n"
    "v_mfma_f32_16x16x32_bf16 v[200:203], a[196:199], v[12:15], v[200:203]\n"
    "v_exp_f32 v209, v209\n"
    "v_mfma_f32_16x16x32_bf16 v[200:203], a[200:203], v[16:19], v[200:203]\n"
    "ds_read_b64_tr_b16 a[234:235], v2 offset:4608\n"
    "v_cvt_pk_bf16_f32 v194, v208, v209\n"
    "v_mfma_f32_16x16x32_bf16 v[200:203], a[204:207], v[20:23], v[200:203]\n"
    "v_exp_f32 v210, v210\n"
    "v_mfma_f32_16x16x32_bf16 v[204:207], a[204:207], v[36:39], v[216:219]\n"
    "ds_read_b128 a[212:215], v0 offset:12544\n"
    "v_mfma_f32_16x16x32_bf16 v[204:207], a[200:203], v[32:35], v[204:207]\n"
    "v_exp_f32 v211, v211\n"
    "v_mfma_f32_16x16x32_bf16 v[204:207], a[196:199], v[28:31], v[204:207]\n"
    "ds_read_b64_tr_b16 a[236:237], v2 offset:8704\n"
    "v_cvt_pk_bf16_f32 v195, v210, v211\n"
    "v_mfma_f32_16x16x32_bf16 v[204:207], a[192:195], v[24:27], v[204:207]\n"
    "v_exp_f32 v200, v200\n"
    "v_mfma_f32_16x16x32_bf16 a[104:107], v[120:123], a[224:227], 0\n"
    "buffer_load_dwordx4 v7, s[48:51], s63 offen offset:2048 nt lds\n"
    "v_mfma_f32_16x16x32_bf16 a[104:107], v[124:127], a[228:231], a[104:107]\n"
    "v_exp_f32 v201, v201\n"
    "v_mfma_f32_16x16x32_bf16 a[108:111], v[132:135], a[228:231], 0\n"
    "ds_read_b64_tr_b16 a[238:239], v2 offset:12800\n"
    "v_cvt_pk_bf16_f32 v156, v200, v201\n"
    "v_mfma_f32_16x16x32_bf16 a[108:111], v[128:131], a[224:227], a[108:111]\n"
    "v_exp_f32 v202, v202\n"
    "v_mfma_f32_16x16x32_bf16 a[112:115], v[136:139], a[224:227], 0\n"
    "ds_read_b128 a[216:219], v0 offset:12800\n"
    "v_mfma_f32_16x16x32_bf16 a[112:115], v[140:143], a[228:231], a[112:115]\n"
    "v_exp_f32 v203, v203\n"
    "v_mfma_f32_16x16x32_bf16 a[116:119], v[148:151], a[228:231], 0\n"
    "v_cvt_pk_bf16_f32 v157, v202, v203\n"
    "v_add_u32 v254, s78, v254\n"
    "v_mfma_f32_16x16x32_bf16 a[116:119], v[144:147], a[224:227], a[116:119]\n"
    "v_exp_f32 v204, v204\n"
    "v_mfma_f32_16x16x32_bf16 v[208:211], a[192:195], v[40:43], v[220:223]\n"
    "v_mfma_f32_16x16x32_bf16 v[208:211], a[196:199], v[44:47], v[208:211]\n"
    "v_exp_f32 v205, v205\n"
    "v_mfma_f32_16x16x32_bf16 v[208:211], a[200:203], v[48:51], v[208:211]\n"
    "v_cvt_pk_bf16_f32 v164, v204, v205\n"
    "v_mfma_f32_16x16x32_bf16 v[208:211], a[204:207], v[52:55], v[208:211]\n"
    "v_exp_f32 v206, v206\n"
    "v_mfma_f32_16x16x32_bf16 v[200:203], a[204:207], v[68:71], v[224:227]\n"
    "ds_read_b128 a[220:223], v0 offset:13056\n"
    "v_mfma_f32_16x16x32_bf16 v[200:203], a[200:203], v[64:67], v[200:203]\n"
    "v_exp_f32 v207, v207\n"
    "v_mfma_f32_16x16x32_bf16 v[200:203], a[196:199], v[60:63], v[200:203]\n"
    "ds_read_b64_tr_b16 a[224:225], v1 offset:768\n"
    "v_cvt_pk_bf16_f32 v165, v206, v207\n"
    "v_mfma_f32_16x16x32_bf16 v[200:203], a[192:195], v[56:59], v[200:203]\n"
    "s_waitcnt lgkmcnt(3)\n"
    "v_exp_f32 v208, v208\n"
    "v_mfma_f32_16x16x32_bf16 a[120:123], v[104:107], a[232:235], 0\n"
    "v_mfma_f32_16x16x32_bf16 a[120:123], v[108:111], a[236:239], a[120:123]\n"
    "v_exp_f32 v209, v209\n"
    "v_mfma_f32_16x16x32_bf16 a[124:127], v[116:119], a[236:239], 0\n"
    "ds_read_b64_tr_b16 a[226:227], v1 offset:4864\n"
    "v_cvt_pk_bf16_f32 v172, v208, v209\n"
    "v_mfma_f32_16x16x32_bf16 a[124:127], v[112:115], a[232:235], a[124:127]\n"
    "v_exp_f32 v210, v210\n"
    "v_smfmac_f32_16x16x64_bf16 v[236:239], a[240:243], v[128:135], v253\n"
    "buffer_load_dwordx4 v255, s[48:51], s63 offen offset:3072 nt lds\n"
    "v_mfma_f32_16x16x32_bf16 a[128:131], v[120:123], a[232:235], 0\n"
    "v_mfma_f32_16x16x32_bf16 a[128:131], v[124:127], a[236:239], a[128:131]\n"
    "v_exp_f32 v211, v211\n"
    "v_mfma_f32_16x16x32_bf16 a[132:135], v[132:135], a[236:239], 0\n"
    "ds_read_b64_tr_b16 a[228:229], v1 offset:8960\n"
    "v_cvt_pk_bf16_f32 v173, v210, v211\n"
    "v_mfma_f32_16x16x32_bf16 a[132:135], v[128:131], a[232:235], a[132:135]\n"
    "v_exp_f32 v200, v200\n"
    "v_mfma_f32_16x16x32_bf16 v[204:207], a[192:195], v[72:75], v[228:231]\n"
    "v_mfma_f32_16x16x32_bf16 v[204:207], a[196:199], v[76:79], v[204:207]\n"
    "v_exp_f32 v201, v201\n"
    "v_mfma_f32_16x16x32_bf16 v[204:207], a[200:203], v[80:83], v[204:207]\n"
    "ds_read_b64_tr_b16 a[230:231], v1 offset:13056\n"
    "v_cvt_pk_bf16_f32 v180, v200, v201\n"
    "v_mfma_f32_16x16x32_bf16 v[204:207], a[204:207], v[84:87], v[204:207]\n"
    "s_nop 0\n"
    "v_exp_f32 v202, v202\n"
    "v_mfma_f32_16x16x32_bf16 v[208:211], a[204:207], v[100:103], v[232:235]\n"
    "v_mfma_f32_16x16x32_bf16 v[208:211], a[200:203], v[96:99], v[208:211]\n"
    "v_exp_f32 v203, v203\n"
    "v_mfma_f32_16x16x32_bf16 v[208:211], a[196:199], v[92:95], v[208:211]\n"
    "v_cvt_pk_bf16_f32 v181, v202, v203\n"
    "v_mfma_f32_16x16x32_bf16 v[208:211], a[192:195], v[88:91], v[208:211]\n"
    "v_exp_f32 v204, v204\n"
    "v_mfma_f32_16x16x32_bf16 a[136:139], v[136:139], a[232:235], 0\n"
    "v_mfma_f32_16x16x32_bf16 a[136:139], v[140:143], a[236:239], a[136:139]\n"
    "v_exp_f32 v205, v205\n"
    "v_mfma_f32_16x16x32_bf16 a[140:143], v[148:151], a[236:239], 0\n"
    "s_waitcnt lgkmcnt(0)\n"
    "v_cvt_pk_bf16_f32 v188, v204, v205\n"
    "v_mfma_f32_16x16x32_bf16 a[140:143], v[144:147], a[232:235], a[140:143]\n"
    "s_waitcnt lgkmcnt(0)\n"
    "v_exp_f32 v206, v206\n"
    "v_mfma_f32_16x16x32_bf16 a[144:147], v[104:107], a[224:227], 0\n"
    "v_mfma_f32_16x16x32_bf16 a[144:147], v[108:111], a[228:231], a[144:147]\n"
    "v_exp_f32 v207, v207\n"
    "v_mfma_f32_16x16x32_bf16 a[148:151], v[116:119], a[228:231], 0\n"
    "ds_read_b64_tr_b16 a[232:233], v2 offset:768\n"
    "v_cvt_pk_bf16_f32 v189, v206, v207\n"
    "v_mfma_f32_16x16x32_bf16 a[148:151], v[112:115], a[224:227], a[148:151]\n"
    "v_exp_f32 v208, v208\n"
    "v_mfma_f32_16x16x32_bf16 v[200:203], a[208:211], v[8:11], v[212:215]\n"
    "v_mfma_f32_16x16x32_bf16 v[200:203], a[212:215], v[12:15], v[200:203]\n"
    "v_exp_f32 v209, v209\n"
    "v_mfma_f32_16x16x32_bf16 v[200:203], a[216:219], v[16:19], v[200:203]\n"
    "ds_read_b64_tr_b16 a[234:235], v2 offset:4864\n"
    "v_cvt_pk_bf16_f32 v196, v208, v209\n"
    "v_mfma_f32_16x16x32_bf16 v[200:203], a[220:223], v[20:23], v[200:203]\n"
    "v_exp_f32 v210, v210\n"
    "v_mfma_f32_16x16x32_bf16 v[204:207], a[220:223], v[36:39], v[216:219]\n"
    "v_mfma_f32_16x16x32_bf16 v[204:207], a[216:219], v[32:35], v[204:207]\n"
    "v_exp_f32 v211, v211\n"
    "v_mfma_f32_16x16x32_bf16 v[204:207], a[212:215], v[28:31], v[204:207]\n"
    "ds_read_b64_tr_b16 a[236:237], v2 offset:8960\n"
    "v_cvt_pk_bf16_f32 v197, v210, v211\n"
    "v_mfma_f32_16x16x32_bf16 v[204:207], a[208:211], v[24:27], v[204:207]\n"
    "v_exp_f32 v200, v200\n"
    "v_mfma_f32_16x16x32_bf16 a[152:155], v[120:123], a[224:227], 0\n"
    "v_mfma_f32_16x16x32_bf16 a[152:155], v[124:127], a[228:231], a[152:155]\n"
    "v_exp_f32 v201, v201\n"
    "v_mfma_f32_16x16x32_bf16 a[156:159], v[132:135], a[228:231], 0\n"
    "ds_read_b64_tr_b16 a[238:239], v2 offset:13056\n"
    "v_cvt_pk_bf16_f32 v158, v200, v201\n"
    "v_mfma_f32_16x16x32_bf16 a[156:159], v[128:131], a[224:227], a[156:159]\n"
    "v_exp_f32 v202, v202\n"
    "v_smfmac_f32_16x16x64_bf16 v[240:243], a[240:243], v[136:143], v253\n"
    "v_mfma_f32_16x16x32_bf16 a[160:163], v[136:139], a[224:227], 0\n"
    "v_add_u32 v0, s67, v3\n"
    "v_add_u32 v1, s68, v4\n"
    "v_mfma_f32_16x16x32_bf16 a[160:163], v[140:143], a[228:231], a[160:163]\n"
    "v_exp_f32 v203, v203\n"
    "v_mfma_f32_16x16x32_bf16 a[164:167], v[148:151], a[228:231], 0\n"
    "v_cvt_pk_bf16_f32 v159, v202, v203\n"
    "v_mfma_f32_16x16x32_bf16 a[164:167], v[144:147], a[224:227], a[164:167]\n"
    "s_waitcnt lgkmcnt(0)\n"
    "v_exp_f32 v204, v204\n"
    "v_mfma_f32_16x16x32_bf16 v[208:211], a[208:211], v[40:43], v[220:223]\n"
    "v_xor_b32 v2, 32, v1\n"
    "v_mfma_f32_16x16x32_bf16 v[208:211], a[212:215], v[44:47], v[208:211]\n"
    "s_waitcnt vmcnt(10)\n"
    "v_exp_f32 v205, v205\n"
    "v_mfma_f32_16x16x32_bf16 v[208:211], a[216:219], v[48:51], v[208:211]\n"
    "v_cvt_pk_bf16_f32 v166, v204, v205\n"
    "v_mfma_f32_16x16x32_bf16 v[208:211], a[220:223], v[52:55], v[208:211]\n"
    "s_barrier\n"
    "v_exp_f32 v206, v206\n"
    "v_mfma_f32_16x16x32_bf16 v[200:203], a[220:223], v[68:71], v[224:227]\n"
    "ds_read_b128 a[192:195], v0 offset:0\n"
    "v_mfma_f32_16x16x32_bf16 v[200:203], a[216:219], v[64:67], v[200:203]\n"
    "v_exp_f32 v207, v207\n"
    "v_mfma_f32_16x16x32_bf16 v[200:203], a[212:215], v[60:63], v[200:203]\n"
    "ds_read_b64_tr_b16 a[224:225], v1 offset:0\n"
    "v_cvt_pk_bf16_f32 v167, v206, v207\n"
    "v_mfma_f32_16x16x32_bf16 v[200:203], a[208:211], v[56:59], v[200:203]\n"
    "v_exp_f32 v208, v208\n"
    "v_mfma_f32_16x16x32_bf16 a[168:171], v[104:107], a[232:235], 0\n"
    "ds_read_b128 a[196:199], v0 offset:256\n"
    "v_mfma_f32_16x16x32_bf16 a[168:171], v[108:111], a[236:239], a[168:171]\n"
    "v_exp_f32 v209, v209\n"
    "v_mfma_f32_16x16x32_bf16 a[172:175], v[116:119], a[236:239], 0\n"
    "ds_read_b64_tr_b16 a[226:227], v1 offset:4096\n"
    "v_cvt_pk_bf16_f32 v174, v208, v209\n"
    "v_mfma_f32_16x16x32_bf16 a[172:175], v[112:115], a[232:235], a[172:175]\n"
    "v_exp_f32 v210, v210\n"
    "v_mfma_f32_16x16x32_bf16 a[176:179], v[120:123], a[232:235], 0\n"
    "ds_read_b128 a[200:203], v0 offset:512\n"
    "s_xor_b32 s78, s78, 12288\n"
    "v_mfma_f32_16x16x32_bf16 a[176:179], v[124:127], a[236:239], a[176:179]\n"
    "s_add_u32 s64, s64, 2048\n"
    "v_exp_f32 v211, v211\n"
    "v_mfma_f32_16x16x32_bf16 a[180:183], v[132:135], a[236:239], 0\n"
    "ds_read_b64_tr_b16 a[228:229], v1 offset:8192\n"
    "v_cvt_pk_bf16_f32 v175, v210, v211\n"
    "v_mfma_f32_16x16x32_bf16 a[180:183], v[128:131], a[232:235], a[180:183]\n"
    "s_mov_b32 s69, 7\n"
    "v_exp_f32 v200, v200\n"
    "v_mfma_f32_16x16x32_bf16 v[204:207], a[208:211], v[72:75], v[228:231]\n"
    "ds_read_b128 a[204:207], v0 offset:768\n"
    "v_mfma_f32_16x16x32_bf16 v[204:207], a[212:215], v[76:79], v[204:207]\n"
    "v_exp_f32 v201, v201\n"
    "v_mfma_f32_16x16x32_bf16 v[204:207], a[216:219], v[80:83], v[204:207]\n"
    "ds_read_b64_tr_b16 a[230:231], v1 offset:12288\n"
    "v_cvt_pk_bf16_f32 v182, v200, v201\n"
    "v_mfma_f32_16x16x32_bf16 v[204:207], a[220:223], v[84:87], v[204:207]\n"
    "s_nop 0\n"
    "v_exp_f32 v202, v202\n"
    "v_mfma_f32_16x16x32_bf16 v[208:211], a[220:223], v[100:103], v[232:235]\n"
    "v_mfma_f32_16x16x32_bf16 v[208:211], a[216:219], v[96:99], v[208:211]\n"
    "v_exp_f32 v203, v203\n"
    "v_mfma_f32_16x16x32_bf16 v[208:211], a[212:215], v[92:95], v[208:211]\n"
    "v_cvt_pk_bf16_f32 v183, v202, v203\n"
    "v_mfma_f32_16x16x32_bf16 v[208:211], a[208:211], v[88:91], v[208:211]\n"
    "v_exp_f32 v204, v204\n"
    "v_mfma_f32_16x16x32_bf16 a[184:187], v[136:139], a[232:235], 0\n"
    "v_mfma_f32_16x16x32_bf16 a[184:187], v[140:143], a[236:239], a[184:187]\n"
    "v_exp_f32 v205, v205\n"
    "v_mfma_f32_16x16x32_bf16 a[188:191], v[148:151], a[236:239], 0\n"
    "v_cvt_pk_bf16_f32 v190, v204, v205\n"
    "v_mfma_f32_16x16x32_bf16 a[188:191], v[144:147], a[232:235], a[188:191]\n"
    "s_waitcnt lgkmcnt(0)\n"
    "v_exp_f32 v206, v206\n"
    "v_smfmac_f32_16x16x64_bf16 v[244:247], a[240:243], v[144:151], v253\n"
    "; ---- second head, tiles 2..14 (also drains the parked output rows of the first head)\n"
    "s_branch Lent2_%=\n"
    ".p2align 6\n"
    "Lloop2_%=:\n"
    "v_mfma_f32_16x16x32_bf16 a[0:3], v[104:107], a[224:227], a[0:3]\n"
    "ds_read_b128 a[208:211], v0 offset:4096\n"
    "s_bitcmp1_b32 s58, 0\n"
    "v_mfma_f32_16x16x32_bf16 a[0:3], v[108:111], a[228:231], a[0:3]\n"
    "s_cselect_b64 s[48:49], s[44:45], s[40:41]\n"
    "v_exp_f32 v207, v207\n"
    "v_mfma_f32_16x16x32_bf16 a[4:7], v[116:119], a[228:231], a[4:7]\n"
    "ds_read_b64_tr_b16 a[232:233], v2 offset:0\n"
    "v_cvt_pk_bf16_f32 v143, v206, v207\n"
    "v_mfma_f32_16x16x32_bf16 a[4:7], v[112:115], a[224:227], a[4:7]\n"
    "s_lshl_b32 s75, s58, 13\n"
    "v_exp_f32 v208, v208\n"
    "v_mfma_f32_16x16x32_bf16 v[200:203], a[192:195], v[8:11], v[212:215]\n"
    "s_and_b32 s75, s75, 0x3c000\n"
    "s_add_u32 s63, s59, s75\n"
    "s_mov_b32 s50, s42\n"
    "v_mfma_f32_16x16x32_bf16 v[200:203], a[196:199], v[12:15], v[200:203]\n"
    "s_add_u32 s65, s61, s62\n"
    "v_exp_f32 v209, v209\n"
    "v_mfma_f32_16x16x32_bf16 v[200:203], a[200:203], v[16:19], v[200:203]\n"
    "ds_read_b64_tr_b16 a[234:235], v2 offset:4096\n"
    "v_cvt_pk_bf16_f32 v150, v208, v209\n"
    "v_mfma_f32_16x16x32_bf16 v[200:203], a[204:207], v[20:23], v[200:203]\n"
    "s_add_u32 s61, s61, 16384\n"
    "v_exp_f32 v210, v210\n"
    "v_mfma_f32_16x16x32_bf16 v[204:207], a[204:207], v[36:39], v[216:219]\n"
    "ds_read_b128 a[212:215], v0 offset:4352\n"
    "s_cmp_eq_u32 s61, 163840\n"
    "v_mfma_f32_16x16x32_bf16 v[204:207], a[200:203], v[32:35], v[204:207]\n"
    "s_cselect_b32 s61, s79, s61\n"
    "v_exp_f32 v211, v211\n"
    "v_mfma_f32_16x16x32_bf16 v[204:207], a[196:199], v[28:31], v[204:207]\n"
    "ds_read_b64_tr_b16 a[236:237], v2 offset:8192\n"
    "v_cvt_pk_bf16_f32 v151, v210, v211\n"
    "v_mfma_f32_16x16x32_bf16 v[204:207], a[192:195], v[24:27], v[204:207]\n"
    "s_add_i32 s58, s58, 1\n"
    "v_exp_f32 v200, v200\n"
    "v_mfma_f32_16x16x32_bf16 a[8:11], v[120:123], a[224:227], a[8:11]\n"
    "s_mov_b32 m0, s65\n"
    "v_mfma_f32_16x16x32_bf16 a[8:11], v[124:127], a[228:231], a[8:11]\n"
    "v_exp_f32 v201, v201\n"
    "v_mfma_f32_16x16x32_bf16 a[12:15], v[132:135], a[228:231], a[12:15]\n"
    "ds_read_b64_tr_b16 a[238:239], v2 offset:12288\n"
    "v_cvt_pk_bf16_f32 v152, v200, v201\n"
    "v_mfma_f32_16x16x32_bf16 a[12:15], v[128:131], a[224:227], a[12:15]\n"
    "v_exp_f32 v202, v202\n"
    "v_mfma_f32_16x16x32_bf16 a[16:19], v[136:139], a[224:227], a[16:19]\n"
    "ds_read_b128 a[216:219], v0 offset:4608\n"
    "v_mfma_f32_16x16x32_bf16 a[16:19], v[140:143], a[228:231], a[16:19]\n"
    "v_exp_f32 v203, v203\n"
    "v_mfma_f32_16x16x32_bf16 a[20:23], v[148:151], a[228:231], a[20:23]\n"
    "v_cvt_pk_bf16_f32 v153, v202, v203\n"
    "v_mfma_f32_16x16x32_bf16 a[20:23], v[144:147], a[224:227], a[20:23]\n"
    "v_exp_f32 v204, v204\n"
    "v_mfma_f32_16x16x32_bf16 v[208:211], a[192:195], v[40:43], v[220:223]\n"
    "buffer_load_dwordx4 v5, s[48:51], s63 offen nt lds\n"
    "v_mfma_f32_16x16x32_bf16 v[208:211], a[196:199], v[44:47], v[208:211]\n"
    "v_exp_f32 v205, v205\n"
    "v_mfma_f32_16x16x32_bf16 v[208:211], a[200:203], v[48:51], v[208:211]\n"
    "v_cvt_pk_bf16_f32 v160, v204, v205\n"
    "v_mfma_f32_16x16x32_bf16 v[208:211], a[204:207], v[52:55], v[208:211]\n"
    "v_exp_f32 v206, v206\n"
    "v_mfma_f32_16x16x32_bf16 v[200:203], a[204:207], v[68:71], v[224:227]\n"
    "ds_read_b128 a[220:223], v0 offset:4864\n"
    "v_mfma_f32_16x16x32_bf16 v[200:203], a[200:203], v[64:67], v[200:203]\n"
    "v_exp_f32 v207, v207\n"
    "v_mfma_f32_16x16x32_bf16 v[200:203], a[196:199], v[60:63], v[200:203]\n"
    "ds_read_b64_tr_b16 a[224:225], v1 offset:256\n"
    "v_cvt_pk_bf16_f32 v161, v206, v207\n"
    "v_mfma_f32_16x16x32_bf16 v[200:203], a[192:195], v[56:59], v[200:203]\n"
    "s_waitcnt lgkmcnt(3)\n"
    "v_exp_f32 v208, v208\n"
    "v_mfma_f32_16x16x32_bf16 a[24:27], v[104:107], a[232:235], a[24:27]\n"
    "buffer_load_dwordx4 v6, s[48:51], s63 offen offset:1024 nt lds\n"
    "v_mfma_f32_16x16x32_bf16 a[24:27], v[108:111], a[236:239], a[24:27]\n"
    "v_exp_f32 v209, v209\n"
    "v_mfma_f32_16x16x32_bf16 a[28:31], v[116:119], a[236:239], a[28:31]\n"
    "ds_read_b64_tr_b16 a[226:227], v1 offset:4352\n"
    "v_cvt_pk_bf16_f32 v168, v208, v209\n"
    "v_mfma_f32_16x16x32_bf16 a[28:31], v[112:115], a[232:235], a[28:31]\n"
    "v_exp_f32 v210, v210\n"
    "v_smfmac_f32_16x16x64_bf16 a[244:247], a[240:243], v[104:111], v253\n"
    "v_mfma_f32_16x16x32_bf16 a[32:35], v[120:123], a[232:235], a[32:35]\n"
    "v_mfma_f32_16x16x32_bf16 a[32:35], v[124:127], a[236:239], a[32:35]\n"
    "v_exp_f32 v211, v211\n"
    "v_mfma_f32_16x16x32_bf16 a[36:39], v[132:135], a[236:239], a[36:39]\n"
    "ds_read_b64_tr_b16 a[228:229], v1 offset:8448\n"
    "v_cvt_pk_bf16_f32 v169, v210, v211\n"
    "v_mfma_f32_16x16x32_bf16 a[36:39], v[128:131], a[232:235], a[36:39]\n"
    "v_exp_f32 v200, v200\n"
    "v_mfma_f32_16x16x32_bf16 v[204:207], a[192:195], v[72:75], v[228:231]\n"
    "v_mfma_f32_16x16x32_bf16 v[204:207], a[196:199], v[76:79], v[204:207]\n"
    "v_exp_f32 v201, v201\n"
    "v_mfma_f32_16x16x32_bf16 v[204:207], a[200:203], v[80:83], v[204:207]\n"
    "ds_read_b64_tr_b16 a[230:231], v1 offset:12544\n"
    "v_cvt_pk_bf16_f32 v176, v200, v201\n"
    "v_mfma_f32_16x16x32_bf16 v[204:207], a[204:207], v[84:87], v[204:207]\n"
    "s_nop 0\n"
    "v_exp_f32 v202, v202\n"
    "v_mfma_f32_16x16x32_bf16 v[208:211], a[204:207], v[100:103], v[232:235]\n"
    "v_mfma_f32_16x16x32_bf16 v[208:211], a[200:203], v[96:99], v[208:211]\n"
    "v_exp_f32 v203, v203\n"
    "v_mfma_f32_16x16x32_bf16 v[208:211], a[196:199], v[92:95], v[208:211]\n"
    "v_cvt_pk_bf16_f32 v177, v202, v203\n"
    "v_mfma_f32_16x16x32_bf16 v[208:211], a[192:195], v[88:91], v[208:211]\n"
    "v_exp_f32 v204, v204\n"
    "v_mfma_f32_16x16x32_bf16 a[40:43], v[136:139], a[232:235], a[40:43]\n"
    "buffer_load_dwordx4 v7, s[48:51], s63 offen offset:2048 nt lds\n"
    "v_mfma_f32_16x16x32_bf16 a[40:43], v[140:143], a[236:239], a[40:43]\n"
    "v_exp_f32 v205, v205\n"
    "v_mfma_f32_16x16x32_bf16 a[44:47], v[148:151], a[236:239], a[44:47]\n"
    "s_waitcnt lgkmcnt(0)\n"
    "v_cvt_pk_bf16_f32 v184, v204, v205\n"
    "v_mfma_f32_16x16x32_bf16 a[44:47], v[144:147], a[232:235], a[44:47]\n"
    "s_waitcnt lgkmcnt(0)\n"
    "v_exp_f32 v206, v206\n"
    "v_mfma_f32_16x16x32_bf16 a[48:51], v[104:107], a[224:227], a[48:51]\n"
    "ds_read_b128 a[192:195], v0 offset:8192\n"
    "v_mfma_f32_16x16x32_bf16 a[48:51], v[108:111], a[228:231], a[48:51]\n"
    "v_exp_f32 v207, v207\n"
    "v_mfma_f32_16x16x32_bf16 a[52:55], v[116:119], a[228:231], a[52:55]\n"
    "ds_read_b64_tr_b16 a[232:233], v2 offset:256\n"
    "v_cvt_pk_bf16_f32 v185, v206, v207\n"
    "v_mfma_f32_16x16x32_bf16 a[52:55], v[112:115], a[224:227], a[52:55]\n"
    "v_exp_f32 v208, v208\n"
    "v_mfma_f32_16x16x32_bf16 v[200:203], a[208:211], v[8:11], v[212:215]\n"
    "v_mfma_f32_16x16x32_bf16 v[200:203], a[212:215], v[12:15], v[200:203]\n"
    "v_exp_f32 v209, v209\n"
    "v_mfma_f32_16x16x32_bf16 v[200:203], a[216:219], v[16:19], v[200:203]\n"
    "ds_read_b64_tr_b16 a[234:235], v2 offset:4352\n"
    "v_cvt_pk_bf16_f32 v192, v208, v209\n"
    "v_mfma_f32_16x16x32_bf16 v[200:203], a[220:223], v[20:23], v[200:203]\n"
    "v_exp_f32 v210, v210\n"
    "v_mfma_f32_16x16x32_bf16 v[204:207], a[220:223], v[36:39], v[216:219]\n"
    "ds_read_b128 a[196:199], v0 offset:8448\n"
    "v_mfma_f32_16x16x32_bf16 v[204:207], a[216:219], v[32:35], v[204:207]\n"
    "v_exp_f32 v211, v211\n"
    "v_mfma_f32_16x16x32_bf16 v[204:207], a[212:215], v[28:31], v[204:207]\n"
    "ds_read_b64_tr_b16 a[236:237], v2 offset:8448\n"
    "v_cvt_pk_bf16_f32 v193, v210, v211\n"
    "v_mfma_f32_16x16x32_bf16 v[204:207], a[208:211], v[24:27], v[204:207]\n"
    "v_exp_f32 v200, v200\n"
    "v_mfma_f32_16x16x32_bf16 a[56:59], v[120:123], a[224:227], a[56:59]\n"
    "ds_read_b64_tr_b16 v[248:249], v254 offset:1024\n"
    "v_mfma_f32_16x16x32_bf16 a[56:59], v[124:127], a[228:231], a[56:59]\n"
    "v_exp_f32 v201, v201\n"
    "v_mfma_f32_16x16x32_bf16 a[60:63], v[132:135], a[228:231], a[60:63]\n"
    "ds_read_b64_tr_b16 a[238:239], v2 offset:12544\n"
    "v_cvt_pk_bf16_f32 v154, v200, v201\n"
    "v_mfma_f32_16x16x32_bf16 a[60:63], v[128:131], a[224:227], a[60:63]\n"
    "v_exp_f32 v202, v202\n"
    "v_smfmac_f32_16x16x64_bf16 a[248:251], a[240:243], v[112:119], v253\n"
    "ds_read_b64_tr_b16 v[250:251], v254 offset:1056\n"
    "v_mfma_f32_16x16x32_bf16 a[64:67], v[136:139], a[224:227], a[64:67]\n"
    "ds_read_b128 a[200:203], v0 offset:8704\n"
    "v_mfma_f32_16x16x32_bf16 a[64:67], v[140:143], a[228:231], a[64:67]\n"
    "v_exp_f32 v203, v203\n"
    "v_mfma_f32_16x16x32_bf16 a[68:71], v[148:151], a[228:231], a[68:71]\n"
    "v_cvt_pk_bf16_f32 v155, v202, v203\n"
    "v_mfma_f32_16x16x32_bf16 a[68:71], v[144:147], a[224:227], a[68:71]\n"
    "v_exp_f32 v204, v204\n"
    "v_mfma_f32_16x16x32_bf16 v[208:211], a[208:211], v[40:43], v[220:223]\n"
    "buffer_load_dwordx4 v255, s[48:51], s63 offen offset:3072 nt lds\n"
    "v_mfma_f32_16x16x32_bf16 v[208:211], a[212:215], v[44:47], v[208:211]\n"
    "s_mov_b32 s68, s66\n"
    "v_exp_f32 v205, v205\n"
    "v_mfma_f32_16x16x32_bf16 v[208:211], a[216:219], v[48:51], v[208:211]\n"
    "s_add_u32 s66, s66, 16384\n"
    "v_cvt_pk_bf16_f32 v162, v204, v205\n"
    "s_cmp_eq_u32 s66, 163840\n"
    "v_mfma_f32_16x16x32_bf16 v[208:211], a[220:223], v[52:55], v[208:211]\n"
    "s_cselect_b32 s66, s79, s66\n"
    "v_exp_f32 v206, v206\n"
    "v_mfma_f32_16x16x32_bf16 v[200:203], a[220:223], v[68:71], v[224:227]\n"
    "ds_read_b128 a[204:207], v0 offset:8960\n"
    "s_mov_b32 s67, s66\n"
    "v_mfma_f32_16x16x32_bf16 v[200:203], a[216:219], v[64:67], v[200:203]\n"
    "s_waitcnt lgkmcnt(2)\n"
    "v_exp_f32 v207, v207\n"
    "v_mfma_f32_16x16x32_bf16 v[200:203], a[212:215], v[60:63], v[200:203]\n"
    "ds_read_b64_tr_b16 a[224:225], v1 offset:512\n"
    "v_cvt_pk_bf16_f32 v163, v206, v207\n"
    "v_mfma_f32_16x16x32_bf16 v[200:203], a[208:211], v[56:59], v[200:203]\n"
    "s_waitcnt lgkmcnt(3)\n"
    "v_exp_f32 v208, v208\n"
    "v_mfma_f32_16x16x32_bf16 a[72:75], v[104:107], a[232:235], a[72:75]\n"
    "buffer_store_dwordx4 v[248:251], v252, s[52:55], s64 offen offset:1024 nt\n"
    "s_add_u32 s66, s66, 16384\n"
    "v_mfma_f32_16x16x32_bf16 a[72:75], v[108:111], a[236:239], a[72:75]\n"
    "s_cmp_eq_u32 s66, 163840\n"
    "v_exp_f32 v209, v209\n"
    "v_mfma_f32_16x16x32_bf16 a[76:79], v[116:119], a[236:239], a[76:79]\n"
    "ds_read_b64_tr_b16 a[226:227], v1 offset:4608\n"
    "v_cvt_pk_bf16_f32 v170, v208, v209\n"
    "v_mfma_f32_16x16x32_bf16 a[76:79], v[112:115], a[232:235], a[76:79]\n"
    "s_cselect_b32 s66, s79, s66\n"
    "v_exp_f32 v210, v210\n"
    "v_mfma_f32_16x16x32_bf16 a[80:83], v[120:123], a[232:235], a[80:83]\n"
    "s_bitcmp1_b32 s58, 0\n"
    "s_cselect_b64 s[48:49], s[44:45], s[40:41]\n"
    "s_lshl_b32 s75, s58, 13\n"
    "v_mfma_f32_16x16x32_bf16 a[80:83], v[124:127], a[236:239], a[80:83]\n"
    "s_and_b32 s75, s75, 0x3c000\n"
    "v_exp_f32 v211, v211\n"
    "v_mfma_f32_16x16x32_bf16 a[84:87], v[132:135], a[236:239], a[84:87]\n"
    "ds_read_b64_tr_b16 a[228:229], v1 offset:8704\n"
    "v_cvt_pk_bf16_f32 v171, v210, v211\n"
    "v_mfma_f32_16x16x32_bf16 a[84:87], v[128:131], a[232:235], a[84:87]\n"
    "s_add_u32 s63, s59, s75\n"
    "v_exp_f32 v200, v200\n"
    "v_mfma_f32_16x16x32_bf16 v[204:207], a[208:211], v[72:75], v[228:231]\n"
    "s_mov_b32 s50, s42\n"
    "s_add_u32 s65, s61, s62\n"
    "s_add_u32 s61, s61, 16384\n"
    "v_mfma_f32_16x16x32_bf16 v[204:207], a[212:215], v[76:79], v[204:207]\n"
    "s_cmp_eq_u32 s61, 163840\n"
    "v_exp_f32 v201, v201\n"
    "v_mfma_f32_16x16x32_bf16 v[204:207], a[216:219], v[80:83], v[204:207]\n"
    "ds_read_b64_tr_b16 a[230:231], v1 offset:12800\n"
    "v_cvt_pk_bf16_f32 v178, v200, v201\n"
    "v_mfma_f32_16x16x32_bf16 v[204:207], a[220:223], v[84:87], v[204:207]\n"
    "s_cselect_b32 s61, s79, s61\n"
    "v_exp_f32 v202, v202\n"
    "v_mfma_f32_16x16x32_bf16 v[208:211], a[220:223], v[100:103], v[232:235]\n"
    "s_add_i32 s58, s58, 1\n"
    "s_mov_b32 m0, s65\n"
    "v_mfma_f32_16x16x32_bf16 v[208:211], a[216:219], v[96:99], v[208:211]\n"
    "v_exp_f32 v203, v203\n"
    "v_mfma_f32_16x16x32_bf16 v[208:211], a[212:215], v[92:95], v[208:211]\n"
    "v_cvt_pk_bf16_f32 v179, v202, v203\n"
    "v_add_u32 v254, s78, v254\n"
    "v_mfma_f32_16x16x32_bf16 v[208:211], a[208:211], v[88:91], v[208:211]\n"
    "v_exp_f32 v204, v204\n"
    "v_mfma_f32_16x16x32_bf16 a[88:91], v[136:139], a[232:235], a[88:91]\n"
    "buffer_load_dwordx4 v5, s[48:51], s63 offen nt lds\n"
    "v_mfma_f32_16x16x32_bf16 a[88:91], v[140:143], a[236:239], a[88:91]\n"
    "v_exp_f32 v205, v205\n"
    "v_mfma_f32_16x16x32_bf16 a[92:95], v[148:151], a[236:239], a[92:95]\n"
    "v_cvt_pk_bf16_f32 v186, v204, v205\n"
    "v_mfma_f32_16x16x32_bf16 a[92:95], v[144:147], a[232:235], a[92:95]\n"
    "s_waitcnt lgkmcnt(0)\n"
    "v_exp_f32 v206, v206\n"
    "v_smfmac_f32_16x16x64_bf16 a[252:255], a[240:243], v[120:127], v253\n"
    "s_waitcnt lgkmcnt(0)\n"
    "v_mfma_f32_16x16x32_bf16 a[96:99], v[104:107], a[224:227], a[96:99]\n"
    "ds_read_b128 a[208:211], v0 offset:12288\n"
    "v_mfma_f32_16x16x32_bf16 a[96:99], v[108:111], a[228:231], a[96:99]\n"
    "v_exp_f32 v207, v207\n"
    "v_mfma_f32_16x16x32_bf16 a[100:103], v[116:119], a[228:231], a[100:103]\n"
    "ds_read_b64_tr_b16 a[232:233], v2 offset:512\n"
    "v_cvt_pk_bf16_f32 v187, v206, v207\n"
    "v_mfma_f32_16x16x32_bf16 a[100:103], v[112:115], a[224:227], a[100:103]\n"
    "v_exp_f32 v208, v208\n"
    "v_mfma_f32_16x16x32_bf16 v[200:203], a[192:195], v[8:11], v[212:215]\n"
    "buffer_load_dwordx4 v6, s[48:51], s63 offen offset:1024 nt lds\n"
    "v_mfma_f32_16x16x32_bf16 v[200:203], a[196:199], v[12:15], v[200:203]\n"
    "v_exp_f32 v209, v209\n"
    "v_mfma_f32_16x16x32_bf16 v[200:203], a[200:203], v[16:19], v[200:203]\n"
    "ds_read_b64_tr_b16 a[234:235], v2 offset:4608\n"
    "v_cvt_pk_bf16_f32 v194, v208, v209\n"
    "v_mfma_f32_16x16x32_bf16 v[200:203], a[204:207], v[20:23], v[200:203]\n"
    "v_exp_f32 v210, v210\n"
    "v_mfma_f32_16x16x32_bf16 v[204:207], a[204:207], v[36:39], v[216:219]\n"
    "ds_read_b128 a[212:215], v0 offset:12544\n"
    "v_mfma_f32_16x16x32_bf16 v[204:207], a[200:203], v[32:35], v[204:207]\n"
    "v_exp_f32 v211, v211\n"
    "v_mfma_f32_16x16x32_bf16 v[204:207], a[196:199], v[28:31], v[204:207]\n"
    "ds_read_b64_tr_b16 a[236:237], v2 offset:8704\n"
    "v_cvt_pk_bf16_f32 v195, v210, v211\n"
    "v_mfma_f32_16x16x32_bf16 v[204:207], a[192:195], v[24:27], v[204:207]\n"
    "v_exp_f32 v200, v200\n"
    "v_mfma_f32_16x16x32_bf16 a[104:107], v[120:123], a[224:227], a[104:107]\n"
    "buffer_load_dwordx4 v7, s[48:51], s63 offen offset:2048 nt lds\n"
    "v_mfma_f32_16x16x32_bf16 a[104:107], v[124:127], a[228:231], a[104:107]\n"
    "v_exp_f32 v201, v201\n"
    "v_mfma_f32_16x16x32_bf16 a[108:111], v[132:135], a[228:231], a[108:111]\n"
    "ds_read_b64_tr_b16 a[238:239], v2 offset:12800\n"
    "v_cvt_pk_bf16_f32 v156, v200, v201\n"
    "v_mfma_f32_16x16x32_bf16 a[108:111], v[128:131], a[224:227], a[108:111]\n"
    "v_exp_f32 v202, v202\n"
    "v_mfma_f32_16x16x32_bf16 a[112:115], v[136:139], a[224:227], a[112:115]\n"
    "ds_read_b128 a[216:219], v0 offset:12800\n"
    "v_mfma_f32_16x16x32_bf16 a[112:115], v[140:143], a[228:231], a[112:115]\n"
    "v_exp_f32 v203, v203\n"
    "v_mfma_f32_16x16x32_bf16 a[116:119], v[148:151], a[228:231], a[116:119]\n"
    "v_cvt_pk_bf16_f32 v157, v202, v203\n"
    "v_mfma_f32_16x16x32_bf16 a[116:119], v[144:147], a[224:227], a[116:119]\n"
    "v_exp_f32 v204, v204\n"
    "v_mfma_f32_16x16x32_bf16 v[208:211], a[192:195], v[40:43], v[220:223]\n"
    "v_mfma_f32_16x16x32_bf16 v[208:211], a[196:199], v[44:47], v[208:211]\n"
    "v_exp_f32 v205, v205\n"
    "v_mfma_f32_16x16x32_bf16 v[208:211], a[200:203], v[48:51], v[208:211]\n"
    "v_cvt_pk_bf16_f32 v164, v204, v205\n"
    "v_mfma_f32_16x16x32_bf16 v[208:211], a[204:207], v[52:55], v[208:211]\n"
    "v_exp_f32 v206, v206\n"
    "v_mfma_f32_16x16x32_bf16 v[200:203], a[204:207], v[68:71], v[224:227]\n"
    "ds_read_b128 a[220:223], v0 offset:13056\n"
    "v_mfma_f32_16x16x32_bf16 v[200:203], a[200:203], v[64:67], v[200:203]\n"
    "v_exp_f32 v207, v207\n"
    "v_mfma_f32_16x16x32_bf16 v[200:203], a[196:199], v[60:63], v[200:203]\n"
    "ds_read_b64_tr_b16 a[224:225], v1 offset:768\n"
    "v_cvt_pk_bf16_f32 v165, v206, v207\n"
    "v_mfma_f32_16x16x32_bf16 v[200:203], a[192:195], v[56:59], v[200:203]\n"
    "s_waitcnt lgkmcnt(3)\n"
    "v_exp_f32 v208, v208\n"
    "v_mfma_f32_16x16x32_bf16 a[120:123], v[104:107], a[232:235], a[120:123]\n"
    "v_mfma_f32_16x16x32_bf16 a[120:123], v[108:111], a[236:239], a[120:123]\n"
    "v_exp_f32 v209, v209\n"
    "v_mfma_f32_16x16x32_bf16 a[124:127], v[116:119], a[236:239], a[124:127]\n"
    "ds_read_b64_tr_b16 a[226:227], v1 offset:4864\n"
    "v_cvt_pk_bf16_f32 v172, v208, v209\n"
    "v_mfma_f32_16x16x32_bf16 a[124:127], v[112:115], a[232:235], a[124:127]\n"
    "v_exp_f32 v210, v210\n"
    "v_smfmac_f32_16x16x64_bf16 v[236:239], a[240:243], v[128:135], v253\n"
    "buffer_load_dwordx4 v255, s[48:51], s63 offen offset:3072 nt lds\n"
    "v_mfma_f32_16x16x32_bf16 a[128:131], v[120:123], a[232:235], a[128:131]\n"
    "v_mfma_f32_16x16x32_bf16 a[128:131], v[124:127], a[236:239], a[128:131]\n"
    "v_exp_f32 v211, v211\n"
    "v_mfma_f32_16x16x32_bf16 a[132:135], v[132:135], a[236:239], a[132:135]\n"
    "ds_read_b64_tr_b16 a[228:229], v1 offset:8960\n"
    "v_cvt_pk_bf16_f32 v173, v210, v211\n"
    "v_mfma_f32_16x16x32_bf16 a[132:135], v[128:131], a[232:235], a[132:135]\n"
    "v_exp_f32 v200, v200\n"
    "v_mfma_f32_16x16x32_bf16 v[204:207], a[192:195], v[72:75], v[228:231]\n"
    "v_mfma_f32_16x16x32_bf16 v[204:207], a[196:199], v[76:79], v[204:207]\n"
    "v_exp_f32 v201, v201\n"
    "v_mfma_f32_16x16x32_bf16 v[204:207], a[200:203], v[80:83], v[204:207]\n"
    "ds_read_b64_tr_b16 a[230:231], v1 offset:13056\n"
    "v_cvt_pk_bf16_f32 v180, v200, v201\n"
    "v_mfma_f32_16x16x32_bf16 v[204:207], a[204:207], v[84:87], v[204:207]\n"
    "s_nop 0\n"
    "v_exp_f32 v202, v202\n"
    "v_mfma_f32_16x16x32_bf16 v[208:211], a[204:207], v[100:103], v[232:235]\n"
    "v_mfma_f32_16x16x32_bf16 v[208:211], a[200:203], v[96:99], v[208:211]\n"
    "v_exp_f32 v203, v203\n"
    "v_mfma_f32_16x16x32_bf16 v[208:211], a[196:199], v[92:95], v[208:211]\n"
    "v_cvt_pk_bf16_f32 v181, v202, v203\n"
    "v_mfma_f32_16x16x32_bf16 v[208:211], a[192:195], v[88:91], v[208:211]\n"
    "v_exp_f32 v204, v204\n"
    "v_mfma_f32_16x16x32_bf16 a[136:139], v[136:139], a[232:235], a[136:139]\n"
    "v_mfma_f32_16x16x32_bf16 a[136:139], v[140:143], a[236:239], a[136:139]\n"
    "v_exp_f32 v205, v205\n"
    "v_mfma_f32_16x16x32_bf16 a[140:143], v[148:151], a[236:239], a[140:143]\n"
    "s_waitcnt lgkmcnt(0)\n"
    "v_cvt_pk_bf16_f32 v188, v204, v205\n"
    "v_mfma_f32_16x16x32_bf16 a[140:143], v[144:147], a[232:235], a[140:143]\n"
    "s_waitcnt lgkmcnt(0)\n"
    "v_exp_f32 v206, v206\n"
    "v_mfma_f32_16x16x32_bf16 a[144:147], v[104:107], a[224:227], a[144:147]\n"
    "v_mfma_f32_16x16x32_bf16 a[144:147], v[108:111], a[228:231], a[144:147]\n"
    "v_exp_f32 v207, v207\n"
    "v_mfma_f32_16x16x32_bf16 a[148:151], v[116:119], a[228:231], a[148:151]\n"
    "ds_read_b64_tr_b16 a[232:233], v2 offset:768\n"
    "v_cvt_pk_bf16_f32 v189, v206, v207\n"
    "v_mfma_f32_16x16x32_bf16 a[148:151], v[112:115], a[224:227], a[148:151]\n"
    "v_exp_f32 v208, v208\n"
    "v_mfma_f32_16x16x32_bf16 v[200:203], a[208:211], v[8:11], v[212:215]\n"
    "v_mfma_f32_16x16x32_bf16 v[200:203], a[212:215], v[12:15], v[200:203]\n"
    "v_exp_f32 v209, v209\n"
    "v_mfma_f32_16x16x32_bf16 v[200:203], a[216:219], v[16:19], v[200:203]\n"
    "ds_read_b64_tr_b16 a[234:235], v2 offset:4864\n"
    "v_cvt_pk_bf16_f32 v196, v208, v209\n"
    "v_mfma_f32_16x16x32_bf16 v[200:203], a[220:223], v[20:23], v[200:203]\n"
    "v_exp_f32 v210, v210\n"
    "v_mfma_f32_16x16x32_bf16 v[204:207], a[220:223], v[36:39], v[216:219]\n"
    "v_mfma_f32_16x16x32_bf16 v[204:207], a[216:219], v[32:35], v[204:207]\n"
    "v_exp_f32 v211, v211\n"
    "v_mfma_f32_16x16x32_bf16 v[204:207], a[212:215], v[28:31], v[204:207]\n"
    "ds_read_b64_tr_b16 a[236:237], v2 offset:8960\n"
    "v_cvt_pk_bf16_f32 v197, v210, v211\n"
    "v_mfma_f32_16x16x32_bf16 v[204:207], a[208:211], v[24:27], v[204:207]\n"
    "v_exp_f32 v200, v200\n"
    "v_mfma_f32_16x16x32_bf16 a[152:155], v[120:123], a[224:227], a[152:155]\n"
    "v_mfma_f32_16x16x32_bf16 a[152:155], v[124:127], a[228:231], a[152:155]\n"
    "v_exp_f32 v201, v201\n"
    "v_mfma_f32_16x16x32_bf16 a[156:159], v[132:135], a[228:231], a[156:159]\n"
    "ds_read_b64_tr_b16 a[238:239], v2 offset:13056\n"
    "v_cvt_pk_bf16_f32 v158, v200, v201\n"
    "v_mfma_f32_16x16x32_bf16 a[156:159], v[128:131], a[224:227], a[156:159]\n"
    "v_exp_f32 v202, v202\n"
    "v_smfmac_f32_16x16x64_bf16 v[240:243], a[240:243], v[136:143], v253\n"
    "v_mfma_f32_16x16x32_bf16 a[160:163], v[136:139], a[224:227], a[160:163]\n"
    "v_add_u32 v0, s67, v3\n"
    "v_add_u32 v1, s68, v4\n"
    "v_mfma_f32_16x16x32_bf16 a[160:163], v[140:143], a[228:231], a[160:163]\n"
    "v_exp_f32 v203, v203\n"
    "v_mfma_f32_16x16x32_bf16 a[164:167], v[148:151], a[228:231], a[164:167]\n"
    "v_cvt_pk_bf16_f32 v159, v202, v203\n"
    "v_mfma_f32_16x16x32_bf16 a[164:167], v[144:147], a[224:227], a[164:167]\n"
    "s_waitcnt lgkmcnt(0)\n"
    "v_exp_f32 v204, v204\n"
    "v_mfma_f32_16x16x32_bf16 v[208:211], a[208:211], v[40:43], v[220:223]\n"
    "v_xor_b32 v2, 32, v1\n"
    "v_mfma_f32_16x16x32_bf16 v[208:211], a[212:215], v[44:47], v[208:211]\n"
    "s_waitcnt vmcnt(9)\n"
    "v_exp_f32 v205, v205\n"
    "v_mfma_f32_16x16x32_bf16 v[208:211], a[216:219], v[48:51], v[208:211]\n"
    "v_cvt_pk_bf16_f32 v166, v204, v205\n"
    "v_mfma_f32_16x16x32_bf16 v[208:211], a[220:223], v[52:55], v[208:211]\n"
    "s_barrier\n"
    "v_exp_f32 v206, v206\n"
    "v_mfma_f32_16x16x32_bf16 v[200:203], a[220:223], v[68:71], v[224:227]\n"
    "ds_read_b128 a[192:195], v0 offset:0\n"
    "v_mfma_f32_16x16x32_bf16 v[200:203], a[216:219], v[64:67], v[200:203]\n"
    "v_exp_f32 v207, v207\n"
    "v_mfma_f32_16x16x32_bf16 v[200:203], a[212:215], v[60:63], v[200:203]\n"
    "ds_read_b64_tr_b16 a[224:225], v1 offset:0\n"
    "v_cvt_pk_bf16_f32 v167, v206, v207\n"
    "v_mfma_f32_16x16x32_bf16 v[200:203], a[208:211], v[56:59], v[200:203]\n"
    "v_exp_f32 v208, v208\n"
    "v_mfma_f32_16x16x32_bf16 a[168:171], v[104:107], a[232:235], a[168:171]\n"
    "ds_read_b128 a[196:199], v0 offset:256\n"
    "v_mfma_f32_16x16x32_bf16 a[168:171], v[108:111], a[236:239], a[168:171]\n"
    "v_exp_f32 v209, v209\n"
    "v_mfma_f32_16x16x32_bf16 a[172:175], v[116:119], a[236:239], a[172:175]\n"
    "ds_read_b64_tr_b16 a[226:227], v1 offset:4096\n"
    "v_cvt_pk_bf16_f32 v174, v208, v209\n"
    "v_mfma_f32_16x16x32_bf16 a[172:175], v[112:115], a[232:235], a[172:175]\n"
    "v_exp_f32 v210, v210\n"
    "v_mfma_f32_16x16x32_bf16 a[176:179], v[120:123], a[232:235], a[176:179]\n"
    "ds_read_b128 a[200:203], v0 offset:512\n"
    "s_xor_b32 s78, s78, 12288\n"
    "v_mfma_f32_16x16x32_bf16 a[176:179], v[124:127], a[236:239], a[176:179]\n"
    "s_add_u32 s64, s64, 2048\n"
    "v_exp_f32 v211, v211\n"
    "v_mfma_f32_16x16x32_bf16 a[180:183], v[132:135], a[236:239], a[180:183]\n"
    "ds_read_b64_tr_b16 a[228:229], v1 offset:8192\n"
    "v_cvt_pk_bf16_f32 v175, v210, v211\n"
    "v_mfma_f32_16x16x32_bf16 a[180:183], v[128:131], a[232:235], a[180:183]\n"
    "s_sub_u32 s69, s69, 1\n"
    "v_exp_f32 v200, v200\n"
    "v_mfma_f32_16x16x32_bf16 v[204:207], a[208:211], v[72:75], v[228:231]\n"
    "ds_read_b128 a[204:207], v0 offset:768\n"
    "v_mfma_f32_16x16x32_bf16 v[204:207], a[212:215], v[76:79], v[204:207]\n"
    "v_exp_f32 v201, v201\n"
    "v_mfma_f32_16x16x32_bf16 v[204:207], a[216:219], v[80:83], v[204:207]\n"
    "ds_read_b64_tr_b16 a[230:231], v1 offset:12288\n"
    "v_cvt_pk_bf16_f32 v182, v200, v201\n"
    "v_mfma_f32_16x16x32_bf16 v[204:207], a[220:223], v[84:87], v[204:207]\n"
    "s_nop 0\n"
    "v_exp_f32 v202, v202\n"
    "v_mfma_f32_16x16x32_bf16 v[208:211], a[220:223], v[100:103], v[232:235]\n"
    "v_mfma_f32_16x16x32_bf16 v[208:211], a[216:219], v[96:99], v[208:211]\n"
    "v_exp_f32 v203, v203\n"
    "v_mfma_f32_16x16x32_bf16 v[208:211], a[212:215], v[92:95], v[208:211]\n"
    "v_cvt_pk_bf16_f32 v183, v202, v203\n"
    "v_mfma_f32_16x16x32_bf16 v[208:211], a[208:211], v[88:91], v[208:211]\n"
    "v_exp_f32 v204, v204\n"
    "v_mfma_f32_16x16x32_bf16 a[184:187], v[136:139], a[232:235], a[184:187]\n"
    "v_mfma_f32_16x16x32_bf16 a[184:187], v[140:143], a[236:239], a[184:187]\n"
    "v_exp_f32 v205, v205\n"
    "v_mfma_f32_16x16x32_bf16 a[188:191], v[148:151], a[236:239], a[188:191]\n"
    "v_cvt_pk_bf16_f32 v190, v204, v205\n"
    "v_mfma_f32_16x16x32_bf16 a[188:191], v[144:147], a[232:235], a[188:191]\n"
    "s_waitcnt lgkmcnt(0)\n"
    "v_exp_f32 v206, v206\n"
    "v_smfmac_f32_16x16x64_bf16 v[244:247], a[240:243], v[144:151], v253\n"
    "Lent2_%=:\n"
    "v_mfma_f32_16x16x32_bf16 a[0:3], v[152:155], a[224:227], a[0:3]\n"
    "ds_read_b128 a[208:211], v0 offset:4096\n"
    "s_bitcmp1_b32 s58, 0\n"
    "v_mfma_f32_16x16x32_bf16 a[0:3], v[156:159], a[228:231], a[0:3]\n"
    "s_cselect_b64 s[48:49], s[44:45], s[40:41]\n"
    "v_exp_f32 v207, v207\n"
    "v_mfma_f32_16x16x32_bf16 a[4:7], v[164:167], a[228:231], a[4:7]\n"
    "ds_read_b64_tr_b16 a[232:233], v2 offset:0\n"
    "v_cvt_pk_bf16_f32 v191, v206, v207\n"
    "v_mfma_f32_16x16x32_bf16 a[4:7], v[160:163], a[224:227], a[4:7]\n"
    "s_lshl_b32 s75, s58, 13\n"
    "v_exp_f32 v208, v208\n"
    "v_mfma_f32_16x16x32_bf16 v[200:203], a[192:195], v[8:11], v[212:215]\n"
    "s_and_b32 s75, s75, 0x3c000\n"
    "s_add_u32 s63, s59, s75\n"
    "s_mov_b32 s50, s42\n"
    "v_mfma_f32_16x16x32_bf16 v[200:203], a[196:199], v[12:15], v[200:203]\n"
    "s_add_u32 s65, s61, s62\n"
    "v_exp_f32 v209, v209\n"
    "v_mfma_f32_16x16x32_bf16 v[200:203], a[200:203], v[16:19], v[200:203]\n"
    "ds_read_b64_tr_b16 a[234:235], v2 offset:4096\n"
    "v_cvt_pk_bf16_f32 v198, v208, v209\n"
    "v_mfma_f32_16x16x32_bf16 v[200:203], a[204:207], v[20:23], v[200:203]\n"
    "s_add_u32 s61, s61, 16384\n"
    "v_exp_f32 v210, v210\n"
    "v_mfma_f32_16x16x32_bf16 v[204:207], a[204:207], v[36:39], v[216:219]\n"
    "ds_read_b128 a[212:215], v0 offset:4352\n"
    "s_cmp_eq_u32 s61, 163840\n"
    "v_mfma_f32_16x16x32_bf16 v[204:207], a[200:203], v[32:35], v[204:207]\n"
    "s_cselect_b32 s61, s79, s61\n"
    "v_exp_f32 v211, v211\n"
    "v_mfma_f32_16x16x32_bf16 v[204:207], a[196:199], v[28:31], v[204:207]\n"
    "ds_read_b64_tr_b16 a[236:237], v2 offset:8192\n"
    "v_cvt_pk_bf16_f32 v199, v210, v211\n"
    "v_mfma_f32_16x16x32_bf16 v[204:207], a[192:195], v[24:27], v[204:207]\n"
    "s_add_i32 s58, s58, 1\n"
    "v_exp_f32 v200, v200\n"
    "v_mfma_f32_16x16x32_bf16 a[8:11], v[168:171], a[224:227], a[8:11]\n"
    "s_mov_b32 m0, s65\n"
    "v_mfma_f32_16x16x32_bf16 a[8:11], v[172:175], a[228:231], a[8:11]\n"
    "v_exp_f32 v201, v201\n"
    "v_mfma_f32_16x16x32_bf16 a[12:15], v[180:183], a[228:231], a[12:15]\n"
    "ds_read_b64_tr_b16 a[238:239], v2 offset:12288\n"
    "v_cvt_pk_bf16_f32 v104, v200, v201\n"
    "v_mfma_f32_16x16x32_bf16 a[12:15], v[176:179], a[224:227], a[12:15]\n"
    "v_exp_f32 v202, v202\n"
    "v_mfma_f32_16x16x32_bf16 a[16:19], v[184:187], a[224:227], a[16:19]\n"
    "ds_read_b128 a[216:219], v0 offset:4608\n"
    "v_mfma_f32_16x16x32_bf16 a[16:19], v[188:191], a[228:231], a[16:19]\n"
    "v_exp_f32 v203, v203\n"
    "v_mfma_f32_16x16x32_bf16 a[20:23], v[196:199], a[228:231], a[20:23]\n"
    "v_cvt_pk_bf16_f32 v105, v202, v203\n"
    "v_mfma_f32_16x16x32_bf16 a[20:23], v[192:195], a[224:227], a[20:23]\n"
    "v_exp_f32 v204, v204\n"
    "v_mfma_f32_16x16x32_bf16 v[208:211], a[192:195], v[40:43], v[220:223]\n"
    "buffer_load_dwordx4 v5, s[48:51], s63 offen nt lds\n"
    "v_mfma_f32_16x16x32_bf16 v[208:211], a[196:199], v[44:47], v[208:211]\n"
    "v_exp_f32 v205, v205\n"
    "v_mfma_f32_16x16x32_bf16 v[208:211], a[200:203], v[48:51], v[208:211]\n"
    "v_cvt_pk_bf16_f32 v112, v204, v205\n"
    "v_mfma_f32_16x16x32_bf16 v[208:211], a[204:207], v[52:55], v[208:211]\n"
    "v_exp_f32 v206, v206\n"
    "v_mfma_f32_16x16x32_bf16 v[200:203], a[204:207], v[68:71], v[224:227]\n"
    "ds_read_b128 a[220:223], v0 offset:4864\n"
    "v_mfma_f32_16x16x32_bf16 v[200:203], a[200:203], v[64:67], v[200:203]\n"
    "v_exp_f32 v207, v207\n"
    "v_mfma_f32_16x16x32_bf16 v[200:203], a[196:199], v[60:63], v[200:203]\n"
    "ds_read_b64_tr_b16 a[224:225], v1 offset:256\n"
    "v_cvt_pk_bf16_f32 v113, v206, v207\n"
    "v_mfma_f32_16x16x32_bf16 v[200:203], a[192:195], v[56:59], v[200:203]\n"
    "s_waitcnt lgkmcnt(3)\n"
    "v_exp_f32 v208, v208\n"
    "v_mfma_f32_16x16x32_bf16 a[24:27], v[152:155], a[232:235], a[24:27]\n"
    "buffer_load_dwordx4 v6, s[48:51], s63 offen offset:1024 nt lds\n"
    "v_mfma_f32_16x16x32_bf16 a[24:27], v[156:159], a[236:239], a[24:27]\n"
    "v_exp_f32 v209, v209\n"
    "v_mfma_f32_16x16x32_bf16 a[28:31], v[164:167], a[236:239], a[28:31]\n"
    "ds_read_b64_tr_b16 a[226:227], v1 offset:4352\n"
    "v_cvt_pk_bf16_f32 v120, v208, v209\n"
    "v_mfma_f32_16x16x32_bf16 a[28:31], v[160:163], a[232:235], a[28:31]\n"
    "v_exp_f32 v210, v210\n"
    "v_smfmac_f32_16x16x64_bf16 a[244:247], a[240:243], v[152:159], v253\n"
    "v_mfma_f32_16x16x32_bf16 a[32:35], v[168:171], a[232:235], a[32:35]\n"
    "v_mfma_f32_16x16x32_bf16 a[32:35], v[172:175], a[236:239], a[32:35]\n"
    "v_exp_f32 v211, v211\n"
    "v_mfma_f32_16x16x32_bf16 a[36:39], v[180:183], a[236:239], a[36:39]\n"
    "ds_read_b64_tr_b16 a[228:229], v1 offset:8448\n"
    "v_cvt_pk_bf16_f32 v121, v210, v211\n"
    "v_mfma_f32_16x16x32_bf16 a[36:39], v[176:179], a[232:235], a[36:39]\n"
    "v_exp_f32 v200, v200\n"
    "v_mfma_f32_16x16x32_bf16 v[204:207], a[192:195], v[72:75], v[228:231]\n"
    "v_mfma_f32_16x16x32_bf16 v[204:207], a[196:199], v[76:79], v[204:207]\n"
    "v_exp_f32 v201, v201\n"
    "v_mfma_f32_16x16x32_bf16 v[204:207], a[200:203], v[80:83], v[204:207]\n"
    "ds_read_b64_tr_b16 a[230:231], v1 offset:12544\n"
    "v_cvt_pk_bf16_f32 v128, v200, v201\n"
    "v_mfma_f32_16x16x32_bf16 v[204:207], a[204:207], v[84:87], v[204:207]\n"
    "s_nop 0\n"
    "v_exp_f32 v202, v202\n"
    "v_mfma_f32_16x16x32_bf16 v[208:211], a[204:207], v[100:103], v[232:235]\n"
    "v_mfma_f32_16x16x32_bf16 v[208:211], a[200:203], v[96:99], v[208:211]\n"
    "v_exp_f32 v203, v203\n"
    "v_mfma_f32_16x16x32_bf16 v[208:211], a[196:199], v[92:95], v[208:211]\n"
    "v_cvt_pk_bf16_f32 v129, v202, v203\n"
    "v_mfma_f32_16x16x32_bf16 v[208:211], a[192:195], v[88:91], v[208:211]\n"
    "v_exp_f32 v204, v204\n"
    "v_mfma_f32_16x16x32_bf16 a[40:43], v[184:187], a[232:235], a[40:43]\n"
    "buffer_load_dwordx4 v7, s[48:51], s63 offen offset:2048 nt lds\n"
    "v_mfma_f32_16x16x32_bf16 a[40:43], v[188:191], a[236:239], a[40:43]\n"
    "v_exp_f32 v205, v205\n"
    "v_mfma_f32_16x16x32_bf16 a[44:47], v[196:199], a[236:239], a[44:47]\n"
    "s_waitcnt lgkmcnt(0)\n"
    "v_cvt_pk_bf16_f32 v136, v204, v205\n"
    "v_mfma_f32_16x16x32_bf16 a[44:47], v[192:195], a[232:235], a[44:47]\n"
    "s_waitcnt lgkmcnt(0)\n"
    "v_exp_f32 v206, v206\n"
    "v_mfma_f32_16x16x32_bf16 a[48:51], v[152:155], a[224:227], a[48:51]\n"
    "ds_read_b128 a[192:195], v0 offset:8192\n"
    "v_mfma_f32_16x16x32_bf16 a[48:51], v[156:159], a[228:231], a[48:51]\n"
    "v_exp_f32 v207, v207\n"
    "v_mfma_f32_16x16x32_bf16 a[52:55], v[164:167], a[228:231], a[52:55]\n"
    "ds_read_b64_tr_b16 a[232:233], v2 offset:256\n"
    "v_cvt_pk_bf16_f32 v137, v206, v207\n"
    "v_mfma_f32_16x16x32_bf16 a[52:55], v[160:163], a[224:227], a[52:55]\n"
    "v_exp_f32 v208, v208\n"
    "v_mfma_f32_16x16x32_bf16 v[200:203], a[208:211], v[8:11], v[212:215]\n"
    "v_mfma_f32_16x16x32_bf16 v[200:203], a[212:215], v[12:15], v[200:203]\n"
    "v_exp_f32 v209, v209\n"
    "v_mfma_f32_16x16x32_bf16 v[200:203], a[216:219], v[16:19], v[200:203]\n"
    "ds_read_b64_tr_b16 a[234:235], v2 offset:4352\n"
    "v_cvt_pk_bf16_f32 v144, v208, v209\n"
    "v_mfma_f32_16x16x32_bf16 v[200:203], a[220:223], v[20:23], v[200:203]\n"
    "v_exp_f32 v210, v210\n"
    "v_mfma_f32_16x16x32_bf16 v[204:207], a[220:223], v[36:39], v[216:219]\n"
    "ds_read_b128 a[196:199], v0 offset:8448\n"
    "v_mfma_f32_16x16x32_bf16 v[204:207], a[216:219], v[32:35], v[204:207]\n"
    "v_exp_f32 v211, v211\n"
    "v_mfma_f32_16x16x32_bf16 v[204:207], a[212:215], v[28:31], v[204:207]\n"
    "ds_read_b64_tr_b16 a[236:237], v2 offset:8448\n"
    "v_cvt_pk_bf16_f32 v145, v210, v211\n"
    "v_mfma_f32_16x16x32_bf16 v[204:207], a[208:211], v[24:27], v[204:207]\n"
    "v_exp_f32 v200, v200\n"
    "v_mfma_f32_16x16x32_bf16 a[56:59], v[168:171], a[224:227], a[56:59]\n"
    "ds_read_b64_tr_b16 v[248:249], v254 offset:0\n"
    "v_mfma_f32_16x16x32_bf16 a[56:59], v[172:175], a[228:231], a[56:59]\n"
    "v_exp_f32 v201, v201\n"
    "v_mfma_f32_16x16x32_bf16 a[60:63], v[180:183], a[228:231], a[60:63]\n"
    "ds_read_b64_tr_b16 a[238:239], v2 offset:12544\n"
    "v_cvt_pk_bf16_f32 v106, v200, v201\n"
    "v_mfma_f32_16x16x32_bf16 a[60:63], v[176:179], a[224:227], a[60:63]\n"
    "v_exp_f32 v202, v202\n"
    "v_smfmac_f32_16x16x64_bf16 a[248:251], a[240:243], v[160:167], v253\n"
    "ds_read_b64_tr_b16 v[250:251], v254 offset:32\n"
    "v_mfma_f32_16x16x32_bf16 a[64:67], v[184:187], a[224:227], a[64:67]\n"
    "ds_read_b128 a[200:203], v0 offset:8704\n"
    "v_mfma_f32_16x16x32_bf16 a[64:67], v[188:191], a[228:231], a[64:67]\n"
    "v_exp_f32 v203, v203\n"
    "v_mfma_f32_16x16x32_bf16 a[68:71], v[196:199], a[228:231], a[68:71]\n"
    "v_cvt_pk_bf16_f32 v107, v202, v203\n"
    "v_mfma_f32_16x16x32_bf16 a[68:71], v[192:195], a[224:227], a[68:71]\n"
    "v_exp_f32 v204, v204\n"
    "v_mfma_f32_16x16x32_bf16 v[208:211], a[208:211], v[40:43], v[220:223]\n"
    "buffer_load_dwordx4 v255, s[48:51], s63 offen offset:3072 nt lds\n"
    "v_mfma_f32_16x16x32_bf16 v[208:211], a[212:215], v[44:47], v[208:211]\n"
    "s_mov_b32 s68, s66\n"
    "v_exp_f32 v205, v205\n"
    "v_mfma_f32_16x16x32_bf16 v[208:211], a[216:219], v[48:51], v[208:211]\n"
    "s_add_u32 s66, s66, 16384\n"
    "v_cvt_pk_bf16_f32 v114, v204, v205\n"
    "s_cmp_eq_u32 s66, 163840\n"
    "v_mfma_f32_16x16x32_bf16 v[208:211], a[220:223], v[52:55], v[208:211]\n"
    "s_cselect_b32 s66, s79, s66\n"
    "v_exp_f32 v206, v206\n"
    "v_mfma_f32_16x16x32_bf16 v[200:203], a[220:223], v[68:71], v[224:227]\n"
    "ds_read_b128 a[204:207], v0 offset:8960\n"
    "s_mov_b32 s67, s66\n"
    "v_mfma_f32_16x16x32_bf16 v[200:203], a[216:219], v[64:67], v[200:203]\n"
    "s_waitcnt lgkmcnt(2)\n"
    "v_exp_f32 v207, v207\n"
    "v_mfma_f32_16x16x32_bf16 v[200:203], a[212:215], v[60:63], v[200:203]\n"
    "ds_read_b64_tr_b16 a[224:225], v1 offset:512\n"
    "v_cvt_pk_bf16_f32 v115, v206, v207\n"
    "v_mfma_f32_16x16x32_bf16 v[200:203], a[208:211], v[56:59], v[200:203]\n"
    "s_waitcnt lgkmcnt(3)\n"
    "v_exp_f32 v208, v208\n"
    "v_mfma_f32_16x16x32_bf16 a[72:75], v[152:155], a[232:235], a[72:75]\n"
    "buffer_store_dwordx4 v[248:251], v252, s[52:55], s64 offen offset:0 nt\n"
    "s_add_u32 s66, s66, 16384\n"
    "v_mfma_f32_16x16x32_bf16 a[72:75], v[156:159], a[236:239], a[72:75]\n"
    "s_cmp_eq_u32 s66, 163840\n"
    "v_exp_f32 v209, v209\n"
    "v_mfma_f32_16x16x32_bf16 a[76:79], v[164:167], a[236:239], a[76:79]\n"
    "ds_read_b64_tr_b16 a[226:227], v1 offset:4608\n"
    "v_cvt_pk_bf16_f32 v122, v208, v209\n"
    "v_mfma_f32_16x16x32_bf16 a[76:79], v[160:163], a[232:235], a[76:79]\n"
    "s_cselect_b32 s66, s79, s66\n"
    "v_exp_f32 v210, v210\n"
    "v_mfma_f32_16x16x32_bf16 a[80:83], v[168:171], a[232:235], a[80:83]\n"
    "s_bitcmp1_b32 s58, 0\n"
    "s_cselect_b64 s[48:49], s[44:45], s[40:41]\n"
    "s_lshl_b32 s75, s58, 13\n"
    "v_mfma_f32_16x16x32_bf16 a[80:83], v[172:175], a[236:239], a[80:83]\n"
    "s_and_b32 s75, s75, 0x3c000\n"
    "v_exp_f32 v211, v211\n"
    "v_mfma_f32_16x16x32_bf16 a[84:87], v[180:183], a[236:239], a[84:87]\n"
    "ds_read_b64_tr_b16 a[228:229], v1 offset:8704\n"
    "v_cvt_pk_bf16_f32 v123, v210, v211\n"
    "v_mfma_f32_16x16x32_bf16 a[84:87], v[176:179], a[232:235], a[84:87]\n"
    "s_add_u32 s63, s59, s75\n"
    "v_exp_f32 v200, v200\n"
    "v_mfma_f32_16x16x32_bf16 v[204:207], a[208:211], v[72:75], v[228:231]\n"
    "s_mov_b32 s50, s42\n"
    "s_add_u32 s65, s61, s62\n"
    "s_add_u32 s61, s61, 16384\n"
    "v_mfma_f32_16x16x32_bf16 v[204:207], a[212:215], v[76:79], v[204:207]\n"
    "s_cmp_eq_u32 s61, 163840\n"
    "v_exp_f32 v201, v201\n"
    "v_mfma_f32_16x16x32_bf16 v[204:207], a[216:219], v[80:83], v[204:207]\n"
    "ds_read_b64_tr_b16 a[230:231], v1 offset:12800\n"
    "v_cvt_pk_bf16_f32 v130, v200, v201\n"
    "v_mfma_f32_16x16x32_bf16 v[204:207], a[220:223], v[84:87], v[204:207]\n"
    "s_cselect_b32 s61, s79, s61\n"
    "v_exp_f32 v202, v202\n"
    "v_mfma_f32_16x16x32_bf16 v[208:211], a[220:223], v[100:103], v[232:235]\n"
    "s_add_i32 s58, s58, 1\n"
    "s_cmp_eq_u32 s69, 1\n"
    "s_cselect_b32 s50, 0, s50\n"
    "v_mfma_f32_16x16x32_bf16 v[208:211], a[216:219], v[96:99], v[208:211]\n"
    "s_mov_b32 m0, s65\n"
    "v_exp_f32 v203, v203\n"
    "v_mfma_f32_16x16x32_bf16 v[208:211], a[212:215], v[92:95], v[208:211]\n"
    "v_cvt_pk_bf16_f32 v131, v202, v203\n"
    "v_mfma_f32_16x16x32_bf16 v[208:211], a[208:211], v[88:91], v[208:211]\n"
    "v_exp_f32 v204, v204\n"
    "v_mfma_f32_16x16x32_bf16 a[88:91], v[184:187], a[232:235], a[88:91]\n"
    "buffer_load_dwordx4 v5, s[48:51], s63 offen nt lds\n"
    "v_mfma_f32_16x16x32_bf16 a[88:91], v[188:191], a[236:239], a[88:91]\n"
    "v_exp_f32 v205, v205\n"
    "v_mfma_f32_16x16x32_bf16 a[92:95], v[196:199], a[236:239], a[92:95]\n"
    "v_cvt_pk_bf16_f32 v138, v204, v205\n"
    "v_mfma_f32_16x16x32_bf16 a[92:95], v[192:195], a[232:235], a[92:95]\n"
    "s_waitcnt lgkmcnt(0)\n"
    "v_exp_f32 v206, v206\n"
    "v_smfmac_f32_16x16x64_bf16 a[252:255], a[240:243], v[168:175], v253\n"
    "s_waitcnt lgkmcnt(0)\n"
    "v_mfma_f32_16x16x32_bf16 a[96:99], v[152:155], a[224:227], a[96:99]\n"
    "ds_read_b128 a[208:211], v0 offset:12288\n"
    "v_mfma_f32_16x16x32_bf16 a[96:99], v[156:159], a[228:231], a[96:99]\n"
    "v_exp_f32 v207, v207\n"
    "v_mfma_f32_16x16x32_bf16 a[100:103], v[164:167], a[228:231], a[100:103]\n"
    "ds_read_b64_tr_b16 a[232:233], v2 offset:512\n"
    "v_cvt_pk_bf16_f32 v139, v206, v207\n"
    "v_mfma_f32_16x16x32_bf16 a[100:103], v[160:163], a[224:227], a[100:103]\n"
    "v_exp_f32 v208, v208\n"
    "v_mfma_f32_16x16x32_bf16 v[200:203], a[192:195], v[8:11], v[212:215]\n"
    "buffer_load_dwordx4 v6, s[48:51], s63 offen offset:1024 nt lds\n"
    "v_mfma_f32_16x16x32_bf16 v[200:203], a[196:199], v[12:15], v[200:203]\n"
    "v_exp_f32 v209, v209\n"
    "v_mfma_f32_16x16x32_bf16 v[200:203], a[200:203], v[16:19], v[200:203]\n"
    "ds_read_b64_tr_b16 a[234:235], v2 offset:4608\n"
    "v_cvt_pk_bf16_f32 v146, v208, v209\n"
    "v_mfma_f32_16x16x32_bf16 v[200:203], a[204:207], v[20:23], v[200:203]\n"
    "v_exp_f32 v210, v210\n"
    "v_mfma_f32_16x16x32_bf16 v[204:207], a[204:207], v[36:39], v[216:219]\n"
    "ds_read_b128 a[212:215], v0 offset:12544\n"
    "v_mfma_f32_16x16x32_bf16 v[204:207], a[200:203], v[32:35], v[204:207]\n"
    "v_exp_f32 v211, v211\n"
    "v_mfma_f32_16x16x32_bf16 v[204:207], a[196:199], v[28:31], v[204:207]\n"
    "ds_read_b64_tr_b16 a[236:237], v2 offset:8704\n"
    "v_cvt_pk_bf16_f32 v147, v210, v211\n"
    "v_mfma_f32_16x16x32_bf16 v[204:207], a[192:195], v[24:27], v[204:207]\n"
    "v_exp_f32 v200, v200\n"
    "v_mfma_f32_16x16x32_bf16 a[104:107], v[168:171], a[224:227], a[104:107]\n"
    "buffer_load_dwordx4 v7, s[48:51], s63 offen offset:2048 nt lds\n"
    "v_mfma_f32_16x16x32_bf16 a[104:107], v[172:175], a[228:231], a[104:107]\n"
    "v_exp_f32 v201, v201\n"
    "v_mfma_f32_16x16x32_bf16 a[108:111], v[180:183], a[228:231], a[108:111]\n"
    "ds_read_b64_tr_b16 a[238:239], v2 offset:12800\n"
    "v_cvt_pk_bf16_f32 v108, v200, v201\n"
    "v_mfma_f32_16x16x32_bf16 a[108:111], v[176:179], a[224:227], a[108:111]\n"
    "v_exp_f32 v202, v202\n"
    "v_mfma_f32_16x16x32_bf16 a[112:115], v[184:187], a[224:227], a[112:115]\n"
    "ds_read_b128 a[216:219], v0 offset:12800\n"
    "v_mfma_f32_16x16x32_bf16 a[112:115], v[188:191], a[228:231], a[112:115]\n"
    "v_exp_f32 v203, v203\n"
    "v_mfma_f32_16x16x32_bf16 a[116:119], v[196:199], a[228:231], a[116:119]\n"
    "v_cvt_pk_bf16_f32 v109, v202, v203\n"
    "v_mfma_f32_16x16x32_bf16 a[116:119], v[192:195], a[224:227], a[116:119]\n"
    "v_exp_f32 v204, v204\n"
    "v_mfma_f32_16x16x32_bf16 v[208:211], a[192:195], v[40:43], v[220:223]\n"
    "v_mfma_f32_16x16x32_bf16 v[208:211], a[196:199], v[44:47], v[208:211]\n"
    "v_exp_f32 v205, v205\n"
    "v_mfma_f32_16x16x32_bf16 v[208:211], a[200:203], v[48:51], v[208:211]\n"
    "v_cvt_pk_bf16_f32 v116, v204, v205\n"
    "v_mfma_f32_16x16x32_bf16 v[208:211], a[204:207], v[52:55], v[208:211]\n"
    "v_exp_f32 v206, v206\n"
    "v_mfma_f32_16x16x32_bf16 v[200:203], a[204:207], v[68:71], v[224:227]\n"
    "ds_read_b128 a[220:223], v0 offset:13056\n"
    "v_mfma_f32_16x16x32_bf16 v[200:203], a[200:203], v[64:67], v[200:203]\n"
    "v_exp_f32 v207, v207\n"
    "v_mfma_f32_16x16x32_bf16 v[200:203], a[196:199], v[60:63], v[200:203]\n"
    "ds_read_b64_tr_b16 a[224:225], v1 offset:768\n"
    "v_cvt_pk_bf16_f32 v117, v206, v207\n"
    "v_mfma_f32_16x16x32_bf16 v[200:203], a[192:195], v[56:59], v[200:203]\n"
    "s_waitcnt lgkmcnt(3)\n"
    "v_exp_f32 v208, v208\n"
    "v_mfma_f32_16x16x32_bf16 a[120:123], v[152:155], a[232:235], a[120:123]\n"
    "v_mfma_f32_16x16x32_bf16 a[120:123], v[156:159], a[236:239], a[120:123]\n"
    "v_exp_f32 v209, v209\n"
    "v_mfma_f32_16x16x32_bf16 a[124:127], v[164:167], a[236:239], a[124:127]\n"
    "ds_read_b64_tr_b16 a[226:227], v1 offset:4864\n"
    "v_cvt_pk_bf16_f32 v124, v208, v209\n"
    "v_mfma_f32_16x16x32_bf16 a[124:127], v[160:163], a[232:235], a[124:127]\n"
    "v_exp_f32 v210, v210\n"
    "v_smfmac_f32_16x16x64_bf16 v[236:239], a[240:243], v[176:183], v253\n"
    "buffer_load_dwordx4 v255, s[48:51], s63 offen offset:3072 nt lds\n"
    "v_mfma_f32_16x16x32_bf16 a[128:131], v[168:171], a[232:235], a[128:131]\n"
    "v_mfma_f32_16x16x32_bf16 a[128:131], v[172:175], a[236:239], a[128:131]\n"
    "v_exp_f32 v211, v211\n"
    "v_mfma_f32_16x16x32_bf16 a[132:135], v[180:183], a[236:239], a[132:135]\n"
    "ds_read_b64_tr_b16 a[228:229], v1 offset:8960\n"
    "v_cvt_pk_bf16_f32 v125, v210, v211\n"
    "v_mfma_f32_16x16x32_bf16 a[132:135], v[176:179], a[232:235], a[132:135]\n"
    "v_exp_f32 v200, v200\n"
    "v_mfma_f32_16x16x32_bf16 v[204:207], a[192:195], v[72:75], v[228:231]\n"
    "v_mfma_f32_16x16x32_bf16 v[204:207], a[196:199], v[76:79], v[204:207]\n"
    "v_exp_f32 v201, v201\n"
    "v_mfma_f32_16x16x32_bf16 v[204:207], a[200:203], v[80:83], v[204:207]\n"
    "ds_read_b64_tr_b16 a[230:231], v1 offset:13056\n"
    "v_cvt_pk_bf16_f32 v132, v200, v201\n"
    "v_mfma_f32_16x16x32_bf16 v[204:207], a[204:207], v[84:87], v[204:207]\n"
    "s_nop 0\n"
    "v_exp_f32 v202, v202\n"
    "v_mfma_f32_16x16x32_bf16 v[208:211], a[204:207], v[100:103], v[232:235]\n"
    "v_mfma_f32_16x16x32_bf16 v[208:211], a[200:203], v[96:99], v[208:211]\n"
    "v_exp_f32 v203, v203\n"
    "v_mfma_f32_16x16x32_bf16 v[208:211], a[196:199], v[92:95], v[208:211]\n"
    "v_cvt_pk_bf16_f32 v133, v202, v203\n"
    "v_mfma_f32_16x16x32_bf16 v[208:211], a[192:195], v[88:91], v[208:211]\n"
    "v_exp_f32 v204, v204\n"
    "v_mfma_f32_16x16x32_bf16 a[136:139], v[184:187], a[232:235], a[136:139]\n"
    "v_mfma_f32_16x16x32_bf16 a[136:139], v[188:191], a[236:239], a[136:139]\n"
    "v_exp_f32 v205, v205\n"
    "v_mfma_f32_16x16x32_bf16 a[140:143], v[196:199], a[236:239], a[140:143]\n"
    "s_waitcnt lgkmcnt(0)\n"
    "v_cvt_pk_bf16_f32 v140, v204, v205\n"
    "v_mfma_f32_16x16x32_bf16 a[140:143], v[192:195], a[232:235], a[140:143]\n"
    "s_waitcnt lgkmcnt(0)\n"
    "v_exp_f32 v206, v206\n"
    "v_mfma_f32_16x16x32_bf16 a[144:147], v[152:155], a[224:227], a[144:147]\n"
    "v_mfma_f32_16x16x32_bf16 a[144:147], v[156:159], a[228:231], a[144:147]\n"
    "v_exp_f32 v207, v207\n"
    "v_mfma_f32_16x16x32_bf16 a[148:151], v[164:167], a[228:231], a[148:151]\n"
    "ds_read_b64_tr_b16 a[232:233], v2 offset:768\n"
    "v_cvt_pk_bf16_f32 v141, v206, v207\n"
    "v_mfma_f32_16x16x32_bf16 a[148:151], v[160:163], a[224:227], a[148:151]\n"
    "v_exp_f32 v208, v208\n"
    "v_mfma_f32_16x16x32_bf16 v[200:203], a[208:211], v[8:11], v[212:215]\n"
    "v_mfma_f32_16x16x32_bf16 v[200:203], a[212:215], v[12:15], v[200:203]\n"
    "v_exp_f32 v209, v209\n"
    "v_mfma_f32_16x16x32_bf16 v[200:203], a[216:219], v[16:19], v[200:203]\n"
    "ds_read_b64_tr_b16 a[234:235], v2 offset:4864\n"
    "v_cvt_pk_bf16_f32 v148, v208, v209\n"
    "v_mfma_f32_16x16x32_bf16 v[200:203], a[220:223], v[20:23], v[200:203]\n"
    "v_exp_f32 v210, v210\n"
    "v_mfma_f32_16x16x32_bf16 v[204:207], a[220:223], v[36:39], v[216:219]\n"
    "v_mfma_f32_16x16x32_bf16 v[204:207], a[216:219], v[32:35], v[204:207]\n"
    "v_exp_f32 v211, v211\n"
    "v_mfma_f32_16x16x32_bf16 v[204:207], a[212:215], v[28:31], v[204:207]\n"
    "ds_read_b64_tr_b16 a[236:237], v2 offset:8960\n"
    "v_cvt_pk_bf16_f32 v149, v210, v211\n"
    "v_mfma_f32_16x16x32_bf16 v[204:207], a[208:211], v[24:27], v[204:207]\n"
    "v_exp_f32 v200, v200\n"
    "v_mfma_f32_16x16x32_bf16 a[152:155], v[168:171], a[224:227], a[152:155]\n"
    "v_mfma_f32_16x16x32_bf16 a[152:155], v[172:175], a[228:231], a[152:155]\n"
    "v_exp_f32 v201, v201\n"
    "v_mfma_f32_16x16x32_bf16 a[156:159], v[180:183], a[228:231], a[156:159]\n"
    "ds_read_b64_tr_b16 a[238:239], v2 offset:13056\n"
    "v_cvt_pk_bf16_f32 v110, v200, v201\n"
    "v_mfma_f32_16x16x32_bf16 a[156:159], v[176:179], a[224:227], a[156:159]\n"
    "v_exp_f32 v202, v202\n"
    "v_smfmac_f32_16x16x64_bf16 v[240:243], a[240:243], v[184:191], v253\n"
    "v_mfma_f32_16x16x32_bf16 a[160:163], v[184:187], a[224:227], a[160:163]\n"
    "v_add_u32 v0, s67, v3\n"
    "v_add_u32 v1, s68, v4\n"
    "v_mfma_f32_16x16x32_bf16 a[160:163], v[188:191], a[228:231], a[160:163]\n"
    "v_exp_f32 v203, v203\n"
    "v_mfma_f32_16x16x32_bf16 a[164:167], v[196:199], a[228:231], a[164:167]\n"
    "v_cvt_pk_bf16_f32 v111, v202, v203\n"
    "v_mfma_f32_16x16x32_bf16 a[164:167], v[192:195], a[224:227], a[164:167]\n"
    "s_waitcnt lgkmcnt(0)\n"
    "v_exp_f32 v204, v204\n"
    "v_mfma_f32_16x16x32_bf16 v[208:211], a[208:211], v[40:43], v[220:223]\n"
    "v_xor_b32 v2, 32, v1\n"
    "v_mfma_f32_16x16x32_bf16 v[208:211], a[212:215], v[44:47], v[208:211]\n"
    "s_waitcnt vmcnt(9)\n"
    "v_exp_f32 v205, v205\n"
    "v_mfma_f32_16x16x32_bf16 v[208:211], a[216:219], v[48:51], v[208:211]\n"
    "v_cvt_pk_bf16_f32 v118, v204, v205\n"
    "v_mfma_f32_16x16x32_bf16 v[208:211], a[220:223], v[52:55], v[208:211]\n"
    "s_barrier\n"
    "v_exp_f32 v206, v206\n"
    "v_mfma_f32_16x16x32_bf16 v[200:203], a[220:223], v[68:71], v[224:227]\n"
    "ds_read_b128 a[192:195], v0 offset:0\n"
    "v_mfma_f32_16x16x32_bf16 v[200:203], a[216:219], v[64:67], v[200:203]\n"
    "v_exp_f32 v207, v207\n"
    "v_mfma_f32_16x16x32_bf16 v[200:203], a[212:215], v[60:63], v[200:203]\n"
    "ds_read_b64_tr_b16 a[224:225], v1 offset:0\n"
    "v_cvt_pk_bf16_f32 v119, v206, v207\n"
    "v_mfma_f32_16x16x32_bf16 v[200:203], a[208:211], v[56:59], v[200:203]\n"
    "v_exp_f32 v208, v208\n"
    "v_mfma_f32_16x16x32_bf16 a[168:171], v[152:155], a[232:235], a[168:171]\n"
    "ds_read_b128 a[196:199], v0 offset:256\n"
    "v_mfma_f32_16x16x32_bf16 a[168:171], v[156:159], a[236:239], a[168:171]\n"
    "v_exp_f32 v209, v209\n"
    "v_mfma_f32_16x16x32_bf16 a[172:175], v[164:167], a[236:239], a[172:175]\n"
    "ds_read_b64_tr_b16 a[226:227], v1 offset:4096\n"
    "v_cvt_pk_bf16_f32 v126, v208, v209\n"
    "v_mfma_f32_16x16x32_bf16 a[172:175], v[160:163], a[232:235], a[172:175]\n"
    "v_exp_f32 v210, v210\n"
    "v_mfma_f32_16x16x32_bf16 a[176:179], v[168:171], a[232:235], a[176:179]\n"
    "ds_read_b128 a[200:203], v0 offset:512\n"
    "s_cmp_lg_u32 s69, 1\n"
    "v_mfma_f32_16x16x32_bf16 a[176:179], v[172:175], a[236:239], a[176:179]\n"
    "v_exp_f32 v211, v211\n"
    "v_mfma_f32_16x16x32_bf16 a[180:183], v[180:183], a[236:239], a[180:183]\n"
    "ds_read_b64_tr_b16 a[228:229], v1 offset:8192\n"
    "v_cvt_pk_bf16_f32 v127, v210, v211\n"
    "v_mfma_f32_16x16x32_bf16 a[180:183], v[176:179], a[232:235], a[180:183]\n"
    "v_exp_f32 v200, v200\n"
    "v_mfma_f32_16x16x32_bf16 v[204:207], a[208:211], v[72:75], v[228:231]\n"
    "ds_read_b128 a[204:207], v0 offset:768\n"
    "v_mfma_f32_16x16x32_bf16 v[204:207], a[212:215], v[76:79], v[204:207]\n"
    "v_exp_f32 v201, v201\n"
    "v_mfma_f32_16x16x32_bf16 v[204:207], a[216:219], v[80:83], v[204:207]\n"
    "ds_read_b64_tr_b16 a[230:231], v1 offset:12288\n"
    "v_cvt_pk_bf16_f32 v134, v200, v201\n"
    "v_mfma_f32_16x16x32_bf16 v[204:207], a[220:223], v[84:87], v[204:207]\n"
    "s_nop 0\n"
    "v_exp_f32 v202, v202\n"
    "v_mfma_f32_16x16x32_bf16 v[208:211], a[220:223], v[100:103], v[232:235]\n"
    "v_mfma_f32_16x16x32_bf16 v[208:211], a[216:219], v[96:99], v[208:211]\n"
    "v_exp_f32 v203, v203\n"
    "v_mfma_f32_16x16x32_bf16 v[208:211], a[212:215], v[92:95], v[208:211]\n"
    "v_cvt_pk_bf16_f32 v135, v202, v203\n"
    "v_mfma_f32_16x16x32_bf16 v[208:211], a[208:211], v[88:91], v[208:211]\n"
    "v_exp_f32 v204, v204\n"
    "v_mfma_f32_16x16x32_bf16 a[184:187], v[184:187], a[232:235], a[184:187]\n"
    "v_mfma_f32_16x16x32_bf16 a[184:187], v[188:191], a[236:239], a[184:187]\n"
    "v_exp_f32 v205, v205\n"
    "v_mfma_f32_16x16x32_bf16 a[188:191], v[196:199], a[236:239], a[188:191]\n"
    "v_cvt_pk_bf16_f32 v142, v204, v205\n"
    "v_mfma_f32_16x16x32_bf16 a[188:191], v[192:195], a[232:235], a[188:191]\n"
    "s_waitcnt lgkmcnt(0)\n"
    "v_exp_f32 v206, v206\n"
    "v_smfmac_f32_16x16x64_bf16 v[244:247], a[240:243], v[192:199], v253\n"
    "s_cbranch_scc1 Lloop2_%=\n"
    "; ---- end: QK(15), PV(14), PV(15), epilogue\n"
    "v_mfma_f32_16x16x32_bf16 v[200:203], a[192:195], v[8:11], v[212:215]\n"
    "s_mov_b64 s[70:71], 0\n"
    "v_exp_f32 v207, v207\n"
    "v_mfma_f32_16x16x32_bf16 v[200:203], a[196:199], v[12:15], v[200:203]\n"
    "ds_read_b64_tr_b16 a[232:233], v2 offset:0\n"
    "v_cvt_pk_bf16_f32 v143, v206, v207\n"
    "v_mfma_f32_16x16x32_bf16 v[200:203], a[200:203], v[16:19], v[200:203]\n"
    "ds_read_b64_tr_b16 a[234:235], v2 offset:4096\n"
    "v_exp_f32 v208, v208\n"
    "v_mfma_f32_16x16x32_bf16 v[200:203], a[204:207], v[20:23], v[200:203]\n"
    "ds_read_b64_tr_b16 a[236:237], v2 offset:8192\n"
    "v_exp_f32 v209, v209\n"
    "v_mfma_f32_16x16x32_bf16 v[204:207], a[192:195], v[24:27], v[216:219]\n"
    "ds_read_b64_tr_b16 a[238:239], v2 offset:12288\n"
    "v_exp_f32 v210, v210\n"
    "v_mfma_f32_16x16x32_bf16 v[204:207], a[196:199], v[28:31], v[204:207]\n"
    "ds_read_b64_tr_b16 v[248:249], v254 offset:1024\n"
    "v_exp_f32 v211, v211\n"
    "v_mfma_f32_16x16x32_bf16 v[204:207], a[200:203], v[32:35], v[204:207]\n"
    "ds_read_b64_tr_b16 v[250:251], v254 offset:1056\n"
    "v_cvt_pk_bf16_f32 v150, v208, v209\n"
    "v_mfma_f32_16x16x32_bf16 v[204:207], a[204:207], v[36:39], v[204:207]\n"
    "ds_read_b128 a[208:211], v0 offset:4096\n"
    "v_cvt_pk_bf16_f32 v151, v210, v211\n"
    "v_mfma_f32_16x16x32_bf16 v[208:211], a[192:195], v[40:43], v[220:223]\n"
    "ds_read_b128 a[212:215], v0 offset:4352\n"
    "v_add_u32 v5, 81920, v4\n"
    "v_mfma_f32_16x16x32_bf16 v[208:211], a[196:199], v[44:47], v[208:211]\n"
    "ds_read_b128 a[216:219], v0 offset:4608\n"
    "v_xor_b32 v6, 32, v5\n"
    "v_mfma_f32_16x16x32_bf16 v[208:211], a[200:203], v[48:51], v[208:211]\n"
    "ds_read_b128 a[220:223], v0 offset:4864\n"
    "v_subrev_u32 v254, 51200, v254\n"
    "v_exp_f32 v200, v200\n"
    "v_mfma_f32_16x16x32_bf16 v[208:211], a[204:207], v[52:55], v[208:211]\n"
    "v_exp_f32 v201, v201\n"
    "v_exp_f32 v202, v202\n"
    "v_smfmac_f32_16x16x64_bf16 a[244:247], a[240:243], v[104:111], v253\n"
    "v_exp_f32 v203, v203\n"
    "v_mfma_f32_16x16x32_bf16 a[0:3], v[104:107], a[224:227], a[0:3]\n"
    "v_cvt_pk_bf16_f32 v152, v200, v201\n"
    "v_cvt_pk_bf16_f32 v153, v202, v203\n"
    "v_mfma_f32_16x16x32_bf16 v[200:203], a[192:195], v[72:75], v[228:231]\n"
    "s_waitcnt lgkmcnt(4)\n"
    "buffer_store_dwordx4 v[248:251], v252, s[52:55], s64 offen offset:1024 nt\n"
    "v_exp_f32 v204, v204\n"
    "v_mfma_f32_16x16x32_bf16 v[200:203], a[196:199], v[76:79], v[200:203]\n"
    "v_exp_f32 v205, v205\n"
    "v_exp_f32 v206, v206\n"
    "v_mfma_f32_16x16x32_bf16 v[248:251], a[192:195], v[56:59], v[224:227]\n"
    "v_exp_f32 v207, v207\n"
    "v_cvt_pk_bf16_f32 v160, v204, v205\n"
    "v_mfma_f32_16x16x32_bf16 v[248:251], a[196:199], v[60:63], v[248:251]\n"
    "v_cvt_pk_bf16_f32 v161, v206, v207\n"
    "v_exp_f32 v208, v208\n"
    "v_mfma_f32_16x16x32_bf16 v[248:251], a[200:203], v[64:67], v[248:251]\n"
    "v_exp_f32 v209, v209\n"
    "v_exp_f32 v210, v210\n"
    "v_mfma_f32_16x16x32_bf16 v[248:251], a[204:207], v[68:71], v[248:251]\n"
    "v_exp_f32 v211, v211\n"
    "v_cvt_pk_bf16_f32 v168, v208, v209\n"
    "v_mfma_f32_16x16x32_bf16 v[200:203], a[200:203], v[80:83], v[200:203]\n"
    "v_cvt_pk_bf16_f32 v169, v210, v211\n"
    "v_mfma_f32_16x16x32_bf16 v[200:203], a[204:207], v[84:87], v[200:203]\n"
    "v_mfma_f32_16x16x32_bf16 v[204:207], a[192:195], v[88:91], v[232:235]\n"
    "ds_read_b128 a[192:195], v0 offset:8192\n"
    "v_mfma_f32_16x16x32_bf16 v[204:207], a[196:199], v[92:95], v[204:207]\n"
    "ds_read_b128 a[196:199], v0 offset:8448\n"
    "v_exp_f32 v248, v248\n"
    "v_mfma_f32_16x16x32_bf16 v[204:207], a[200:203], v[96:99], v[204:207]\n"
    "ds_read_b128 a[200:203], v0 offset:8704\n"
    "v_exp_f32 v249, v249\n"
    "v_mfma_f32_16x16x32_bf16 v[204:207], a[204:207], v[100:103], v[204:207]\n"
    "ds_read_b128 a[204:207], v0 offset:8960\n"
    "v_exp_f32 v250, v250\n"
    "s_waitcnt lgkmcnt(4)\n"
    "v_mfma_f32_16x16x32_bf16 v[208:211], a[208:211], v[8:11], v[212:215]\n"
    "v_exp_f32 v251, v251\n"
    "v_cvt_pk_bf16_f32 v176, v248, v249\n"
    "v_mfma_f32_16x16x32_bf16 v[208:211], a[212:215], v[12:15], v[208:211]\n"
    "v_cvt_pk_bf16_f32 v177, v250, v251\n"
    "v_exp_f32 v200, v200\n"
    "v_mfma_f32_16x16x32_bf16 v[208:211], a[216:219], v[16:19], v[208:211]\n"
    "v_exp_f32 v201, v201\n"
    "v_exp_f32 v202, v202\n"
    "v_mfma_f32_16x16x32_bf16 v[208:211], a[220:223], v[20:23], v[208:211]\n"
    "v_exp_f32 v203, v203\n"
    "v_cvt_pk_bf16_f32 v184, v200, v201\n"
    "v_mfma_f32_16x16x32_bf16 v[248:251], a[208:211], v[24:27], v[216:219]\n"
    "v_cvt_pk_bf16_f32 v185, v202, v203\n"
    "v_exp_f32 v204, v204\n"
    "v_mfma_f32_16x16x32_bf16 v[248:251], a[212:215], v[28:31], v[248:251]\n"
    "v_exp_f32 v205, v205\n"
    "v_exp_f32 v206, v206\n"
    "v_mfma_f32_16x16x32_bf16 v[248:251], a[216:219], v[32:35], v[248:251]\n"
    "v_exp_f32 v207, v207\n"
    "v_cvt_pk_bf16_f32 v192, v204, v205\n"
    "v_mfma_f32_16x16x32_bf16 v[248:251], a[220:223], v[36:39], v[248:251]\n"
    "v_cvt_pk_bf16_f32 v193, v206, v207\n"
    "v_exp_f32 v208, v208\n"
    "v_mfma_f32_16x16x32_bf16 v[200:203], a[208:211], v[40:43], v[220:223]\n"
    "v_exp_f32 v209, v209\n"
    "v_exp_f32 v210, v210\n"
    "v_mfma_f32_16x16x32_bf16 v[200:203], a[212:215], v[44:47], v[200:203]\n"
    "v_exp_f32 v211, v211\n"
    "v_cvt_pk_bf16_f32 v154, v208, v209\n"
    "v_mfma_f32_16x16x32_bf16 v[200:203], a[216:219], v[48:51], v[200:203]\n"
    "v_cvt_pk_bf16_f32 v155, v210, v211\n"
    "v_exp_f32 v248, v248\n"
    "v_mfma_f32_16x16x32_bf16 v[200:203], a[220:223], v[52:55], v[200:203]\n"
    "v_exp_f32 v249, v249\n"
    "v_exp_f32 v250, v250\n"
    "v_mfma_f32_16x16x32_bf16 v[204:207], a[208:211], v[56:59], v[224:227]\n"
    "v_exp_f32 v251, v251\n"
    "v_cvt_pk_bf16_f32 v162, v248, v249\n"
    "v_mfma_f32_16x16x32_bf16 v[204:207], a[212:215], v[60:63], v[204:207]\n"
    "v_cvt_pk_bf16_f32 v163, v250, v251\n"
    "v_mfma_f32_16x16x32_bf16 v[204:207], a[216:219], v[64:67], v[204:207]\n"
    "v_exp_f32 v200, v200\n"
    "v_exp_f32 v201, v201\n"
    "v_mfma_f32_16x16x32_bf16 v[204:207], a[220:223], v[68:71], v[204:207]\n"
    "v_exp_f32 v202, v202\n"
    "v_exp_f32 v203, v203\n"
    "v_mfma_f32_16x16x32_bf16 v[208:211], a[208:211], v[72:75], v[228:231]\n"
    "v_cvt_pk_bf16_f32 v170, v200, v201\n"
    "v_cvt_pk_bf16_f32 v171, v202, v203\n"
    "v_mfma_f32_16x16x32_bf16 v[208:211], a[212:215], v[76:79], v[208:211]\n"
    "v_mfma_f32_16x16x32_bf16 v[208:211], a[216:219], v[80:83], v[208:211]\n"
    "v_mfma_f32_16x16x32_bf16 v[208:211], a[220:223], v[84:87], v[208:211]\n"
    "v_exp_f32 v204, v204\n"
    "v_mfma_f32_16x16x32_bf16 v[248:251], a[208:211], v[88:91], v[232:235]\n"
    "s_waitcnt vmcnt(6)\n"
    "v_exp_f32 v205, v205\n"
    "v_mfma_f32_16x16x32_bf16 v[248:251], a[212:215], v[92:95], v[248:251]\n"
    "s_barrier\n"
    "v_exp_f32 v206, v206\n"
    "v_mfma_f32_16x16x32_bf16 v[248:251], a[216:219], v[96:99], v[248:251]\n"
    "ds_read_b128 a[208:211], v0 offset:12288\n"
    "v_exp_f32 v207, v207\n"
    "v_mfma_f32_16x16x32_bf16 v[248:251], a[220:223], v[100:103], v[248:251]\n"
    "ds_read_b128 a[212:215], v0 offset:12544\n"
    "v_cvt_pk_bf16_f32 v178, v204, v205\n"
    "s_waitcnt lgkmcnt(2)\n"
    "v_mfma_f32_16x16x32_bf16 v[200:203], a[192:195], v[8:11], v[212:215]\n"
    "ds_read_b128 a[216:219], v0 offset:12800\n"
    "v_cvt_pk_bf16_f32 v179, v206, v207\n"
    "v_mfma_f32_16x16x32_bf16 v[200:203], a[196:199], v[12:15], v[200:203]\n"
    "ds_read_b128 a[220:223], v0 offset:13056\n"
    "v_exp_f32 v208, v208\n"
    "v_mfma_f32_16x16x32_bf16 v[200:203], a[200:203], v[16:19], v[200:203]\n"
    "v_exp_f32 v209, v209\n"
    "v_exp_f32 v210, v210\n"
    "v_mfma_f32_16x16x32_bf16 v[200:203], a[204:207], v[20:23], v[200:203]\n"
    "v_exp_f32 v211, v211\n"
    "v_cvt_pk_bf16_f32 v186, v208, v209\n"
    "v_mfma_f32_16x16x32_bf16 v[204:207], a[192:195], v[24:27], v[216:219]\n"
    "v_cvt_pk_bf16_f32 v187, v210, v211\n"
    "v_exp_f32 v248, v248\n"
    "v_mfma_f32_16x16x32_bf16 v[204:207], a[196:199], v[28:31], v[204:207]\n"
    "v_exp_f32 v249, v249\n"
    "v_exp_f32 v250, v250\n"
    "v_mfma_f32_16x16x32_bf16 v[204:207], a[200:203], v[32:35], v[204:207]\n"
    "v_exp_f32 v251, v251\n"
    "v_cvt_pk_bf16_f32 v194, v248, v249\n"
    "v_mfma_f32_16x16x32_bf16 v[204:207], a[204:207], v[36:39], v[204:207]\n"
    "v_cvt_pk_bf16_f32 v195, v250, v251\n"
    "v_exp_f32 v200, v200\n"
    "v_mfma_f32_16x16x32_bf16 v[208:211], a[192:195], v[40:43], v[220:223]\n"
    "v_exp_f32 v201, v201\n"
    "v_exp_f32 v202, v202\n"
    "v_mfma_f32_16x16x32_bf16 v[208:211], a[196:199], v[44:47], v[208:211]\n"
    "v_exp_f32 v203, v203\n"
    "v_cvt_pk_bf16_f32 v156, v200, v201\n"
    "v_mfma_f32_16x16x32_bf16 v[208:211], a[200:203], v[48:51], v[208:211]\n"
    "v_cvt_pk_bf16_f32 v157, v202, v203\n"
    "v_exp_f32 v204, v204\n"
    "v_mfma_f32_16x16x32_bf16 v[208:211], a[204:207], v[52:55], v[208:211]\n"
    "v_exp_f32 v205, v205\n"
    "v_exp_f32 v206, v206\n"
    "v_mfma_f32_16x16x32_bf16 v[248:251], a[192:195], v[56:59], v[224:227]\n"
    "v_exp_f32 v207, v207\n"
    "v_cvt_pk_bf16_f32 v164, v204, v205\n"
    "v_mfma_f32_16x16x32_bf16 v[248:251], a[196:199], v[60:63], v[248:251]\n"
    "v_cvt_pk_bf16_f32 v165, v206, v207\n"
    "v_mfma_f32_16x16x32_bf16 v[248:251], a[200:203], v[64:67], v[248:251]\n"
    "v_exp_f32 v208, v208\n"
    "v_exp_f32 v209, v209\n"
    "v_mfma_f32_16x16x32_bf16 v[248:251], a[204:207], v[68:71], v[248:251]\n"
    "v_exp_f32 v210, v210\n"
    "v_exp_f32 v211, v211\n"
    "v_mfma_f32_16x16x32_bf16 v[200:203], a[192:195], v[72:75], v[228:231]\n"
    "v_cvt_pk_bf16_f32 v172, v208, v209\n"
    "v_cvt_pk_bf16_f32 v173, v210, v211\n"
    "v_mfma_f32_16x16x32_bf16 v[200:203], a[196:199], v[76:79], v[200:203]\n"
    "v_mfma_f32_16x16x32_bf16 v[200:203], a[200:203], v[80:83], v[200:203]\n"
    "v_mfma_f32_16x16x32_bf16 v[200:203], a[204:207], v[84:87], v[200:203]\n"
    "v_exp_f32 v248, v248\n"
    "v_exp_f32 v249, v249\n"
    "v_mfma_f32_16x16x32_bf16 v[204:207], a[192:195], v[88:91], v[232:235]\n"
    "ds_read_b64_tr_b16 a[192:193], v5 offset:0\n"
    "v_exp_f32 v250, v250\n"
    "v_mfma_f32_16x16x32_bf16 v[204:207], a[196:199], v[92:95], v[204:207]\n"
    "ds_read_b64_tr_b16 a[194:195], v5 offset:4096\n"
    "v_exp_f32 v251, v251\n"
    "v_mfma_f32_16x16x32_bf16 v[204:207], a[200:203], v[96:99], v[204:207]\n"
    "ds_read_b64_tr_b16 a[196:197], v5 offset:8192\n"
    "v_cvt_pk_bf16_f32 v180, v248, v249\n"
    "v_mfma_f32_16x16x32_bf16 v[204:207], a[204:207], v[100:103], v[204:207]\n"
    "ds_read_b64_tr_b16 a[198:199], v5 offset:12288\n"
    "v_cvt_pk_bf16_f32 v181, v250, v251\n"
    "s_waitcnt lgkmcnt(4)\n"
    "v_mfma_f32_16x16x32_bf16 v[208:211], a[208:211], v[8:11], v[212:215]\n"
    "ds_read_b64_tr_b16 a[200:201], v6 offset:0\n"
    "v_exp_f32 v200, v200\n"
    "v_mfma_f32_16x16x32_bf16 v[208:211], a[212:215], v[12:15], v[208:211]\n"
    "ds_read_b64_tr_b16 a[202:203], v6 offset:4096\n"
    "v_exp_f32 v201, v201\n"
    "v_mfma_f32_16x16x32_bf16 v[208:211], a[216:219], v[16:19], v[208:211]\n"
    "ds_read_b64_tr_b16 a[204:205], v6 offset:8192\n"
    "v_exp_f32 v202, v202\n"
    "v_mfma_f32_16x16x32_bf16 v[208:211], a[220:223], v[20:23], v[208:211]\n"
    "ds_read_b64_tr_b16 a[206:207], v6 offset:12288\n"
    "v_exp_f32 v203, v203\n"
    "v_mfma_f32_16x16x32_bf16 v[248:251], a[208:211], v[24:27], v[216:219]\n"
    "ds_read_b64_tr_b16 v[8:9], v1 offset:256\n"
    "v_cvt_pk_bf16_f32 v188, v200, v201\n"
    "v_mfma_f32_16x16x32_bf16 v[248:251], a[212:215], v[28:31], v[248:251]\n"
    "ds_read_b64_tr_b16 v[10:11], v1 offset:4352\n"
    "v_cvt_pk_bf16_f32 v189, v202, v203\n"
    "v_mfma_f32_16x16x32_bf16 v[248:251], a[216:219], v[32:35], v[248:251]\n"
    "ds_read_b64_tr_b16 v[12:13], v1 offset:8448\n"
    "v_exp_f32 v204, v204\n"
    "v_mfma_f32_16x16x32_bf16 v[248:251], a[220:223], v[36:39], v[248:251]\n"
    "ds_read_b64_tr_b16 v[14:15], v1 offset:12544\n"
    "v_exp_f32 v205, v205\n"
    "v_mfma_f32_16x16x32_bf16 v[200:203], a[208:211], v[40:43], v[220:223]\n"
    "ds_read_b64_tr_b16 v[16:17], v5 offset:256\n"
    "v_exp_f32 v206, v206\n"
    "v_mfma_f32_16x16x32_bf16 v[200:203], a[212:215], v[44:47], v[200:203]\n"
    "ds_read_b64_tr_b16 v[18:19], v5 offset:4352\n"
    "v_exp_f32 v207, v207\n"
    "v_mfma_f32_16x16x32_bf16 v[200:203], a[216:219], v[48:51], v[200:203]\n"
    "ds_read_b64_tr_b16 v[20:21], v5 offset:8448\n"
    "v_cvt_pk_bf16_f32 v196, v204, v205\n"
    "v_mfma_f32_16x16x32_bf16 v[200:203], a[220:223], v[52:55], v[200:203]\n"
    "ds_read_b64_tr_b16 v[22:23], v5 offset:12544\n"
    "v_cvt_pk_bf16_f32 v197, v206, v207\n"
    "v_mfma_f32_16x16x32_bf16 v[204:207], a[208:211], v[56:59], v[224:227]\n"
    "ds_read_b64_tr_b16 v[24:25], v2 offset:256\n"
    "v_exp_f32 v208, v208\n"
    "v_mfma_f32_16x16x32_bf16 v[204:207], a[212:215], v[60:63], v[204:207]\n"
    "ds_read_b64_tr_b16 v[26:27], v2 offset:4352\n"
    "v_exp_f32 v209, v209\n"
    "v_mfma_f32_16x16x32_bf16 v[204:207], a[216:219], v[64:67], v[204:207]\n"
    "ds_read_b64_tr_b16 v[28:29], v2 offset:8448\n"
    "v_exp_f32 v210, v210\n"
    "v_mfma_f32_16x16x32_bf16 v[204:207], a[220:223], v[68:71], v[204:207]\n"
    "ds_read_b64_tr_b16 v[30:31], v2 offset:12544\n"
    "v_exp_f32 v211, v211\n"
    "v_mfma_f32_16x16x32_bf16 a[0:3], v[108:111], a[228:231], a[0:3]\n"
    "ds_read_b64_tr_b16 v[32:33], v6 offset:256\n"
    "v_cvt_pk_bf16_f32 v158, v208, v209\n"
    "s_waitcnt lgkmcnt(7)\n"
    "v_mfma_f32_16x16x32_bf16 a[0:3], v[152:155], a[192:195], a[0:3]\n"
    "ds_read_b64_tr_b16 v[34:35], v6 offset:4352\n"
    "v_cvt_pk_bf16_f32 v159, v210, v211\n"
    "v_mfma_f32_16x16x32_bf16 v[208:211], a[208:211], v[72:75], v[228:231]\n"
    "ds_read_b64_tr_b16 v[36:37], v6 offset:8448\n"
    "v_exp_f32 v248, v248\n"
    "v_mfma_f32_16x16x32_bf16 v[208:211], a[212:215], v[76:79], v[208:211]\n"
    "ds_read_b64_tr_b16 v[38:39], v6 offset:12544\n"
    "v_exp_f32 v249, v249\n"
    "v_mfma_f32_16x16x32_bf16 v[208:211], a[216:219], v[80:83], v[208:211]\n"
    "ds_read_b64_tr_b16 v[40:41], v1 offset:512\n"
    "v_exp_f32 v250, v250\n"
    "v_mfma_f32_16x16x32_bf16 v[208:211], a[220:223], v[84:87], v[208:211]\n"
    "ds_read_b64_tr_b16 v[42:43], v1 offset:4608\n"
    "v_exp_f32 v251, v251\n"
    "v_smfmac_f32_16x16x64_bf16 a[244:247], a[240:243], v[152:159], v253\n"
    "ds_read_b64_tr_b16 v[44:45], v1 offset:8704\n"
    "v_cvt_pk_bf16_f32 v166, v248, v249\n"
    "v_mfma_f32_16x16x32_bf16 a[0:3], v[156:159], a[196:199], a[0:3]\n"
    "ds_read_b64_tr_b16 v[46:47], v1 offset:12800\n"
    "v_cvt_pk_bf16_f32 v167, v250, v251\n"
    "v_mfma_f32_16x16x32_bf16 v[248:251], a[208:211], v[88:91], v[232:235]\n"
    "ds_read_b64_tr_b16 v[48:49], v5 offset:512\n"
    "v_exp_f32 v200, v200\n"
    "v_mfma_f32_16x16x32_bf16 v[248:251], a[212:215], v[92:95], v[248:251]\n"
    "ds_read_b64_tr_b16 v[50:51], v5 offset:4608\n"
    "v_exp_f32 v201, v201\n"
    "v_mfma_f32_16x16x32_bf16 v[248:251], a[216:219], v[96:99], v[248:251]\n"
    "ds_read_b64_tr_b16 v[52:53], v5 offset:8704\n"
    "v_exp_f32 v202, v202\n"
    "v_mfma_f32_16x16x32_bf16 v[248:251], a[220:223], v[100:103], v[248:251]\n"
    "ds_read_b64_tr_b16 v[54:55], v5 offset:12800\n"
    "v_exp_f32 v203, v203\n"
    "v_mfma_f32_16x16x32_bf16 a[24:27], v[104:107], a[232:235], a[24:27]\n"
    "ds_read_b64_tr_b16 v[56:57], v2 offset:512\n"
    "v_cvt_pk_bf16_f32 v174, v200, v201\n"
    "v_mfma_f32_16x16x32_bf16 a[24:27], v[108:111], a[236:239], a[24:27]\n"
    "ds_read_b64_tr_b16 v[58:59], v2 offset:4608\n"
    "v_cvt_pk_bf16_f32 v175, v202, v203\n"
    "v_mfma_f32_16x16x32_bf16 a[24:27], v[152:155], a[200:203], a[24:27]\n"
    "ds_read_b64_tr_b16 v[60:61], v2 offset:8704\n"
    "v_exp_f32 v204, v204\n"
    "v_mfma_f32_16x16x32_bf16 a[24:27], v[156:159], a[204:207], a[24:27]\n"
    "ds_read_b64_tr_b16 v[62:63], v2 offset:12800\n"
    "v_exp_f32 v205, v205\n"
    "v_mfma_f32_16x16x32_bf16 a[48:51], v[104:107], v[8:11], a[48:51]\n"
    "ds_read_b64_tr_b16 v[64:65], v6 offset:512\n"
    "v_exp_f32 v206, v206\n"
    "v_mfma_f32_16x16x32_bf16 a[48:51], v[108:111], v[12:15], a[48:51]\n"
    "ds_read_b64_tr_b16 v[66:67], v6 offset:4608\n"
    "v_exp_f32 v207, v207\n"
    "v_mfma_f32_16x16x32_bf16 a[48:51], v[152:155], v[16:19], a[48:51]\n"
    "ds_read_b64_tr_b16 v[68:69], v6 offset:8704\n"
    "v_cvt_pk_bf16_f32 v182, v204, v205\n"
    "s_waitcnt lgkmcnt(7)\n"
    "v_mfma_f32_16x16x32_bf16 a[48:51], v[156:159], v[20:23], a[48:51]\n"
    "ds_read_b64_tr_b16 v[70:71], v6 offset:12800\n"
    "v_cvt_pk_bf16_f32 v183, v206, v207\n"
    "v_mfma_f32_16x16x32_bf16 a[72:75], v[104:107], v[24:27], a[72:75]\n"
    "ds_read_b64_tr_b16 v[72:73], v1 offset:768\n"
    "v_exp_f32 v208, v208\n"
    "v_mfma_f32_16x16x32_bf16 a[72:75], v[108:111], v[28:31], a[72:75]\n"
    "ds_read_b64_tr_b16 v[74:75], v1 offset:4864\n"
    "v_exp_f32 v209, v209\n"
    "v_mfma_f32_16x16x32_bf16 a[72:75], v[152:155], v[32:35], a[72:75]\n"
    "ds_read_b64_tr_b16 v[76:77], v1 offset:8960\n"
    "v_exp_f32 v210, v210\n"
    "v_mfma_f32_16x16x32_bf16 a[72:75], v[156:159], v[36:39], a[72:75]\n"
    "ds_read_b64_tr_b16 v[78:79], v1 offset:13056\n"
    "v_exp_f32 v211, v211\n"
    "v_mfma_f32_16x16x32_bf16 a[96:99], v[104:107], v[40:43], a[96:99]\n"
    "ds_read_b64_tr_b16 v[80:81], v5 offset:768\n"
    "v_cvt_pk_bf16_f32 v190, v208, v209\n"
    "v_mfma_f32_16x16x32_bf16 a[96:99], v[108:111], v[44:47], a[96:99]\n"
    "ds_read_b64_tr_b16 v[82:83], v5 offset:4864\n"
    "v_cvt_pk_bf16_f32 v191, v210, v211\n"
    "v_mfma_f32_16x16x32_bf16 a[96:99], v[152:155], v[48:51], a[96:99]\n"
    "ds_read_b64_tr_b16 v[84:85], v5 offset:8960\n"
    "v_exp_f32 v248, v248\n"
    "v_mfma_f32_16x16x32_bf16 a[96:99], v[156:159], v[52:55], a[96:99]\n"
    "ds_read_b64_tr_b16 v[86:87], v5 offset:13056\n"
    "v_exp_f32 v249, v249\n"
    "s_waitcnt lgkmcnt(7)\n"
    "v_mfma_f32_16x16x32_bf16 a[120:123], v[104:107], v[56:59], a[120:123]\n"
    "ds_read_b64_tr_b16 v[88:89], v2 offset:768\n"
    "v_exp_f32 v250, v250\n"
    "v_mfma_f32_16x16x32_bf16 a[120:123], v[108:111], v[60:63], a[120:123]\n"
    "ds_read_b64_tr_b16 v[90:91], v2 offset:4864\n"
    "v_exp_f32 v251, v251\n"
    "v_mfma_f32_16x16x32_bf16 a[120:123], v[152:155], v[64:67], a[120:123]\n"
    "ds_read_b64_tr_b16 v[92:93], v2 offset:8960\n"
    "v_cvt_pk_bf16_f32 v198, v248, v249\n"
    "v_mfma_f32_16x16x32_bf16 a[120:123], v[156:159], v[68:71], a[120:123]\n"
    "ds_read_b64_tr_b16 v[94:95], v2 offset:13056\n"
    "v_cvt_pk_bf16_f32 v199, v250, v251\n"
    "s_waitcnt lgkmcnt(7)\n"
    "v_mfma_f32_16x16x32_bf16 a[144:147], v[104:107], v[72:75], a[144:147]\n"
    "ds_read_b64_tr_b16 v[96:97], v6 offset:768\n"
    "v_mbcnt_lo_u32_b32 v228, -1, 0\n"
    "v_mfma_f32_16x16x32_bf16 a[144:147], v[108:111], v[76:79], a[144:147]\n"
    "ds_read_b64_tr_b16 v[98:99], v6 offset:4864\n"
    "v_mbcnt_hi_u32_b32 v228, -1, v228\n"
    "s_waitcnt lgkmcnt(7)\n"
    "v_mfma_f32_16x16x32_bf16 a[144:147], v[152:155], v[80:83], a[144:147]\n"
    "ds_read_b64_tr_b16 v[100:101], v6 offset:8960\n"
    "v_lshrrev_b32 v229, 4, v228\n"
    "s_waitcnt lgkmcnt(7)\n"
    "v_mfma_f32_16x16x32_bf16 a[144:147], v[156:159], v[84:87], a[144:147]\n"
    "ds_read_b64_tr_b16 v[102:103], v6 offset:13056\n"
    "v_lshlrev_b32 v229, 10, v229\n"
    "v_and_b32 v230, 15, v228\n"
    "v_smfmac_f32_16x16x64_bf16 a[248:251], a[240:243], v[112:119], v253\n"
    "v_lshl_add_u32 v229, v230, 3, v229\n"
    "v_add_u32 v7, s62, v229\n"
    "s_waitcnt lgkmcnt(6)\n"
    "v_mfma_f32_16x16x32_bf16 a[168:171], v[104:107], v[88:91], a[168:171]\n"
    "v_sub_u32 v230, 15, v228\n"
    "v_ashrrev_i32 v230, 31, v230\n"
    "v_not_b32 v230, v230\n"
    "v_smfmac_f32_16x16x64_bf16 a[248:251], a[240:243], v[160:167], v253\n"
    "v_and_b32 v255, 1.0, v230\n"
    "s_waitcnt lgkmcnt(4)\n"
    "v_mfma_f32_16x16x32_bf16 a[168:171], v[108:111], v[92:95], a[168:171]\n"
    "v_accvgpr_read_b32 v232, a244\n"
    "v_accvgpr_read_b32 v233, a245\n"
    "v_add_f32 v233, v232, v233\n"
    "s_waitcnt lgkmcnt(2)\n"
    "v_mfma_f32_16x16x32_bf16 a[168:171], v[152:155], v[96:99], a[168:171]\n"
    "v_rcp_f32 v232, v233\n"
    "v_accvgpr_read_b32 v212, a0\n"
    "v_mfma_f32_16x16x32_bf16 a[4:7], v[112:115], a[224:227], a[4:7]\n"
    "v_max_f32 v233, v232, v233\n"
    "v_cmp_nge_f32 vcc, s80, v233\n"
    "s_waitcnt lgkmcnt(0)\n"
    "v_mfma_f32_16x16x32_bf16 a[168:171], v[156:159], v[100:103], a[168:171]\n"
    "s_or_b64 s[70:71], s[70:71], vcc\n"
    "v_accvgpr_read_b32 v213, a1\n"
    "v_accvgpr_read_b32 v214, a24\n"
    "v_mfma_f32_16x16x4_f32 v[228:231], v232, v255, 0\n"
    "s_add_u32 s88, s82, 0\n"
    "v_accvgpr_read_b32 v215, a25\n"
    "v_accvgpr_read_b32 v216, a48\n"
    "v_accvgpr_read_b32 v217, a49\n"
    "v_accvgpr_read_b32 v218, a72\n"
    "v_accvgpr_read_b32 v219, a73\n"
    "v_accvgpr_read_b32 v220, a2\n"
    "v_accvgpr_read_b32 v221, a3\n"
    "v_mfma_f32_16x16x32_bf16 a[4:7], v[116:119], a[228:231], a[4:7]\n"
    "v_accvgpr_read_b32 v222, a26\n"
    "v_accvgpr_read_b32 v223, a27\n"
    "v_accvgpr_read_b32 v224, a50\n"
    "v_accvgpr_read_b32 v225, a51\n"
    "v_accvgpr_read_b32 v226, a74\n"
    "v_pk_mul_f32 v[212:213], v[212:213], v[228:229]\n"
    "v_pk_mul_f32 v[214:215], v[214:215], v[228:229]\n"
    "v_pk_mul_f32 v[216:217], v[216:217], v[228:229]\n"
    "v_pk_mul_f32 v[218:219], v[218:219], v[228:229]\n"
    "v_pk_mul_f32 v[220:221], v[220:221], v[230:231]\n"
    "v_pk_mul_f32 v[222:223], v[222:223], v[230:231]\n"
    "v_pk_mul_f32 v[224:225], v[224:225], v[230:231]\n"
    "v_cvt_pk_bf16_f32 v212, v212, v214\n"
    "v_mfma_f32_16x16x32_bf16 a[4:7], v[160:163], a[192:195], a[4:7]\n"
    "v_cvt_pk_bf16_f32 v214, v213, v215\n"
    "v_cvt_pk_bf16_f32 v213, v216, v218\n"
    "v_mfma_f32_16x16x32_bf16 a[4:7], v[164:167], a[196:199], a[4:7]\n"
    "ds_write_b64 v7, v[212:213] offset:0\n"
    "v_mfma_f32_16x16x32_bf16 a[28:31], v[112:115], a[232:235], a[28:31]\n"
    "v_cvt_pk_bf16_f32 v215, v217, v219\n"
    "v_accvgpr_read_b32 v227, a75\n"
    "v_mfma_f32_16x16x32_bf16 a[28:31], v[116:119], a[236:239], a[28:31]\n"
    "ds_write_b64 v7, v[214:215] offset:256\n"
    "v_mfma_f32_16x16x32_bf16 a[28:31], v[160:163], a[200:203], a[28:31]\n"
    "v_cvt_pk_bf16_f32 v220, v220, v222\n"
    "v_cvt_pk_bf16_f32 v222, v221, v223\n"
    "v_accvgpr_read_b32 v212, a96\n"
    "v_mfma_f32_16x16x32_bf16 a[28:31], v[164:167], a[204:207], a[28:31]\n"
    "v_accvgpr_read_b32 v213, a97\n"
    "v_accvgpr_read_b32 v214, a120\n"
    "v_accvgpr_read_b32 v215, a121\n"
    "v_accvgpr_read_b32 v216, a144\n"
    "v_accvgpr_read_b32 v217, a145\n"
    "v_pk_mul_f32 v[226:227], v[226:227], v[230:231]\n"
    "v_pk_mul_f32 v[212:213], v[212:213], v[228:229]\n"
    "v_pk_mul_f32 v[214:215], v[214:215], v[228:229]\n"
    "v_pk_mul_f32 v[216:217], v[216:217], v[228:229]\n"
    "v_cvt_pk_bf16_f32 v221, v224, v226\n"
    "v_mfma_f32_16x16x32_bf16 a[52:55], v[112:115], v[8:11], a[52:55]\n"
    "ds_write_b64 v7, v[220:221] offset:512\n"
    "v_mfma_f32_16x16x32_bf16 a[52:55], v[116:119], v[12:15], a[52:55]\n"
    "v_cvt_pk_bf16_f32 v223, v225, v227\n"
    "v_accvgpr_read_b32 v218, a168\n"
    "v_mfma_f32_16x16x32_bf16 a[52:55], v[160:163], v[16:19], a[52:55]\n"
    "ds_write_b64 v7, v[222:223] offset:768\n"
    "v_mfma_f32_16x16x32_bf16 a[52:55], v[164:167], v[20:23], a[52:55]\n"
    "v_accvgpr_read_b32 v219, a169\n"
    "v_cvt_pk_bf16_f32 v212, v212, v214\n"
    "v_cvt_pk_bf16_f32 v214, v213, v215\n"
    "v_mfma_f32_16x16x32_bf16 a[76:79], v[112:115], v[24:27], a[76:79]\n"
    "v_accvgpr_read_b32 v220, a98\n"
    "v_accvgpr_read_b32 v221, a99\n"
    "v_accvgpr_read_b32 v222, a122\n"
    "v_mfma_f32_16x16x32_bf16 a[76:79], v[116:119], v[28:31], a[76:79]\n"
    "v_accvgpr_read_b32 v223, a123\n"
    "v_accvgpr_read_b32 v224, a146\n"
    "v_accvgpr_read_b32 v225, a147\n"
    "v_accvgpr_read_b32 v226, a170\n"
    "v_accvgpr_read_b32 v227, a171\n"
    "v_pk_mul_f32 v[218:219], v[218:219], v[228:229]\n"
    "v_pk_mul_f32 v[220:221], v[220:221], v[230:231]\n"
    "v_pk_mul_f32 v[222:223], v[222:223], v[230:231]\n"
    "v_pk_mul_f32 v[224:225], v[224:225], v[230:231]\n"
    "v_pk_mul_f32 v[226:227], v[226:227], v[230:231]\n"
    "v_cvt_pk_bf16_f32 v213, v216, v218\n"
    "v_mfma_f32_16x16x32_bf16 a[76:79], v[160:163], v[32:35], a[76:79]\n"
    "ds_write_b64 v7, v[212:213] offset:128\n"
    "v_mfma_f32_16x16x32_bf16 a[76:79], v[164:167], v[36:39], a[76:79]\n"
    "v_cvt_pk_bf16_f32 v215, v217, v219\n"
    "v_cvt_pk_bf16_f32 v220, v220, v222\n"
    "v_mfma_f32_16x16x32_bf16 a[100:103], v[112:115], v[40:43], a[100:103]\n"
    "ds_write_b64 v7, v[214:215] offset:384\n"
    "v_mfma_f32_16x16x32_bf16 a[100:103], v[116:119], v[44:47], a[100:103]\n"
    "v_cvt_pk_bf16_f32 v222, v221, v223\n"
    "v_cvt_pk_bf16_f32 v221, v224, v226\n"
    "v_mfma_f32_16x16x32_bf16 a[100:103], v[160:163], v[48:51], a[100:103]\n"
    "ds_write_b64 v7, v[220:221] offset:640\n"
    "v_mfma_f32_16x16x32_bf16 a[100:103], v[164:167], v[52:55], a[100:103]\n"
    "v_cvt_pk_bf16_f32 v223, v225, v227\n"
    "ds_write_b64 v7, v[222:223] offset:896\n"
    "v_mfma_f32_16x16x32_bf16 a[124:127], v[112:115], v[56:59], a[124:127]\n"
    "ds_read_b64_tr_b16 v[104:105], v254 offset:0\n"
    "ds_read_b64_tr_b16 v[106:107], v254 offset:32\n"
    "v_mfma_f32_16x16x32_bf16 a[124:127], v[116:119], v[60:63], a[124:127]\n"
    "ds_read_b64_tr_b16 v[108:109], v254 offset:1024\n"
    "ds_read_b64_tr_b16 v[110:111], v254 offset:1056\n"
    "v_mfma_f32_16x16x32_bf16 a[124:127], v[160:163], v[64:67], a[124:127]\n"
    "ds_read_b64_tr_b16 v[156:157], v254 offset:2048\n"
    "ds_read_b64_tr_b16 v[158:159], v254 offset:2080\n"
    "v_mfma_f32_16x16x32_bf16 a[124:127], v[164:167], v[68:71], a[124:127]\n"
    "ds_read_b64_tr_b16 v[200:201], v254 offset:3072\n"
    "ds_read_b64_tr_b16 v[202:203], v254 offset:3104\n"
    "v_mfma_f32_16x16x32_bf16 a[148:151], v[112:115], v[72:75], a[148:151]\n"
    "v_accvgpr_read_b32 v234, a248\n"
    "v_accvgpr_read_b32 v235, a249\n"
    "v_mfma_f32_16x16x32_bf16 a[148:151], v[116:119], v[76:79], a[148:151]\n"
    "v_add_f32 v235, v234, v235\n"
    "v_rcp_f32 v234, v235\n"
    "v_mfma_f32_16x16x32_bf16 a[148:151], v[160:163], v[80:83], a[148:151]\n"
    "v_max_f32 v235, v234, v235\n"
    "v_cmp_nge_f32 vcc, s80, v235\n"
    "v_mfma_f32_16x16x32_bf16 a[148:151], v[164:167], v[84:87], a[148:151]\n"
    "v_accvgpr_read_b32 v212, a4\n"
    "v_accvgpr_read_b32 v213, a5\n"
    "v_mfma_f32_16x16x32_bf16 a[172:175], v[112:115], v[88:91], a[172:175]\n"
    "s_waitcnt lgkmcnt(6)\n"
    "buffer_store_dwordx4 v[104:107], v252, s[52:55], s88 offen offset:0 sc1\n"
    "v_mfma_f32_16x16x32_bf16 a[172:175], v[116:119], v[92:95], a[172:175]\n"
    "v_accvgpr_read_b32 v214, a28\n"
    "v_accvgpr_read_b32 v215, a29\n"
    "v_mfma_f32_16x16x32_bf16 a[172:175], v[160:163], v[96:99], a[172:175]\n"
    "v_accvgpr_read_b32 v216, a52\n"
    "v_accvgpr_read_b32 v217, a53\n"
    "v_mfma_f32_16x16x32_bf16 a[172:175], v[164:167], v[100:103], a[172:175]\n"
    "v_accvgpr_read_b32 v218, a76\n"
    "v_accvgpr_read_b32 v219, a77\n"
    "v_mfma_f32_16x16x4_f32 v[248:251], v234, v255, 0\n"
    "v_accvgpr_read_b32 v220, a6\n"
    "v_accvgpr_read_b32 v221, a7\n"
    "s_waitcnt lgkmcnt(0)\n"
    "buffer_store_dwordx4 v[108:111], v252, s[52:55], s88 offen offset:1024 sc1\n"
    "v_accvgpr_read_b32 v222, a30\n"
    "v_accvgpr_read_b32 v223, a31\n"
    "v_smfmac_f32_16x16x64_bf16 a[252:255], a[240:243], v[120:127], v253\n"
    "v_accvgpr_read_b32 v224, a54\n"
    "v_accvgpr_read_b32 v225, a55\n"
    "v_smfmac_f32_16x16x64_bf16 a[252:255], a[240:243], v[168:175], v253\n"
    "v_accvgpr_read_b32 v226, a78\n"
    "v_accvgpr_read_b32 v227, a79\n"
    "v_pk_mul_f32 v[212:213], v[212:213], v[248:249]\n"
    "v_pk_mul_f32 v[214:215], v[214:215], v[248:249]\n"
    "v_pk_mul_f32 v[216:217], v[216:217], v[248:249]\n"
    "v_pk_mul_f32 v[218:219], v[218:219], v[248:249]\n"
    "v_pk_mul_f32 v[220:221], v[220:221], v[250:251]\n"
    "v_pk_mul_f32 v[222:223], v[222:223], v[250:251]\n"
    "v_pk_mul_f32 v[224:225], v[224:225], v[250:251]\n"
    "v_pk_mul_f32 v[226:227], v[226:227], v[250:251]\n"
    "buffer_store_dwordx4 v[156:159], v252, s[52:55], s88 offen offset:2048 sc1\n"
    "v_mfma_f32_16x16x32_bf16 a[8:11], v[120:123], a[224:227], a[8:11]\n"
    "v_cvt_pk_bf16_f32 v212, v212, v214\n"
    "v_cvt_pk_bf16_f32 v214, v213, v215\n"
    "v_cvt_pk_bf16_f32 v213, v216, v218\n"
    "v_mfma_f32_16x16x32_bf16 a[8:11], v[124:127], a[228:231], a[8:11]\n"
    "ds_write_b64 v7, v[212:213] offset:16384\n"
    "v_mfma_f32_16x16x32_bf16 a[8:11], v[168:171], a[192:195], a[8:11]\n"
    "v_cvt_pk_bf16_f32 v215, v217, v219\n"
    "v_cvt_pk_bf16_f32 v220, v220, v222\n"
    "v_mfma_f32_16x16x32_bf16 a[8:11], v[172:175], a[196:199], a[8:11]\n"
    "ds_write_b64 v7, v[214:215] offset:16640\n"
    "v_mfma_f32_16x16x32_bf16 a[32:35], v[120:123], a[232:235], a[32:35]\n"
    "buffer_store_dwordx4 v[200:203], v252, s[52:55], s88 offen offset:3072 sc1\n"
    "s_or_b64 s[70:71], s[70:71], vcc\n"
    "v_mfma_f32_16x16x32_bf16 a[32:35], v[124:127], a[236:239], a[32:35]\n"
    "s_add_u32 s88, s82, 4096\n"
    "v_cvt_pk_bf16_f32 v222, v221, v223\n"
    "v_cvt_pk_bf16_f32 v221, v224, v226\n"
    "v_mfma_f32_16x16x32_bf16 a[32:35], v[168:171], a[200:203], a[32:35]\n"
    "ds_write_b64 v7, v[220:221] offset:16896\n"
    "v_mfma_f32_16x16x32_bf16 a[32:35], v[172:175], a[204:207], a[32:35]\n"
    "v_cvt_pk_bf16_f32 v223, v225, v227\n"
    "v_accvgpr_read_b32 v212, a100\n"
    "v_mfma_f32_16x16x32_bf16 a[56:59], v[120:123], v[8:11], a[56:59]\n"
    "ds_write_b64 v7, v[222:223] offset:17152\n"
    "v_mfma_f32_16x16x32_bf16 a[56:59], v[124:127], v[12:15], a[56:59]\n"
    "v_accvgpr_read_b32 v213, a101\n"
    "v_accvgpr_read_b32 v214, a124\n"
    "v_accvgpr_read_b32 v215, a125\n"
    "v_mfma_f32_16x16x32_bf16 a[56:59], v[168:171], v[16:19], a[56:59]\n"
    "v_accvgpr_read_b32 v216, a148\n"
    "v_accvgpr_read_b32 v217, a149\n"
    "v_accvgpr_read_b32 v218, a172\n"
    "v_accvgpr_read_b32 v219, a173\n"
    "v_accvgpr_read_b32 v220, a102\n"
    "v_pk_mul_f32 v[212:213], v[212:213], v[248:249]\n"
    "v_pk_mul_f32 v[214:215], v[214:215], v[248:249]\n"
    "v_pk_mul_f32 v[216:217], v[216:217], v[248:249]\n"
    "v_pk_mul_f32 v[218:219], v[218:219], v[248:249]\n"
    "v_cvt_pk_bf16_f32 v212, v212, v214\n"
    "v_mfma_f32_16x16x32_bf16 a[56:59], v[172:175], v[20:23], a[56:59]\n"
    "v_cvt_pk_bf16_f32 v214, v213, v215\n"
    "v_cvt_pk_bf16_f32 v213, v216, v218\n"
    "v_mfma_f32_16x16x32_bf16 a[80:83], v[120:123], v[24:27], a[80:83]\n"
    "ds_write_b64 v7, v[212:213] offset:16512\n"
    "v_mfma_f32_16x16x32_bf16 a[80:83], v[124:127], v[28:31], a[80:83]\n"
    "v_cvt_pk_bf16_f32 v215, v217, v219\n"
    "v_accvgpr_read_b32 v221, a103\n"
    "v_mfma_f32_16x16x32_bf16 a[80:83], v[168:171], v[32:35], a[80:83]\n"
    "ds_write_b64 v7, v[214:215] offset:16768\n"
    "v_mfma_f32_16x16x32_bf16 a[80:83], v[172:175], v[36:39], a[80:83]\n"
    "v_accvgpr_read_b32 v222, a126\n"
    "v_accvgpr_read_b32 v223, a127\n"
    "v_accvgpr_read_b32 v224, a150\n"
    "v_mfma_f32_16x16x32_bf16 a[104:107], v[120:123], v[40:43], a[104:107]\n"
    "v_accvgpr_read_b32 v225, a151\n"
    "v_accvgpr_read_b32 v226, a174\n"
    "v_accvgpr_read_b32 v227, a175\n"
    "v_accvgpr_read_b32 v232, a252\n"
    "v_accvgpr_read_b32 v233, a253\n"
    "v_pk_mul_f32 v[220:221], v[220:221], v[250:251]\n"
    "v_pk_mul_f32 v[222:223], v[222:223], v[250:251]\n"
    "v_pk_mul_f32 v[224:225], v[224:225], v[250:251]\n"
    "v_pk_mul_f32 v[226:227], v[226:227], v[250:251]\n"
    "v_cvt_pk_bf16_f32 v220, v220, v222\n"
    "v_mfma_f32_16x16x32_bf16 a[104:107], v[124:127], v[44:47], a[104:107]\n"
    "v_cvt_pk_bf16_f32 v222, v221, v223\n"
    "v_cvt_pk_bf16_f32 v221, v224, v226\n"
    "v_mfma_f32_16x16x32_bf16 a[104:107], v[168:171], v[48:51], a[104:107]\n"
    "ds_write_b64 v7, v[220:221] offset:17024\n"
    "v_mfma_f32_16x16x32_bf16 a[104:107], v[172:175], v[52:55], a[104:107]\n"
    "v_cvt_pk_bf16_f32 v223, v225, v227\n"
    "ds_write_b64 v7, v[222:223] offset:17280\n"
    "v_mfma_f32_16x16x32_bf16 a[128:131], v[120:123], v[56:59], a[128:131]\n"
    "ds_read_b64_tr_b16 v[112:113], v254 offset:16384\n"
    "ds_read_b64_tr_b16 v[114:115], v254 offset:16416\n"
    "v_mfma_f32_16x16x32_bf16 a[128:131], v[124:127], v[60:63], a[128:131]\n"
    "ds_read_b64_tr_b16 v[116:117], v254 offset:17408\n"
    "ds_read_b64_tr_b16 v[118:119], v254 offset:17440\n"
    "v_mfma_f32_16x16x32_bf16 a[128:131], v[168:171], v[64:67], a[128:131]\n"
    "ds_read_b64_tr_b16 v[164:165], v254 offset:18432\n"
    "ds_read_b64_tr_b16 v[166:167], v254 offset:18464\n"
    "v_mfma_f32_16x16x32_bf16 a[128:131], v[172:175], v[68:71], a[128:131]\n"
    "ds_read_b64_tr_b16 v[204:205], v254 offset:19456\n"
    "ds_read_b64_tr_b16 v[206:207], v254 offset:19488\n"
    "v_mfma_f32_16x16x32_bf16 a[152:155], v[120:123], v[72:75], a[152:155]\n"
    "v_add_f32 v233, v232, v233\n"
    "v_rcp_f32 v232, v233\n"
    "v_mfma_f32_16x16x32_bf16 a[152:155], v[124:127], v[76:79], a[152:155]\n"
    "v_max_f32 v233, v232, v233\n"
    "v_cmp_nge_f32 vcc, s80, v233\n"
    "v_mfma_f32_16x16x32_bf16 a[152:155], v[168:171], v[80:83], a[152:155]\n"
    "v_accvgpr_read_b32 v212, a8\n"
    "v_accvgpr_read_b32 v213, a9\n"
    "v_mfma_f32_16x16x32_bf16 a[152:155], v[172:175], v[84:87], a[152:155]\n"
    "v_accvgpr_read_b32 v214, a32\n"
    "v_accvgpr_read_b32 v215, a33\n"
    "v_mfma_f32_16x16x32_bf16 a[176:179], v[120:123], v[88:91], a[176:179]\n"
    "s_waitcnt lgkmcnt(6)\n"
    "buffer_store_dwordx4 v[112:115], v252, s[52:55], s88 offen offset:0 sc1\n"
    "v_mfma_f32_16x16x32_bf16 a[176:179], v[124:127], v[92:95], a[176:179]\n"
    "v_accvgpr_read_b32 v216, a56\n"
    "v_accvgpr_read_b32 v217, a57\n"
    "v_mfma_f32_16x16x32_bf16 a[176:179], v[168:171], v[96:99], a[176:179]\n"
    "v_accvgpr_read_b32 v218, a80\n"
    "v_accvgpr_read_b32 v219, a81\n"
    "v_mfma_f32_16x16x32_bf16 a[176:179], v[172:175], v[100:103], a[176:179]\n"
    "v_accvgpr_read_b32 v220, a10\n"
    "v_accvgpr_read_b32 v221, a11\n"
    "v_mfma_f32_16x16x4_f32 v[228:231], v232, v255, 0\n"
    "v_accvgpr_read_b32 v222, a34\n"
    "v_accvgpr_read_b32 v223, a35\n"
    "s_waitcnt lgkmcnt(0)\n"
    "buffer_store_dwordx4 v[116:119], v252, s[52:55], s88 offen offset:1024 sc1\n"
    "v_accvgpr_read_b32 v224, a58\n"
    "v_accvgpr_read_b32 v225, a59\n"
    "v_accvgpr_read_b32 v226, a82\n"
    "v_smfmac_f32_16x16x64_bf16 v[236:239], a[240:243], v[128:135], v253\n"
    "v_accvgpr_read_b32 v227, a83\n"
    "v_smfmac_f32_16x16x64_bf16 v[236:239], a[240:243], v[176:183], v253\n"
    "v_pk_mul_f32 v[212:213], v[212:213], v[228:229]\n"
    "v_pk_mul_f32 v[214:215], v[214:215], v[228:229]\n"
    "v_pk_mul_f32 v[216:217], v[216:217], v[228:229]\n"
    "v_pk_mul_f32 v[218:219], v[218:219], v[228:229]\n"
    "v_pk_mul_f32 v[220:221], v[220:221], v[230:231]\n"
    "v_pk_mul_f32 v[222:223], v[222:223], v[230:231]\n"
    "v_pk_mul_f32 v[224:225], v[224:225], v[230:231]\n"
    "v_pk_mul_f32 v[226:227], v[226:227], v[230:231]\n"
    "buffer_store_dwordx4 v[164:167], v252, s[52:55], s88 offen offset:2048 sc1\n"
    "v_mfma_f32_16x16x32_bf16 a[12:15], v[128:131], a[224:227], a[12:15]\n"
    "v_cvt_pk_bf16_f32 v212, v212, v214\n"
    "v_cvt_pk_bf16_f32 v214, v213, v215\n"
    "v_cvt_pk_bf16_f32 v213, v216, v218\n"
    "v_mfma_f32_16x16x32_bf16 a[12:15], v[132:135], a[228:231], a[12:15]\n"
    "ds_write_b64 v7, v[212:213] offset:32768\n"
    "v_cvt_pk_bf16_f32 v215, v217, v219\n"
    "v_mfma_f32_16x16x32_bf16 a[12:15], v[176:179], a[192:195], a[12:15]\n"
    "ds_write_b64 v7, v[214:215] offset:33024\n"
    "v_mfma_f32_16x16x32_bf16 a[12:15], v[180:183], a[196:199], a[12:15]\n"
    "buffer_store_dwordx4 v[204:207], v252, s[52:55], s88 offen offset:3072 sc1\n"
    "s_or_b64 s[70:71], s[70:71], vcc\n"
    "v_mfma_f32_16x16x32_bf16 a[36:39], v[128:131], a[232:235], a[36:39]\n"
    "s_add_u32 s88, s82, 8192\n"
    "v_cvt_pk_bf16_f32 v220, v220, v222\n"
    "v_cvt_pk_bf16_f32 v222, v221, v223\n"
    "v_cvt_pk_bf16_f32 v221, v224, v226\n"
    "v_mfma_f32_16x16x32_bf16 a[36:39], v[132:135], a[236:239], a[36:39]\n"
    "ds_write_b64 v7, v[220:221] offset:33280\n"
    "v_cvt_pk_bf16_f32 v223, v225, v227\n"
    "v_mfma_f32_16x16x32_bf16 a[36:39], v[176:179], a[200:203], a[36:39]\n"
    "ds_write_b64 v7, v[222:223] offset:33536\n"
    "v_accvgpr_read_b32 v212, a104\n"
    "v_mfma_f32_16x16x32_bf16 a[36:39], v[180:183], a[204:207], a[36:39]\n"
    "v_accvgpr_read_b32 v213, a105\n"
    "v_accvgpr_read_b32 v214, a128\n"
    "v_accvgpr_read_b32 v215, a129\n"
    "v_accvgpr_read_b32 v216, a152\n"
    "v_mfma_f32_16x16x32_bf16 a[60:63], v[128:131], v[8:11], a[60:63]\n"
    "v_accvgpr_read_b32 v217, a153\n"
    "v_accvgpr_read_b32 v218, a176\n"
    "v_accvgpr_read_b32 v219, a177\n"
    "v_accvgpr_read_b32 v220, a106\n"
    "v_accvgpr_read_b32 v221, a107\n"
    "v_pk_mul_f32 v[212:213], v[212:213], v[228:229]\n"
    "v_pk_mul_f32 v[214:215], v[214:215], v[228:229]\n"
    "v_pk_mul_f32 v[216:217], v[216:217], v[228:229]\n"
    "v_pk_mul_f32 v[218:219], v[218:219], v[228:229]\n"
    "v_pk_mul_f32 v[220:221], v[220:221], v[230:231]\n"
    "v_cvt_pk_bf16_f32 v212, v212, v214\n"
    "v_mfma_f32_16x16x32_bf16 a[60:63], v[132:135], v[12:15], a[60:63]\n"
    "v_cvt_pk_bf16_f32 v214, v213, v215\n"
    "v_cvt_pk_bf16_f32 v213, v216, v218\n"
    "v_mfma_f32_16x16x32_bf16 a[60:63], v[176:179], v[16:19], a[60:63]\n"
    "ds_write_b64 v7, v[212:213] offset:32896\n"
    "v_cvt_pk_bf16_f32 v215, v217, v219\n"
    "v_mfma_f32_16x16x32_bf16 a[60:63], v[180:183], v[20:23], a[60:63]\n"
    "ds_write_b64 v7, v[214:215] offset:33152\n"
    "v_mfma_f32_16x16x32_bf16 a[84:87], v[128:131], v[24:27], a[84:87]\n"
    "v_accvgpr_read_b32 v222, a130\n"
    "v_accvgpr_read_b32 v223, a131\n"
    "v_accvgpr_read_b32 v224, a154\n"
    "v_accvgpr_read_b32 v225, a155\n"
    "v_mfma_f32_16x16x32_bf16 a[84:87], v[132:135], v[28:31], a[84:87]\n"
    "v_accvgpr_read_b32 v226, a178\n"
    "v_accvgpr_read_b32 v227, a179\n"
    "v_add_f32 v235, v236, v237\n"
    "v_rcp_f32 v234, v235\n"
    "v_pk_mul_f32 v[222:223], v[222:223], v[230:231]\n"
    "v_pk_mul_f32 v[224:225], v[224:225], v[230:231]\n"
    "v_pk_mul_f32 v[226:227], v[226:227], v[230:231]\n"
    "v_cvt_pk_bf16_f32 v220, v220, v222\n"
    "v_mfma_f32_16x16x32_bf16 a[84:87], v[176:179], v[32:35], a[84:87]\n"
    "v_cvt_pk_bf16_f32 v222, v221, v223\n"
    "v_cvt_pk_bf16_f32 v221, v224, v226\n"
    "v_mfma_f32_16x16x32_bf16 a[84:87], v[180:183], v[36:39], a[84:87]\n"
    "ds_write_b64 v7, v[220:221] offset:33408\n"
    "v_mfma_f32_16x16x32_bf16 a[108:111], v[128:131], v[40:43], a[108:111]\n"
    "v_cvt_pk_bf16_f32 v223, v225, v227\n"
    "ds_write_b64 v7, v[222:223] offset:33664\n"
    "v_mfma_f32_16x16x32_bf16 a[108:111], v[132:135], v[44:47], a[108:111]\n"
    "ds_read_b64_tr_b16 v[120:121], v254 offset:32768\n"
    "ds_read_b64_tr_b16 v[122:123], v254 offset:32800\n"
    "v_mfma_f32_16x16x32_bf16 a[108:111], v[176:179], v[48:51], a[108:111]\n"
    "ds_read_b64_tr_b16 v[124:125], v254 offset:33792\n"
    "ds_read_b64_tr_b16 v[126:127], v254 offset:33824\n"
    "v_mfma_f32_16x16x32_bf16 a[108:111], v[180:183], v[52:55], a[108:111]\n"
    "ds_read_b64_tr_b16 v[172:173], v254 offset:34816\n"
    "ds_read_b64_tr_b16 v[174:175], v254 offset:34848\n"
    "v_mfma_f32_16x16x32_bf16 a[132:135], v[128:131], v[56:59], a[132:135]\n"
    "ds_read_b64_tr_b16 v[208:209], v254 offset:35840\n"
    "ds_read_b64_tr_b16 v[210:211], v254 offset:35872\n"
    "v_mfma_f32_16x16x32_bf16 a[132:135], v[132:135], v[60:63], a[132:135]\n"
    "v_max_f32 v235, v234, v235\n"
    "v_cmp_nge_f32 vcc, s80, v235\n"
    "v_mfma_f32_16x16x32_bf16 a[132:135], v[176:179], v[64:67], a[132:135]\n"
    "v_accvgpr_read_b32 v212, a12\n"
    "v_accvgpr_read_b32 v213, a13\n"
    "v_mfma_f32_16x16x32_bf16 a[132:135], v[180:183], v[68:71], a[132:135]\n"
    "v_accvgpr_read_b32 v214, a36\n"
    "v_accvgpr_read_b32 v215, a37\n"
    "v_mfma_f32_16x16x32_bf16 a[156:159], v[128:131], v[72:75], a[156:159]\n"
    "v_accvgpr_read_b32 v216, a60\n"
    "v_accvgpr_read_b32 v217, a61\n"
    "v_mfma_f32_16x16x32_bf16 a[156:159], v[132:135], v[76:79], a[156:159]\n"
    "s_waitcnt lgkmcnt(6)\n"
    "buffer_store_dwordx4 v[120:123], v252, s[52:55], s88 offen offset:0 sc1\n"
    "v_mfma_f32_16x16x32_bf16 a[156:159], v[176:179], v[80:83], a[156:159]\n"
    "v_accvgpr_read_b32 v218, a84\n"
    "v_accvgpr_read_b32 v219, a85\n"
    "v_mfma_f32_16x16x32_bf16 a[156:159], v[180:183], v[84:87], a[156:159]\n"
    "v_accvgpr_read_b32 v220, a14\n"
    "v_accvgpr_read_b32 v221, a15\n"
    "v_mfma_f32_16x16x32_bf16 a[180:183], v[128:131], v[88:91], a[180:183]\n"
    "v_accvgpr_read_b32 v222, a38\n"
    "v_accvgpr_read_b32 v223, a39\n"
    "v_mfma_f32_16x16x32_bf16 a[180:183], v[132:135], v[92:95], a[180:183]\n"
    "v_accvgpr_read_b32 v224, a62\n"
    "v_accvgpr_read_b32 v225, a63\n"
    "v_mfma_f32_16x16x32_bf16 a[180:183], v[176:179], v[96:99], a[180:183]\n"
    "s_waitcnt lgkmcnt(0)\n"
    "buffer_store_dwordx4 v[124:127], v252, s[52:55], s88 offen offset:1024 sc1\n"
    "v_mfma_f32_16x16x32_bf16 a[180:183], v[180:183], v[100:103], a[180:183]\n"
    "v_accvgpr_read_b32 v226, a86\n"
    "v_accvgpr_read_b32 v227, a87\n"
    "v_mfma_f32_16x16x4_f32 v[248:251], v234, v255, 0\n"
    "v_smfmac_f32_16x16x64_bf16 v[240:243], a[240:243], v[136:143], v253\n"
    "v_smfmac_f32_16x16x64_bf16 v[240:243], a[240:243], v[184:191], v253\n"
    "buffer_store_dwordx4 v[172:175], v252, s[52:55], s88 offen offset:2048 sc1\n"
    "v_mfma_f32_16x16x32_bf16 a[16:19], v[136:139], a[224:227], a[16:19]\n"
    "v_mfma_f32_16x16x32_bf16 a[16:19], v[140:143], a[228:231], a[16:19]\n"
    "v_mfma_f32_16x16x32_bf16 a[16:19], v[184:187], a[192:195], a[16:19]\n"
    "v_mfma_f32_16x16x32_bf16 a[16:19], v[188:191], a[196:199], a[16:19]\n"
    "buffer_store_dwordx4 v[208:211], v252, s[52:55], s88 offen offset:3072 sc1\n"
    "v_mfma_f32_16x16x32_bf16 a[40:43], v[136:139], a[232:235], a[40:43]\n"
    "s_or_b64 s[70:71], s[70:71], vcc\n"
    "s_add_u32 s88, s82, 12288\n"
    "v_add_f32 v233, v240, v241\n"
    "v_rcp_f32 v232, v233\n"
    "v_pk_mul_f32 v[212:213], v[212:213], v[248:249]\n"
    "v_pk_mul_f32 v[214:215], v[214:215], v[248:249]\n"
    "v_pk_mul_f32 v[216:217], v[216:217], v[248:249]\n"
    "v_pk_mul_f32 v[218:219], v[218:219], v[248:249]\n"
    "v_pk_mul_f32 v[220:221], v[220:221], v[250:251]\n"
    "v_pk_mul_f32 v[222:223], v[222:223], v[250:251]\n"
    "v_pk_mul_f32 v[224:225], v[224:225], v[250:251]\n"
    "v_pk_mul_f32 v[226:227], v[226:227], v[250:251]\n"
    "v_cvt_pk_bf16_f32 v212, v212, v214\n"
    "v_mfma_f32_16x16x32_bf16 a[40:43], v[140:143], a[236:239], a[40:43]\n"
    "v_cvt_pk_bf16_f32 v214, v213, v215\n"
    "v_cvt_pk_bf16_f32 v213, v216, v218\n"
    "v_mfma_f32_16x16x32_bf16 a[40:43], v[184:187], a[200:203], a[40:43]\n"
    "ds_write_b64 v7, v[212:213] offset:49152\n"
    "v_cvt_pk_bf16_f32 v215, v217, v219\n"
    "v_mfma_f32_16x16x32_bf16 a[40:43], v[188:191], a[204:207], a[40:43]\n"
    "ds_write_b64 v7, v[214:215] offset:49408\n"
    "v_cvt_pk_bf16_f32 v220, v220, v222\n"
    "v_mfma_f32_16x16x32_bf16 a[64:67], v[136:139], v[8:11], a[64:67]\n"
    "v_cvt_pk_bf16_f32 v222, v221, v223\n"
    "v_cvt_pk_bf16_f32 v221, v224, v226\n"
    "v_mfma_f32_16x16x32_bf16 a[64:67], v[140:143], v[12:15], a[64:67]\n"
    "ds_write_b64 v7, v[220:221] offset:49664\n"
    "v_cvt_pk_bf16_f32 v223, v225, v227\n"
    "v_mfma_f32_16x16x32_bf16 a[64:67], v[184:187], v[16:19], a[64:67]\n"
    "ds_write_b64 v7, v[222:223] offset:49920\n"
    "v_accvgpr_read_b32 v212, a108\n"
    "v_accvgpr_read_b32 v213, a109\n"
    "v_pk_mul_f32 v[212:213], v[212:213], v[248:249]\n"
    "v_accvgpr_read_b32 v214, a132\n"
    "v_mfma_f32_16x16x32_bf16 a[64:67], v[188:191], v[20:23], a[64:67]\n"
    "v_accvgpr_read_b32 v215, a133\n"
    "v_accvgpr_read_b32 v216, a156\n"
    "v_accvgpr_read_b32 v217, a157\n"
    "v_accvgpr_read_b32 v218, a180\n"
    "v_accvgpr_read_b32 v219, a181\n"
    "v_pk_mul_f32 v[214:215], v[214:215], v[248:249]\n"
    "v_pk_mul_f32 v[216:217], v[216:217], v[248:249]\n"
    "v_pk_mul_f32 v[218:219], v[218:219], v[248:249]\n"
    "v_cvt_pk_bf16_f32 v212, v212, v214\n"
    "v_mfma_f32_16x16x32_bf16 a[88:91], v[136:139], v[24:27], a[88:91]\n"
    "v_cvt_pk_bf16_f32 v214, v213, v215\n"
    "v_cvt_pk_bf16_f32 v213, v216, v218\n"
    "v_mfma_f32_16x16x32_bf16 a[88:91], v[140:143], v[28:31], a[88:91]\n"
    "ds_write_b64 v7, v[212:213] offset:49280\n"
    "v_cvt_pk_bf16_f32 v215, v217, v219\n"
    "v_mfma_f32_16x16x32_bf16 a[88:91], v[184:187], v[32:35], a[88:91]\n"
    "ds_write_b64 v7, v[214:215] offset:49536\n"
    "v_accvgpr_read_b32 v220, a110\n"
    "v_accvgpr_read_b32 v221, a111\n"
    "v_pk_mul_f32 v[220:221], v[220:221], v[250:251]\n"
    "v_accvgpr_read_b32 v222, a134\n"
    "v_mfma_f32_16x16x32_bf16 a[88:91], v[188:191], v[36:39], a[88:91]\n"
    "v_accvgpr_read_b32 v223, a135\n"
    "v_accvgpr_read_b32 v224, a158\n"
    "v_accvgpr_read_b32 v225, a159\n"
    "v_accvgpr_read_b32 v226, a182\n"
    "v_mfma_f32_16x16x32_bf16 a[112:115], v[136:139], v[40:43], a[112:115]\n"
    "v_accvgpr_read_b32 v227, a183\n"
    "v_max_f32 v233, v232, v233\n"
    "v_cmp_nge_f32 vcc, s80, v233\n"
    "v_accvgpr_read_b32 v212, a16\n"
    "v_accvgpr_read_b32 v213, a17\n"
    "v_pk_mul_f32 v[222:223], v[222:223], v[250:251]\n"
    "v_pk_mul_f32 v[224:225], v[224:225], v[250:251]\n"
    "v_pk_mul_f32 v[226:227], v[226:227], v[250:251]\n"
    "v_cvt_pk_bf16_f32 v220, v220, v222\n"
    "v_mfma_f32_16x16x32_bf16 a[112:115], v[140:143], v[44:47], a[112:115]\n"
    "v_cvt_pk_bf16_f32 v222, v221, v223\n"
    "v_cvt_pk_bf16_f32 v221, v224, v226\n"
    "v_mfma_f32_16x16x32_bf16 a[112:115], v[184:187], v[48:51], a[112:115]\n"
    "ds_write_b64 v7, v[220:221] offset:49792\n"
    "v_cvt_pk_bf16_f32 v223, v225, v227\n"
    "v_mfma_f32_16x16x32_bf16 a[112:115], v[188:191], v[52:55], a[112:115]\n"
    "ds_write_b64 v7, v[222:223] offset:50048\n"
    "v_mfma_f32_16x16x32_bf16 a[136:139], v[136:139], v[56:59], a[136:139]\n"
    "ds_read_b64_tr_b16 v[128:129], v254 offset:49152\n"
    "ds_read_b64_tr_b16 v[130:131], v254 offset:49184\n"
    "v_mfma_f32_16x16x32_bf16 a[136:139], v[140:143], v[60:63], a[136:139]\n"
    "ds_read_b64_tr_b16 v[132:133], v254 offset:50176\n"
    "ds_read_b64_tr_b16 v[134:135], v254 offset:50208\n"
    "v_mfma_f32_16x16x32_bf16 a[136:139], v[184:187], v[64:67], a[136:139]\n"
    "ds_read_b64_tr_b16 v[180:181], v254 offset:51200\n"
    "ds_read_b64_tr_b16 v[182:183], v254 offset:51232\n"
    "v_mfma_f32_16x16x32_bf16 a[136:139], v[188:191], v[68:71], a[136:139]\n"
    "ds_read_b64_tr_b16 v[200:201], v254 offset:52224\n"
    "ds_read_b64_tr_b16 v[202:203], v254 offset:52256\n"
    "v_mfma_f32_16x16x32_bf16 a[160:163], v[136:139], v[72:75], a[160:163]\n"
    "v_accvgpr_read_b32 v214, a40\n"
    "v_accvgpr_read_b32 v215, a41\n"
    "v_mfma_f32_16x16x32_bf16 a[160:163], v[140:143], v[76:79], a[160:163]\n"
    "v_accvgpr_read_b32 v216, a64\n"
    "v_accvgpr_read_b32 v217, a65\n"
    "v_mfma_f32_16x16x32_bf16 a[160:163], v[184:187], v[80:83], a[160:163]\n"
    "v_accvgpr_read_b32 v218, a88\n"
    "v_accvgpr_read_b32 v219, a89\n"
    "v_mfma_f32_16x16x32_bf16 a[160:163], v[188:191], v[84:87], a[160:163]\n"
    "v_accvgpr_read_b32 v220, a18\n"
    "v_accvgpr_read_b32 v221, a19\n"
    "v_mfma_f32_16x16x32_bf16 a[184:187], v[136:139], v[88:91], a[184:187]\n"
    "s_waitcnt lgkmcnt(6)\n"
    "buffer_store_dwordx4 v[128:131], v252, s[52:55], s88 offen offset:0 sc1\n"
    "v_mfma_f32_16x16x32_bf16 a[184:187], v[140:143], v[92:95], a[184:187]\n"
    "v_accvgpr_read_b32 v222, a42\n"
    "v_accvgpr_read_b32 v223, a43\n"
    "v_mfma_f32_16x16x32_bf16 a[184:187], v[184:187], v[96:99], a[184:187]\n"
    "v_accvgpr_read_b32 v224, a66\n"
    "v_accvgpr_read_b32 v225, a67\n"
    "v_mfma_f32_16x16x32_bf16 a[184:187], v[188:191], v[100:103], a[184:187]\n"
    "v_accvgpr_read_b32 v226, a90\n"
    "v_accvgpr_read_b32 v227, a91\n"
    "v_mfma_f32_16x16x4_f32 v[228:231], v232, v255, 0\n"
    "s_waitcnt lgkmcnt(0)\n"
    "buffer_store_dwordx4 v[132:135], v252, s[52:55], s88 offen offset:1024 sc1\n"
    "v_smfmac_f32_16x16x64_bf16 v[244:247], a[240:243], v[144:151], v253\n"
    "v_smfmac_f32_16x16x64_bf16 v[244:247], a[240:243], v[192:199], v253\n"
    "v_mfma_f32_16x16x32_bf16 a[20:23], v[144:147], a[224:227], a[20:23]\n"
    "v_mfma_f32_16x16x32_bf16 a[20:23], v[148:151], a[228:231], a[20:23]\n"
    "buffer_store_dwordx4 v[180:183], v252, s[52:55], s88 offen offset:2048 sc1\n"
    "v_mfma_f32_16x16x32_bf16 a[20:23], v[192:195], a[192:195], a[20:23]\n"
    "v_mfma_f32_16x16x32_bf16 a[20:23], v[196:199], a[196:199], a[20:23]\n"
    "v_mfma_f32_16x16x32_bf16 a[44:47], v[144:147], a[232:235], a[44:47]\n"
    "v_pk_mul_f32 v[212:213], v[212:213], v[228:229]\n"
    "v_pk_mul_f32 v[214:215], v[214:215], v[228:229]\n"
    "v_pk_mul_f32 v[216:217], v[216:217], v[228:229]\n"
    "v_pk_mul_f32 v[218:219], v[218:219], v[228:229]\n"
    "v_pk_mul_f32 v[220:221], v[220:221], v[230:231]\n"
    "v_pk_mul_f32 v[222:223], v[222:223], v[230:231]\n"
    "v_pk_mul_f32 v[224:225], v[224:225], v[230:231]\n"
    "v_pk_mul_f32 v[226:227], v[226:227], v[230:231]\n"
    "buffer_store_dwordx4 v[200:203], v252, s[52:55], s88 offen offset:3072 sc1\n"
    "v_mfma_f32_16x16x32_bf16 a[44:47], v[148:151], a[236:239], a[44:47]\n"
    "s_or_b64 s[70:71], s[70:71], vcc\n"
    "v_cvt_pk_bf16_f32 v212, v212, v214\n"
    "v_cvt_pk_bf16_f32 v214, v213, v215\n"
    "v_mfma_f32_16x16x32_bf16 a[44:47], v[192:195], a[200:203], a[44:47]\n"
    "s_add_u32 s88, s82, 16384\n"
    "v_cvt_pk_bf16_f32 v213, v216, v218\n"
    "v_cvt_pk_bf16_f32 v215, v217, v219\n"
    "v_mfma_f32_16x16x32_bf16 a[44:47], v[196:199], a[204:207], a[44:47]\n"
    "ds_write_b64 v7, v[212:213] offset:0\n"
    "ds_write_b64 v7, v[214:215] offset:256\n"
    "v_cvt_pk_bf16_f32 v220, v220, v222\n"
    "v_cvt_pk_bf16_f32 v222, v221, v223\n"
    "v_mfma_f32_16x16x32_bf16 a[68:71], v[144:147], v[8:11], a[68:71]\n"
    "v_cvt_pk_bf16_f32 v221, v224, v226\n"
    "v_cvt_pk_bf16_f32 v223, v225, v227\n"
    "v_mfma_f32_16x16x32_bf16 a[68:71], v[148:151], v[12:15], a[68:71]\n"
    "ds_write_b64 v7, v[220:221] offset:512\n"
    "ds_write_b64 v7, v[222:223] offset:768\n"
    "v_accvgpr_read_b32 v212, a112\n"
    "v_accvgpr_read_b32 v213, a113\n"
    "v_pk_mul_f32 v[212:213], v[212:213], v[228:229]\n"
    "v_accvgpr_read_b32 v214, a136\n"
    "v_mfma_f32_16x16x32_bf16 a[68:71], v[192:195], v[16:19], a[68:71]\n"
    "v_accvgpr_read_b32 v215, a137\n"
    "v_accvgpr_read_b32 v216, a160\n"
    "v_accvgpr_read_b32 v217, a161\n"
    "v_accvgpr_read_b32 v218, a184\n"
    "v_accvgpr_read_b32 v219, a185\n"
    "v_pk_mul_f32 v[214:215], v[214:215], v[228:229]\n"
    "v_pk_mul_f32 v[216:217], v[216:217], v[228:229]\n"
    "v_pk_mul_f32 v[218:219], v[218:219], v[228:229]\n"
    "v_cvt_pk_bf16_f32 v212, v212, v214\n"
    "v_mfma_f32_16x16x32_bf16 a[68:71], v[196:199], v[20:23], a[68:71]\n"
    "v_cvt_pk_bf16_f32 v214, v213, v215\n"
    "v_cvt_pk_bf16_f32 v213, v216, v218\n"
    "v_mfma_f32_16x16x32_bf16 a[92:95], v[144:147], v[24:27], a[92:95]\n"
    "ds_write_b64 v7, v[212:213] offset:128\n"
    "v_cvt_pk_bf16_f32 v215, v217, v219\n"
    "v_mfma_f32_16x16x32_bf16 a[92:95], v[148:151], v[28:31], a[92:95]\n"
    "ds_write_b64 v7, v[214:215] offset:384\n"
    "v_accvgpr_read_b32 v220, a114\n"
    "v_accvgpr_read_b32 v221, a115\n"
    "v_pk_mul_f32 v[220:221], v[220:221], v[230:231]\n"
    "v_accvgpr_read_b32 v222, a138\n"
    "v_accvgpr_read_b32 v223, a139\n"
    "v_pk_mul_f32 v[222:223], v[222:223], v[230:231]\n"
    "v_accvgpr_read_b32 v224, a162\n"
    "v_mfma_f32_16x16x32_bf16 a[92:95], v[192:195], v[32:35], a[92:95]\n"
    "v_accvgpr_read_b32 v225, a163\n"
    "v_accvgpr_read_b32 v226, a186\n"
    "v_accvgpr_read_b32 v227, a187\n"
    "v_cvt_pk_bf16_f32 v220, v220, v222\n"
    "v_cvt_pk_bf16_f32 v222, v221, v223\n"
    "v_pk_mul_f32 v[224:225], v[224:225], v[230:231]\n"
    "v_pk_mul_f32 v[226:227], v[226:227], v[230:231]\n"
    "v_cvt_pk_bf16_f32 v221, v224, v226\n"
    "v_mfma_f32_16x16x32_bf16 a[92:95], v[196:199], v[36:39], a[92:95]\n"
    "ds_write_b64 v7, v[220:221] offset:640\n"
    "v_cvt_pk_bf16_f32 v223, v225, v227\n"
    "v_mfma_f32_16x16x32_bf16 a[116:119], v[144:147], v[40:43], a[116:119]\n"
    "ds_write_b64 v7, v[222:223] offset:896\n"
    "v_mfma_f32_16x16x32_bf16 a[116:119], v[148:151], v[44:47], a[116:119]\n"
    "ds_read_b64_tr_b16 v[136:137], v254 offset:0\n"
    "ds_read_b64_tr_b16 v[138:139], v254 offset:32\n"
    "v_mfma_f32_16x16x32_bf16 a[116:119], v[192:195], v[48:51], a[116:119]\n"
    "ds_read_b64_tr_b16 v[140:141], v254 offset:1024\n"
    "ds_read_b64_tr_b16 v[142:143], v254 offset:1056\n"
    "v_mfma_f32_16x16x32_bf16 a[116:119], v[196:199], v[52:55], a[116:119]\n"
    "ds_read_b64_tr_b16 v[188:189], v254 offset:2048\n"
    "ds_read_b64_tr_b16 v[190:191], v254 offset:2080\n"
    "v_mfma_f32_16x16x32_bf16 a[140:143], v[144:147], v[56:59], a[140:143]\n"
    "ds_read_b64_tr_b16 v[204:205], v254 offset:3072\n"
    "ds_read_b64_tr_b16 v[206:207], v254 offset:3104\n"
    "v_mfma_f32_16x16x32_bf16 a[140:143], v[148:151], v[60:63], a[140:143]\n"
    "v_add_f32 v235, v244, v245\n"
    "v_rcp_f32 v234, v235\n"
    "v_mfma_f32_16x16x32_bf16 a[140:143], v[192:195], v[64:67], a[140:143]\n"
    "v_max_f32 v235, v234, v235\n"
    "v_cmp_nge_f32 vcc, s80, v235\n"
    "v_mfma_f32_16x16x32_bf16 a[140:143], v[196:199], v[68:71], a[140:143]\n"
    "v_accvgpr_read_b32 v212, a20\n"
    "v_accvgpr_read_b32 v213, a21\n"
    "v_mfma_f32_16x16x32_bf16 a[164:167], v[144:147], v[72:75], a[164:167]\n"
    "v_accvgpr_read_b32 v214, a44\n"
    "v_accvgpr_read_b32 v215, a45\n"
    "v_mfma_f32_16x16x32_bf16 a[164:167], v[148:151], v[76:79], a[164:167]\n"
    "s_waitcnt lgkmcnt(6)\n"
    "buffer_store_dwordx4 v[136:139], v252, s[52:55], s88 offen offset:0 sc1\n"
    "v_mfma_f32_16x16x32_bf16 a[164:167], v[192:195], v[80:83], a[164:167]\n"
    "v_accvgpr_read_b32 v216, a68\n"
    "v_accvgpr_read_b32 v217, a69\n"
    "v_mfma_f32_16x16x32_bf16 a[164:167], v[196:199], v[84:87], a[164:167]\n"
    "v_accvgpr_read_b32 v218, a92\n"
    "v_accvgpr_read_b32 v219, a93\n"
    "v_mfma_f32_16x16x32_bf16 a[188:191], v[144:147], v[88:91], a[188:191]\n"
    "v_accvgpr_read_b32 v220, a22\n"
    "v_accvgpr_read_b32 v221, a23\n"
    "v_mfma_f32_16x16x32_bf16 a[188:191], v[148:151], v[92:95], a[188:191]\n"
    "v_accvgpr_read_b32 v222, a46\n"
    "v_accvgpr_read_b32 v223, a47\n"
    "v_mfma_f32_16x16x32_bf16 a[188:191], v[192:195], v[96:99], a[188:191]\n"
    "s_waitcnt lgkmcnt(0)\n"
    "buffer_store_dwordx4 v[140:143], v252, s[52:55], s88 offen offset:1024 sc1\n"
    "v_mfma_f32_16x16x32_bf16 a[188:191], v[196:199], v[100:103], a[188:191]\n"
    "v_accvgpr_read_b32 v224, a70\n"
    "v_accvgpr_read_b32 v225, a71\n"
    "v_mfma_f32_16x16x4_f32 v[248:251], v234, v255, 0\n"
    "v_accvgpr_read_b32 v226, a94\n"
    "v_accvgpr_read_b32 v227, a95\n"
    "buffer_store_dwordx4 v[188:191], v252, s[52:55], s88 offen offset:2048 sc1\n"
    "buffer_store_dwordx4 v[204:207], v252, s[52:55], s88 offen offset:3072 sc1\n"
    "s_or_b64 s[70:71], s[70:71], vcc\n"
    "s_add_u32 s88, s82, 20480\n"
    "s_nop 0\n"
    "s_nop 0\n"
    "s_nop 0\n"
    "s_nop 0\n"
    "v_pk_mul_f32 v[212:213], v[212:213], v[248:249]\n"
    "v_pk_mul_f32 v[214:215], v[214:215], v[248:249]\n"
    "v_pk_mul_f32 v[216:217], v[216:217], v[248:249]\n"
    "v_pk_mul_f32 v[218:219], v[218:219], v[248:249]\n"
    "v_pk_mul_f32 v[220:221], v[220:221], v[250:251]\n"
    "v_pk_mul_f32 v[222:223], v[222:223], v[250:251]\n"
    "v_pk_mul_f32 v[224:225], v[224:225], v[250:251]\n"
    "v_pk_mul_f32 v[226:227], v[226:227], v[250:251]\n"
    "v_cvt_pk_bf16_f32 v212, v212, v214\n"
    "v_cvt_pk_bf16_f32 v214, v213, v215\n"
    "v_cvt_pk_bf16_f32 v213, v216, v218\n"
    "v_cvt_pk_bf16_f32 v215, v217, v219\n"
    "ds_write_b64 v7, v[212:213] offset:16384\n"
    "ds_write_b64 v7, v[214:215] offset:16640\n"
    "v_cvt_pk_bf16_f32 v220, v220, v222\n"
    "v_cvt_pk_bf16_f32 v222, v221, v223\n"
    "v_cvt_pk_bf16_f32 v221, v224, v226\n"
    "v_cvt_pk_bf16_f32 v223, v225, v227\n"
    "ds_write_b64 v7, v[220:221] offset:16896\n"
    "ds_write_b64 v7, v[222:223] offset:17152\n"
    "v_accvgpr_read_b32 v212, a116\n"
    "v_accvgpr_read_b32 v213, a117\n"
    "v_pk_mul_f32 v[212:213], v[212:213], v[248:249]\n"
    "v_accvgpr_read_b32 v214, a140\n"
    "v_accvgpr_read_b32 v215, a141\n"
    "v_pk_mul_f32 v[214:215], v[214:215], v[248:249]\n"
    "v_accvgpr_read_b32 v216, a164\n"
    "v_accvgpr_read_b32 v217, a165\n"
    "v_pk_mul_f32 v[216:217], v[216:217], v[248:249]\n"
    "v_accvgpr_read_b32 v218, a188\n"
    "v_accvgpr_read_b32 v219, a189\n"
    "v_pk_mul_f32 v[218:219], v[218:219], v[248:249]\n"
    "v_cvt_pk_bf16_f32 v212, v212, v214\n"
    "v_cvt_pk_bf16_f32 v214, v213, v215\n"
    "v_cvt_pk_bf16_f32 v213, v216, v218\n"
    "v_cvt_pk_bf16_f32 v215, v217, v219\n"
    "ds_write_b64 v7, v[212:213] offset:16512\n"
    "ds_write_b64 v7, v[214:215] offset:16768\n"
    "v_accvgpr_read_b32 v220, a118\n"
    "v_accvgpr_read_b32 v221, a119\n"
    "v_pk_mul_f32 v[220:221], v[220:221], v[250:251]\n"
    "v_accvgpr_read_b32 v222, a142\n"
    "v_accvgpr_read_b32 v223, a143\n"
    "v_pk_mul_f32 v[222:223], v[222:223], v[250:251]\n"
    "v_accvgpr_read_b32 v224, a166\n"
    "v_accvgpr_read_b32 v225, a167\n"
    "v_pk_mul_f32 v[224:225], v[224:225], v[250:251]\n"
    "v_accvgpr_read_b32 v226, a190\n"
    "v_accvgpr_read_b32 v227, a191\n"
    "v_pk_mul_f32 v[226:227], v[226:227], v[250:251]\n"
    "v_cvt_pk_bf16_f32 v220, v220, v222\n"
    "v_cvt_pk_bf16_f32 v222, v221, v223\n"
    "v_cvt_pk_bf16_f32 v221, v224, v226\n"
    "v_cvt_pk_bf16_f32 v223, v225, v227\n"
    "ds_write_b64 v7, v[220:221] offset:17024\n"
    "ds_write_b64 v7, v[222:223] offset:17280\n"
    "ds_read_b64_tr_b16 v[144:145], v254 offset:16384\n"
    "ds_read_b64_tr_b16 v[146:147], v254 offset:16416\n"
    "ds_read_b64_tr_b16 v[148:149], v254 offset:17408\n"
    "ds_read_b64_tr_b16 v[150:151], v254 offset:17440\n"
    "ds_read_b64_tr_b16 v[196:197], v254 offset:18432\n"
    "ds_read_b64_tr_b16 v[198:199], v254 offset:18464\n"
    "ds_read_b64_tr_b16 v[208:209], v254 offset:19456\n"
    "ds_read_b64_tr_b16 v[210:211], v254 offset:19488\n"
    "s_waitcnt lgkmcnt(6)\n"
    "buffer_store_dwordx4 v[144:147], v252, s[52:55], s88 offen offset:0 sc1\n"
    "s_waitcnt lgkmcnt(0)\n"
    "buffer_store_dwordx4 v[148:151], v252, s[52:55], s88 offen offset:1024 sc1\n"
    "buffer_store_dwordx4 v[196:199], v252, s[52:55], s88 offen offset:2048 sc1\n"
    "buffer_store_dwordx4 v[208:211], v252, s[52:55], s88 offen offset:3072 sc1\n"
    "s_cmp_lg_u64 s[70:71], 0\n"
    "s_cselect_b32 s75, 2, 0\n"
    "s_or_b32 s81, s81, s75\n"
    "s_waitcnt vmcnt(0)\n"
    "s_mov_b32 %0, s81\n"
    : "=s"(flag)
    : "s"(__builtin_amdgcn_kernarg_segment_ptr()), "s"(head0), "s"(blockIdx.x), "s"(wid), "s"(ldsb)
    : "v0","v1","v2","v3","v4","v5","v6","v7","v8","v9","v10","v11","v12","v13","v14","v15","v16","v17","v18","v19","v20","v21","v22","v23","v24","v25","v26","v27","v28","v29","v30","v31","v32","v33","v34","v35","v36","v37","v38","v39","v40","v41","v42","v43","v44","v45","v46","v47","v48","v49","v50","v51","v52","v53","v54","v55","v56","v57","v58","v59","v60","v61","v62","v63","v64","v65","v66","v67","v68","v69","v70","v71","v72","v73","v74","v75","v76","v77","v78","v79","v80","v81","v82","v83","v84","v85","v86","v87","v88","v89","v90","v91","v92","v93","v94","v95","v96","v97","v98","v99","v100","v101","v102","v103","v104","v105","v106","v107","v108","v109","v110","v111","v112","v113","v114","v115","v116","v117","v118","v119","v120","v121","v122","v123","v124","v125","v126","v127","v128","v129","v130","v131","v132","v133","v134","v135","v136","v137","v138","v139","v140","v141","v142","v143","v144","v145","v146","v147","v148","v149","v150","v151","v152","v153","v154","v155","v156","v157","v158","v159","v160","v161","v162","v163","v164","v165","v166","v167","v168","v169","v170","v171","v172","v173","v174","v175","v176","v177","v178","v179","v180","v181","v182","v183","v184","v185","v186","v187","v188","v189","v190","v191","v192","v193","v194","v195","v196","v197","v198","v199","v200","v201","v202","v203","v204","v205","v206","v207","v208","v209","v210","v211","v212","v213","v214","v215","v216","v217","v218","v219","v220","v221","v222","v223","v224","v225","v226","v227","v228","v229","v230","v231","v232","v233","v234","v235","v236","v237","v238","v239","v240","v241","v242","v243","v244","v245","v246","v247","v248","v249","v250","v251","v252","v253","v254","v255","a0","a1","a2","a3","a4","a5","a6","a7","a8","a9","a10","a11","a12","a13","a14","a15","a16","a17","a18","a19","a20","a21","a22","a23","a24","a25","a26","a27","a28","a29","a30","a31","a32","a33","a34","a35","a36","a37","a38","a39","a40","a41","a42","a43","a44","a45","a46","a47","a48","a49","a50","a51","a52","a53","a54","a55","a56","a57","a58","a59","a60","a61","a62","a63","a64","a65","a66","a67","a68","a69","a70","a71","a72","a73","a74","a75","a76","a77","a78","a79","a80","a81","a82","a83","a84","a85","a86","a87","a88","a89","a90","a91","a92","a93","a94","a95","a96","a97","a98","a99","a100","a101","a102","a103","a104","a105","a106","a107","a108","a109","a110","a111","a112","a113","a114","a115","a116","a117","a118","a119","a120","a121","a122","a123","a124","a125","a126","a127","a128","a129","a130","a131","a132","a133","a134","a135","a136","a137","a138","a139","a140","a141","a142","a143","a144","a145","a146","a147","a148","a149","a150","a151","a152","a153","a154","a155","a156","a157","a158","a159","a160","a161","a162","a163","a164","a165","a166","a167","a168","a169","a170","a171","a172","a173","a174","a175","a176","a177","a178","a179","a180","a181","a182","a183","a184","a185","a186","a187","a188","a189","a190","a191","a192","a193","a194","a195","a196","a197","a198","a199","a200","a201","a202","a203","a204","a205","a206","a207","a208","a209","a210","a211","a212","a213","a214","a215","a216","a217","a218","a219","a220","a221","a222","a223","a224","a225","a226","a227","a228","a229","a230","a231","a232","a233","a234","a235","a236","a237","a238","a239","a240","a241","a242","a243","a244","a245","a246","a247","a248","a249","a250","a251","a252","a253","a254","a255","s36","s37","s38","s39","s40","s41","s42","s43","s44","s45","s46","s47","s48","s49","s50","s51","s52","s53","s54","s55","s56","s57","s58","s59","s60","s61","s62","s63","s64","s65","s66","s67","s68","s69","s70","s71","s72","s73","s74","s75","s76","s77","s78","s79","s80","s81","s82","s83","s84","s85","s86","s87","s88","s89","s90","s91","s92","s93","s94","s95","s96","s97","s98","s99","vcc","scc","memory");
  if (flag) {
    const int lane = __builtin_amdgcn_mbcnt_hi(-1, __builtin_amdgcn_mbcnt_lo(-1, 0));
#pragma unroll 1
    for (int i = 0; i < 6; ++i) {
      const int hh = i / 3, qb = i % 3;
      if (!((flag >> hh) & 1)) continue;
      const long head = head0 + 256 * hh, row0 = head * SQ + wid * 96 + qb * 32;
      safe_block(Q + row0 * D, K + head * SK * D, V + head * SK * D, O + row0 * D, lane);
    }
  }
}

extern "C" void kernel_launch(void* const* d_in, const int* in_sizes, int n_in, void* d_out, int out_size, void* d_ws, size_t ws_size, hipStream_t stream) {
  attn_fwd<<<256, 256, 0, stream>>>((const unsigned short*)d_in[0], (const unsigned short*)d_in[1], (const unsigned short*)d_in[2], (unsigned short*)d_out);
}
```
